```python
import torch

def _softmax(x, axis=-1):
    return torch.softmax(x, dim=axis)

def _gelu(x):
    return torch.nn.functional.gelu(x, approximate='tanh')

def _silu(x):
    return torch.nn.functional.silu(x)

def _sigmoid(x):
    return torch.sigmoid(x)

def _layernorm(x, axis=-1, eps=1e-05):
    m = x.mean(dim=axis, keepdim=True)
    v = x.var(dim=axis, keepdim=True, unbiased=False)
    return (x - m) / torch.sqrt(v + eps)

def _scatter_add(tgt, idx, src, axis=0):
    return tgt.clone().index_add_(axis, idx, src)

def _segment_sum(src, seg, n):
    out = torch.zeros((n,) + src.shape[1:], dtype=src.dtype)
    return out.index_add_(0, seg, src)

def _pad(x, pw):
    flat = tuple((v for pair in reversed(pw) for v in pair))
    return torch.nn.functional.pad(x, flat)
kernel_name = 'interleave_layers__memory_aug_recurrent_mem_tr__dense_activation_gelu__prompt'

def setup_inputs():

    _w = lambda a: (lambda t: t.to(torch.bfloat16) if t.is_floating_point() else t)(torch.as_tensor(a))

    def _A_setup():
        B, S, Dm = (1, 2048, 576)
        H, Dh = (32, 128)
        M, seg = (8, 32)
        seg = min(seg, S)
        S = S // seg * seg
        return {'x': _w((lambda *s: torch.randn(*s, dtype=torch.bfloat16) * 0.05)(B, S, Dm)), 'mem0': _w((lambda *s: torch.randn(*s, dtype=torch.bfloat16) * 0.2)(B, M, Dm)), 'Wq': _w((lambda *s: torch.randn(*s, dtype=torch.bfloat16) * 1.6)(Dm, H, Dh) * Dm ** (-0.5)), 'Wk': _w((lambda *s: torch.randn(*s, dtype=torch.bfloat16) * 1.6)(Dm, H, Dh) * Dm ** (-0.5)), 'Wv': _w((lambda *s: torch.randn(*s, dtype=torch.bfloat16) * 0.2)(Dm, H, Dh) * Dm ** (-0.5)), 'Wo': _w((lambda *s: torch.randn(*s, dtype=torch.bfloat16) * 0.2)(H, Dh, Dm) * (H * Dh) ** (-0.5)), 'seg_len': seg}
    _A_in = _A_setup()

    def _B_setup():
        B, S, D, F = (1, 2048, 576, 4608)
        return {'x': _w((lambda *s: torch.randn(*s, dtype=torch.bfloat16) * 0.05)(B, S, D)), 'w1': _w((lambda *s: torch.randn(*s, dtype=torch.bfloat16) * 0.4)(D, F) * D ** (-0.5)), 'b1': _w((lambda *s: torch.randn(*s, dtype=torch.bfloat16) * 12.8)(F) * 0.01), 'w2': _w((lambda *s: torch.randn(*s, dtype=torch.bfloat16) * 0.1)(F, D) * F ** (-0.5)), 'b2': _w((lambda *s: torch.randn(*s, dtype=torch.bfloat16) * 0.8)(D) * 0.01)}
    _B_in = _B_setup()
    return {'A_x': _A_in['x'], 'A_mem0': _A_in['mem0'], 'A_Wq': _A_in['Wq'], 'A_Wk': _A_in['Wk'], 'A_Wv': _A_in['Wv'], 'A_Wo': _A_in['Wo'], 'A_seg_len': _A_in['seg_len'], 'B_x': _B_in['x'], 'B_w1': _B_in['w1'], 'B_b1': _B_in['b1'], 'B_w2': _B_in['w2'], 'B_b2': _B_in['b2']}

def reference(A_x, A_mem0, A_Wq, A_Wk, A_Wv, A_Wo, A_seg_len, B_x, B_w1, B_b1, B_w2, B_b2):

    def _A_ref(x, mem0, Wq, Wk, Wv, Wo, seg_len):
        B, S, Dm = x.shape
        H, Dh = (Wq.shape[1], Wq.shape[2])
        M = mem0.shape[1]
        scale = Dh ** (-0.5)
        n_seg = S // seg_len
        L = M + seg_len
        idx = torch.arange(L)
        mask = (idx[:, None] < M) | (idx[:, None] >= idx[None, :])
        mem = mem0
        outs = []
        for s in range(n_seg):
            xs = x[:, s * seg_len:(s + 1) * seg_len, :].clone()
            h = torch.concatenate([mem, xs], axis=1)
            q = torch.einsum('btd,dhe->bhte', h, Wq)
            k = torch.einsum('btd,dhe->bhte', h, Wk)
            v = torch.einsum('btd,dhe->bhte', h, Wv)
            scores = torch.einsum('bhqe,bhke->bhqk', q, k) * scale
            scores = torch.where(mask[None, None], scores, -1000000000.0)
            p = _softmax(scores, axis=-1)
            o = torch.einsum('bhqk,bhke->bhqe', p, v)
            o = torch.einsum('bhte,hed->btd', o, Wo)
            mem = o[:, :M, :].clone()
            outs.append(o[:, M:, :])
        return torch.concatenate(outs, axis=1)

    def _B_ref(x, w1, b1, w2, b2):
        h = torch.einsum('bsd,df->bsf', x, w1) + b1
        h = _gelu(h)
        return torch.einsum('bsf,fd->bsd', h, w2) + b2
    x = A_x
    x = x + _A_ref(_layernorm(x, axis=-1), A_mem0, A_Wq, A_Wk, A_Wv, A_Wo, A_seg_len)
    x = x + _B_ref(_layernorm(x, axis=-1), B_w1, B_b1, B_w2, B_b2)
    x = x + _A_ref(_layernorm(x, axis=-1), A_mem0, A_Wq, A_Wk, A_Wv, A_Wo, A_seg_len)
    x = x + _B_ref(_layernorm(x, axis=-1), B_w1, B_b1, B_w2, B_b2)
    return x
```

```cpp
#include <hip/hip_runtime.h>
#include <hip/hip_bf16.h>
typedef __hip_bfloat16 bf;
constexpr int S = 2048, D = 576, H = 32, E = 128, HE = 4096, M = 8, SEG = 32, F = 4608, NSEG = 64;
__device__ inline float b2f(bf x) { return __bfloat162float(x); }
__device__ inline bf f2b(float x) { return __float2bfloat16(x); }

__global__ void k_ln(const bf* x, bf* y) {
  int r = blockIdx.x, t = threadIdx.x;
  float v[9], s = 0;
  for (int i = 0; i < 9; ++i) { v[i] = b2f(x[r * D + t + 64 * i]); s += v[i]; }
  for (int o = 32; o; o >>= 1) s += __shfl_xor(s, o);
  float m = s / D, q = 0;
  for (int i = 0; i < 9; ++i) { v[i] -= m; q += v[i] * v[i]; }
  for (int o = 32; o; o >>= 1) q += __shfl_xor(q, o);
  float rs = rsqrtf(q / D + 1e-5f);
  for (int i = 0; i < 9; ++i) y[r * D + t + 64 * i] = f2b(v[i] * rs);
}
template <int EPI>
__global__ void k_gemm(const bf* A, const bf* B, bf* C, const bf* bias, const bf* res, int Mr, int N, int K) {
  __shared__ float As[32][33], Bs[32][65];
  int tx = threadIdx.x & 63, ty = threadIdx.x >> 6;
  int n = blockIdx.x * 64 + tx, m0 = blockIdx.y * 32;
  float acc[8] = {0};
  for (int k0 = 0; k0 < K; k0 += 32) {
    for (int i = threadIdx.x; i < 1024; i += 256) { int r = i >> 5, c = i & 31; As[r][c] = (m0 + r < Mr) ? b2f(A[(size_t)(m0 + r) * K + k0 + c]) : 0.f; }
    for (int i = threadIdx.x; i < 2048; i += 256) { int r = i >> 6, c = i & 63; Bs[r][c] = b2f(B[(size_t)(k0 + r) * N + blockIdx.x * 64 + c]); }
    __syncthreads();
    for (int k = 0; k < 32; ++k) { float b = Bs[k][tx];
#pragma unroll
      for (int j = 0; j < 8; ++j) acc[j] += As[ty * 8 + j][k] * b; }
    __syncthreads();
  }
#pragma unroll
  for (int j = 0; j < 8; ++j) { int m = m0 + ty * 8 + j; if (m >= Mr) continue; float v = acc[j];
    if (EPI == 1) { v += b2f(bias[n]); float u = 0.7978845608f * (v + 0.044715f * v * v * v); v = 0.5f * v * (1.f + tanhf(u)); }
    if (EPI == 2) { if (bias) v += b2f(bias[n]); v += b2f(res[(size_t)m * N + n]); }
    C[(size_t)m * N + n] = f2b(v); }
}
__global__ void __launch_bounds__(128) k_att(const bf* q, const bf* km, const bf* vm, const bf* kt, const bf* vt, bf* o, int nq, int causal) {
  int h = blockIdx.x, seg = blockIdx.y, t = threadIdx.x;
  q += (size_t)seg * nq * HE; o += (size_t)seg * nq * HE; km += (size_t)seg * M * HE; vm += (size_t)seg * M * HE; kt += (size_t)seg * SEG * HE; vt += (size_t)seg * SEG * HE;
  __shared__ float p[40];
  for (int i = 0; i < nq; ++i) {
    if (t < 40) { const bf* kr = t < M ? km + t * HE : kt + (t - M) * HE; float s = 0;
      _Pragma("unroll 4") for (int e = 0; e < E; ++e) s += b2f(q[i * HE + h * E + e]) * b2f(kr[h * E + e]);
      s *= 0.08838834764f; if (causal && t >= M && t - M > i) s = -1e9f; p[t] = s; }
    __syncthreads();
    float mx = -1e30f; for (int j = 0; j < 40; ++j) mx = fmaxf(mx, p[j]);
    float sum = 0, acc = 0;
    _Pragma("unroll 2") for (int j = 0; j < 40; ++j) { float e = __expf(p[j] - mx); sum += e; const bf* vr = j < M ? vm + j * HE : vt + (j - M) * HE; acc += e * b2f(vr[h * E + t]); }
    o[i * HE + h * E + t] = f2b(acc / sum);
    __syncthreads();
  }
}
extern "C" void kernel_launch(void* const* d_in, const int* in_sizes, int n_in, void* d_out, int out_size, void* d_ws, size_t ws_size, hipStream_t st) {
  const bf *x0 = (const bf*)d_in[0], *mem0 = (const bf*)d_in[1], *Wq = (const bf*)d_in[2], *Wk = (const bf*)d_in[3], *Wv = (const bf*)d_in[4], *Wo = (const bf*)d_in[5];
  const bf *w1 = (const bf*)d_in[8], *b1 = (const bf*)d_in[9], *w2 = (const bf*)d_in[10], *b2 = (const bf*)d_in[11];
  bf* ws = (bf*)d_ws;
  bf *xn = ws, *x1 = xn + S * D, *x2 = x1 + S * D, *Q = x2 + S * D, *K = Q + S * HE, *V = K + S * HE, *O = V + S * HE, *Hh = O + S * HE, *km = Hh + S * F, *vm = km + NSEG * M * HE, *qm = vm + NSEG * M * HE, *om = qm + M * HE, *mem = om + M * HE;
  const bf* xin = x0;
  for (int l = 0; l < 2; ++l) {
    k_ln<<<S, 64, 0, st>>>(xin, xn);
    k_gemm<0><<<dim3(HE / 64, S / 32), 256, 0, st>>>(xn, Wq, Q, nullptr, nullptr, S, HE, D);
    k_gemm<0><<<dim3(HE / 64, S / 32), 256, 0, st>>>(xn, Wk, K, nullptr, nullptr, S, HE, D);
    k_gemm<0><<<dim3(HE / 64, S / 32), 256, 0, st>>>(xn, Wv, V, nullptr, nullptr, S, HE, D);
    const bf* mc = mem0;
    for (int s = 0; s < NSEG; ++s) {
      k_gemm<0><<<dim3(HE / 64, 1), 256, 0, st>>>(mc, Wq, qm, nullptr, nullptr, M, HE, D);
      k_gemm<0><<<dim3(HE / 64, 1), 256, 0, st>>>(mc, Wk, km + s * M * HE, nullptr, nullptr, M, HE, D);
      k_gemm<0><<<dim3(HE / 64, 1), 256, 0, st>>>(mc, Wv, vm + s * M * HE, nullptr, nullptr, M, HE, D);
      k_att<<<dim3(H, 1), 128, 0, st>>>(qm, km + s * M * HE, vm + s * M * HE, K + (size_t)s * SEG * HE, V + (size_t)s * SEG * HE, om, M, 0);
      k_gemm<0><<<dim3(D / 64, 1), 256, 0, st>>>(om, Wo, mem, nullptr, nullptr, M, D, HE);
      mc = mem;
    }
    k_att<<<dim3(H, NSEG), 128, 0, st>>>(Q, km, vm, K, V, O, SEG, 1);
    k_gemm<2><<<dim3(D / 64, S / 32), 256, 0, st>>>(O, Wo, x1, nullptr, xin, S, D, HE);
    k_ln<<<S, 64, 0, st>>>(x1, xn);
    k_gemm<1><<<dim3(F / 64, S / 32), 256, 0, st>>>(xn, w1, Hh, b1, nullptr, S, F, D);
    bf* xo = l == 0 ? x2 : (bf*)d_out;
    k_gemm<2><<<dim3(D / 64, S / 32), 256, 0, st>>>(Hh, w2, xo, b2, x1, S, D, F);
    xin = x2;
  }
}
```

```cpp
#include <hip/hip_runtime.h>
#include <hip/hip_bf16.h>
#include <stdint.h>

struct Params {
  const void *x0, *mem0, *wq, *wk, *wv, *wo, *w1, *b1, *w2, *b2;
  void *out, *ws;
  uint32_t tune[4];
};

typedef uint32_t u32x4 __attribute__((ext_vector_type(4)));

__global__ void __launch_bounds__(256) rmt_setup(char* ws, const uint16_t* mem0) {
  char* base = (char*)(((uintptr_t)ws + (2u << 20) - 1) & ~(uintptr_t)((2u << 20) - 1));
  if (blockIdx.x != 0) {
    const __amdgpu_buffer_rsrc_t r = __builtin_amdgcn_make_buffer_rsrc(base, 0, 100663296u, 0x00020000);
    for (uint32_t c = blockIdx.x - 1; c < 109u + 72u + 36u * 16u; c += 253u) {
      const uint32_t j = c - 109u - 72u;
      const uint32_t o = c < 109u ? 131072u + c * 16384u : c < 109u + 72u ? 4194304u + 131072u + (c - 109u) * 16384u : 33554432u + (j / 36u) * 4194304u + 3538944u + (j % 36u) * 16384u;
      for (uint32_t i = 0; i < 4; ++i) {
        const uint32_t in = i * 4096u + threadIdx.x * 16u, h = in & 8191u;
        const uint32_t f = (c < 72u || (c >= 109u && c < 109u + 72u)) ? 0x80000000u : ~0u;
        u32x4 x = {f, f, f, f};
        if (o == 131072u || (o == 1179648u && h < 1024u)) {
          const uint32_t p = (h >> 7) + (o == 1179648u ? 64u : 0u);
          const uint16_t* m = mem0 + ((h >> 4) & 7u) * 576u + (p >> 1) * 16u + (p & 1u);
          for (int e = 0; e < 4; ++e) { const uint32_t y = m[4 * e] | ((uint32_t)m[4 * e + 2] << 16); x[e] = y == f ? 0u : y; }
        }
        __builtin_amdgcn_raw_buffer_store_b128(x, r, o + in, 0, 16);
        if (0u) __builtin_amdgcn_raw_buffer_store_b128(x, r, o + in + 0u, 0, 16);
      }
    }
    return;
  }
  if (threadIdx.x != 0) return;

  uint32_t m0 = ~0u, m1 = ~0u;
  for (int i = 0; i < 6; ++i) {
    uint32_t* q = (uint32_t*)(base + 16384 + (i & 1) * 8192);
    const uint64_t t0 = __builtin_amdgcn_s_memtime();
    asm volatile("s_waitcnt lgkmcnt(0)" ::: "memory");
    __hip_atomic_store(q, (uint32_t)i, __ATOMIC_RELAXED, __HIP_MEMORY_SCOPE_AGENT);
    asm volatile("s_waitcnt vmcnt(0)" ::: "memory");
    const uint32_t d = (uint32_t)(__builtin_amdgcn_s_memtime() - t0);
    if (i & 1) m1 = d < m1 ? d : m1; else m0 = d < m0 ? d : m0;
  }
  const uint32_t side = (__builtin_amdgcn_s_getreg(20 | (3 << 11)) >> 2) & 1u;
  *(uint32_t*)base = (m1 < m0 ? 1u : 0u) ^ side;
}

__global__ void __launch_bounds__(256, 1) rmt_pipeline(Params p) {
  const int wave = __builtin_amdgcn_readfirstlane(threadIdx.x >> 6);
  asm volatile(
"s_mov_b64 s[6:7], %0\n"
"s_mov_b32 s14, %1\n"
"s_mov_b32 s15, %2\n"
"s_load_dwordx4 s[20:23], s[6:7], 96\n"
"s_load_dwordx2 s[8:9], s[6:7], 88\n"
"s_mov_b32 s10, 100663296\n"
"s_mov_b32 s11, 131072\n"
"s_waitcnt lgkmcnt(0)\n"
"s_add_u32 s8, s8, 2097151\n"
"s_addc_u32 s9, s9, 0\n"
"s_and_b32 s8, s8, 0xffe00000\n"
"s_load_dword s26, s[8:9], 0\n"
"s_waitcnt lgkmcnt(0)\n"
"s_and_b32 s9, s9, 65535\n"
"s_and_b32 s13, s14, 7\n"
"s_lshr_b32 s14, s14, 3\n"
"s_mov_b32 s30, 0\n"
"s_cmp_eq_u32 s13, 1\n"
"s_cbranch_scc1 MK_map1\n"
"s_cmp_eq_u32 s13, 2\n"
"s_cbranch_scc1 MK_map2\n"
"s_cmp_eq_u32 s13, 3\n"
"s_cbranch_scc1 MK_map3\n"
"s_cmp_eq_u32 s13, 4\n"
"s_cbranch_scc1 MK_map4\n"
"s_cmp_eq_u32 s13, 5\n"
"s_cbranch_scc1 MK_map5\n"
"s_cmp_eq_u32 s13, 6\n"
"s_cbranch_scc1 MK_map6\n"
"s_cmp_eq_u32 s13, 7\n"
"s_cbranch_scc1 MK_map7\n"
"s_mov_b32 s25, 64\n"
"s_branch MK_mapped\n"
"MK_map1:\n"
"s_mov_b32 s25, 65600\n"
"s_cmp_lt_u32 s14, 11\n"
"s_cbranch_scc1 MK_mapped\n"
"s_mov_b32 s25, 131125\n"
"s_cmp_lt_u32 s14, 16\n"
"s_cbranch_scc1 MK_mapped\n"
"s_mov_b32 s25, 196656\n"
"s_cmp_lt_u32 s14, 19\n"
"s_cbranch_scc1 MK_mapped\n"
"s_mov_b32 s25, 196909\n"
"s_cmp_lt_u32 s14, 22\n"
"s_cbranch_scc1 MK_mapped\n"
"s_mov_b32 s25, 262186\n"
"s_cmp_lt_u32 s14, 27\n"
"s_cbranch_scc1 MK_mapped\n"
"s_mov_b32 s25, 327717\n"
"s_cmp_lt_u32 s14, 30\n"
"s_cbranch_scc1 MK_mapped\n"
"s_mov_b32 s25, 393250\n"
"s_cmp_lt_u32 s14, 31\n"
"s_cbranch_scc1 MK_mapped\n"
"s_mov_b32 s25, 458785\n"
"s_branch MK_mapped\n"
"MK_map2:\n"
"s_mov_b32 s25, 65611\n"
"s_cmp_lt_u32 s14, 10\n"
"s_cbranch_scc1 MK_mapped\n"
"s_mov_b32 s25, 131131\n"
"s_cmp_lt_u32 s14, 15\n"
"s_cbranch_scc1 MK_mapped\n"
"s_mov_b32 s25, 197169\n"
"s_cmp_lt_u32 s14, 18\n"
"s_cbranch_scc1 MK_mapped\n"
"s_mov_b32 s25, 262195\n"
"s_cmp_lt_u32 s14, 23\n"
"s_cbranch_scc1 MK_mapped\n"
"s_mov_b32 s25, 327977\n"
"s_cmp_lt_u32 s14, 26\n"
"s_cbranch_scc1 MK_mapped\n"
"s_mov_b32 s25, 328230\n"
"s_cmp_lt_u32 s14, 29\n"
"s_cbranch_scc1 MK_mapped\n"
"s_mov_b32 s25, 393507\n"
"s_cmp_lt_u32 s14, 30\n"
"s_cbranch_scc1 MK_mapped\n"
"s_mov_b32 s25, 459042\n"
"s_cmp_lt_u32 s14, 31\n"
"s_cbranch_scc1 MK_mapped\n"
"s_mov_b32 s25, 393761\n"
"s_branch MK_mapped\n"
"MK_map3:\n"
"s_mov_b32 s25, 65621\n"
"s_cmp_lt_u32 s14, 11\n"
"s_cbranch_scc1 MK_mapped\n"
"s_mov_b32 s25, 131135\n"
"s_cmp_lt_u32 s14, 17\n"
"s_cbranch_scc1 MK_mapped\n"
"s_mov_b32 s25, 197423\n"
"s_cmp_lt_u32 s14, 20\n"
"s_cbranch_scc1 MK_mapped\n"
"s_mov_b32 s25, 262198\n"
"s_cmp_lt_u32 s14, 26\n"
"s_cbranch_scc1 MK_mapped\n"
"s_mov_b32 s25, 328486\n"
"s_cmp_lt_u32 s14, 29\n"
"s_cbranch_scc1 MK_mapped\n"
"s_mov_b32 s25, 459299\n"
"s_cmp_lt_u32 s14, 30\n"
"s_cbranch_scc1 MK_mapped\n"
"s_mov_b32 s25, 394018\n"
"s_cmp_lt_u32 s14, 31\n"
"s_cbranch_scc1 MK_mapped\n"
"s_mov_b32 s25, 459553\n"
"s_branch MK_mapped\n"
"MK_map4:\n"
"s_mov_b32 s25, 4160\n"
"s_branch MK_mapped\n"
"MK_map5:\n"
"s_mov_b32 s25, 69696\n"
"s_cmp_lt_u32 s14, 11\n"
"s_cbranch_scc1 MK_mapped\n"
"s_mov_b32 s25, 135221\n"
"s_cmp_lt_u32 s14, 16\n"
"s_cbranch_scc1 MK_mapped\n"
"s_mov_b32 s25, 200752\n"
"s_cmp_lt_u32 s14, 19\n"
"s_cbranch_scc1 MK_mapped\n"
"s_mov_b32 s25, 201005\n"
"s_cmp_lt_u32 s14, 22\n"
"s_cbranch_scc1 MK_mapped\n"
"s_mov_b32 s25, 266282\n"
"s_cmp_lt_u32 s14, 27\n"
"s_cbranch_scc1 MK_mapped\n"
"s_mov_b32 s25, 331813\n"
"s_cmp_lt_u32 s14, 30\n"
"s_cbranch_scc1 MK_mapped\n"
"s_mov_b32 s25, 397346\n"
"s_cmp_lt_u32 s14, 31\n"
"s_cbranch_scc1 MK_mapped\n"
"s_mov_b32 s25, 462881\n"
"s_branch MK_mapped\n"
"MK_map6:\n"
"s_mov_b32 s25, 69707\n"
"s_cmp_lt_u32 s14, 10\n"
"s_cbranch_scc1 MK_mapped\n"
"s_mov_b32 s25, 135227\n"
"s_cmp_lt_u32 s14, 15\n"
"s_cbranch_scc1 MK_mapped\n"
"s_mov_b32 s25, 201265\n"
"s_cmp_lt_u32 s14, 18\n"
"s_cbranch_scc1 MK_mapped\n"
"s_mov_b32 s25, 266291\n"
"s_cmp_lt_u32 s14, 23\n"
"s_cbranch_scc1 MK_mapped\n"
"s_mov_b32 s25, 332073\n"
"s_cmp_lt_u32 s14, 26\n"
"s_cbranch_scc1 MK_mapped\n"
"s_mov_b32 s25, 332326\n"
"s_cmp_lt_u32 s14, 29\n"
"s_cbranch_scc1 MK_mapped\n"
"s_mov_b32 s25, 397603\n"
"s_cmp_lt_u32 s14, 30\n"
"s_cbranch_scc1 MK_mapped\n"
"s_mov_b32 s25, 463138\n"
"s_cmp_lt_u32 s14, 31\n"
"s_cbranch_scc1 MK_mapped\n"
"s_mov_b32 s25, 397857\n"
"s_branch MK_mapped\n"
"MK_map7:\n"
"s_mov_b32 s25, 69717\n"
"s_cmp_lt_u32 s14, 11\n"
"s_cbranch_scc1 MK_mapped\n"
"s_mov_b32 s25, 135231\n"
"s_cmp_lt_u32 s14, 17\n"
"s_cbranch_scc1 MK_mapped\n"
"s_mov_b32 s25, 201519\n"
"s_cmp_lt_u32 s14, 20\n"
"s_cbranch_scc1 MK_mapped\n"
"s_mov_b32 s25, 266294\n"
"s_cmp_lt_u32 s14, 26\n"
"s_cbranch_scc1 MK_mapped\n"
"s_mov_b32 s25, 332582\n"
"s_cmp_lt_u32 s14, 29\n"
"s_cbranch_scc1 MK_mapped\n"
"s_mov_b32 s25, 463395\n"
"s_cmp_lt_u32 s14, 30\n"
"s_cbranch_scc1 MK_mapped\n"
"s_mov_b32 s25, 398114\n"
"s_cmp_lt_u32 s14, 31\n"
"s_cbranch_scc1 MK_mapped\n"
"s_mov_b32 s25, 463649\n"
"s_branch MK_mapped\n"
"MK_mapped:\n"
"s_and_b32 s24, s25, 255\n"
"s_add_u32 s14, s14, s24\n"
"s_sub_u32 s14, s14, 64\n"
"s_bfe_u32 s13, s25, 262152\n"
"s_bfe_u32 s12, s25, 65548\n"
"s_lshr_b32 s25, s25, 16\n"
"s_xor_b32 s24, s20, s26\n"
"s_and_b32 s24, s24, 1\n"
"s_xor_b32 s24, s24, s12\n"
"s_lshl_b32 s16, s24, 13\n"
"s_xor_b32 s17, s16, 8192\n"
"s_cmp_eq_u32 s25, 0\n"
"s_cbranch_scc1 MK_CH\n"
"s_cmp_eq_u32 s25, 1\n"
"s_cbranch_scc1 MK_KV\n"
"s_cmp_eq_u32 s25, 2\n"
"s_cbranch_scc1 MK_Q\n"
"s_cmp_eq_u32 s25, 3\n"
"s_cbranch_scc1 MK_W\n"
"s_cmp_eq_u32 s25, 4\n"
"s_cbranch_scc1 MK_F1\n"
"s_cmp_eq_u32 s25, 5\n"
"s_cbranch_scc1 MK_F2\n"
"s_cmp_eq_u32 s25, 6\n"
"s_cbranch_scc1 MK_R1\n"
"s_cmp_eq_u32 s25, 7\n"
"s_cbranch_scc1 MK_R2\n"
"MK_exit:\n"
"s_endpgm\n"
"MK_CH:\n"
"s_memrealtime s[18:19]\n"
"s_mul_i32 s24, s12, 500\n"
"s_add_u32 s24, s24, 0\n"
"s_waitcnt lgkmcnt(0)\n"
"s_add_u32 s24, s24, s18\n"
"MK_CH_dly:\n"
"s_memrealtime s[18:19]\n"
"s_waitcnt lgkmcnt(0)\n"
"s_sub_u32 s25, s24, s18\n"
"s_cmp_gt_i32 s25, 0\n"
"s_cbranch_scc0 MK_CH_dlx\n"
"s_sleep 4\n"
"s_branch MK_CH_dly\n"
"MK_CH_dlx:\n"
"v_mbcnt_lo_u32_b32 v64, -1, 0\n"
"v_mbcnt_hi_u32_b32 v64, -1, v64\n"
"s_load_dwordx2 s[32:33], s[6:7], 16\n"
"s_mov_b32 s34, 4718592\n"
"s_mov_b32 s35, 131072\n"
"s_waitcnt lgkmcnt(0)\n"
"s_and_b32 s33, s33, 65535\n"
"s_load_dwordx2 s[36:37], s[6:7], 24\n"
"s_mov_b32 s38, 4718592\n"
"s_mov_b32 s39, 131072\n"
"s_waitcnt lgkmcnt(0)\n"
"s_and_b32 s37, s37, 65535\n"
"s_load_dwordx2 s[40:41], s[6:7], 32\n"
"s_mov_b32 s42, 4718592\n"
"s_mov_b32 s43, 131072\n"
"s_waitcnt lgkmcnt(0)\n"
"s_and_b32 s41, s41, 65535\n"
"s_load_dwordx2 s[44:45], s[6:7], 40\n"
"s_mov_b32 s46, 4718592\n"
"s_mov_b32 s47, 131072\n"
"s_waitcnt lgkmcnt(0)\n"
"s_and_b32 s45, s45, 65535\n"
"v_bfe_u32 v65, v64, 5, 1\n"
"v_mul_u32_u24 v66, 131072, v65\n"
"v_bfe_u32 v65, v64, 4, 1\n"
"v_lshl_add_u32 v66, v65, 15, v66\n"
"v_bfe_u32 v65, v64, 0, 4\n"
"v_lshl_add_u32 v66, v65, 4, v66\n"
"v_bfe_u32 v65, v64, 5, 1\n"
"v_mul_u32_u24 v67, 131072, v65\n"
"v_bfe_u32 v65, v64, 4, 1\n"
"v_lshl_add_u32 v67, v65, 15, v67\n"
"v_bfe_u32 v65, v64, 0, 4\n"
"v_lshl_add_u32 v67, v65, 4, v67\n"
"v_add_u32 v67, 262144, v67\n"
"s_lshr_b32 s24, s15, 1\n"
"s_lshl_b32 s24, s24, 3\n"
"s_and_b32 s25, s15, 1\n"
"s_add_u32 s24, s24, s25\n"
"s_lshl_b32 s24, s24, 13\n"
"s_lshl_b32 s25, s14, 8\n"
"s_add_u32 s62, s24, s25\n"
"s_add_u32 s63, s62, 16384\n"
"s_lshl_b32 s26, s15, 6\n"
"v_bfe_u32 v65, v64, 4, 1\n"
"v_mul_u32_u24 v32, 8320, v65\n"
"v_bfe_u32 v65, v64, 2, 2\n"
"v_mul_u32_u24 v65, 2080, v65\n"
"v_add_u32 v32, v32, v65\n"
"v_bfe_u32 v65, v64, 5, 1\n"
"v_lshl_add_u32 v32, v65, 9, v32\n"
"v_bfe_u32 v65, v64, 0, 2\n"
"v_lshl_add_u32 v32, v65, 4, v32\n"
"v_add_u32 v32, s26, v32\n"
"v_bfe_u32 v65, v64, 4, 1\n"
"v_mul_u32_u24 v35, 8320, v65\n"
"v_bfe_u32 v65, v64, 2, 2\n"
"v_mul_u32_u24 v65, 2080, v65\n"
"v_add_u32 v35, v35, v65\n"
"v_bfe_u32 v65, v64, 5, 1\n"
"v_lshl_add_u32 v35, v65, 9, v35\n"
"v_bfe_u32 v65, v64, 0, 2\n"
"v_lshl_add_u32 v35, v65, 3, v35\n"
"v_add_u32 v35, s26, v35\n"
"v_bfe_u32 v65, v64, 5, 1\n"
"v_mul_u32_u24 v68, 9216, v65\n"
"v_bfe_u32 v65, v64, 4, 1\n"
"v_mul_u32_u24 v65, 4608, v65\n"
"v_add_u32 v68, v68, v65\n"
"v_bfe_u32 v65, v64, 0, 4\n"
"v_lshl_add_u32 v68, v65, 4, v68\n"
"v_bfe_u32 v65, v64, 5, 1\n"
"v_mul_u32_u24 v69, 9216, v65\n"
"v_bfe_u32 v65, v64, 4, 1\n"
"v_mul_u32_u24 v65, 4608, v65\n"
"v_add_u32 v69, v69, v65\n"
"v_bfe_u32 v65, v64, 0, 4\n"
"v_lshl_add_u32 v69, v65, 4, v69\n"
"v_add_u32 v69, 36864, v69\n"
"s_lshr_b32 s24, s15, 1\n"
"s_lshl_b32 s24, s24, 4\n"
"s_and_b32 s25, s15, 1\n"
"s_add_u32 s24, s24, s25\n"
"s_lshl_b32 s25, s14, 7\n"
"s_add_u32 s24, s24, s25\n"
"s_mul_i32 s64, s24, 1152\n"
"s_add_u32 s65, s64, 2304\n"
"v_bfe_u32 v65, v64, 4, 1\n"
"v_mul_u32_u24 v38, 8320, v65\n"
"v_bfe_u32 v65, v64, 2, 1\n"
"v_mul_u32_u24 v65, 4160, v65\n"
"v_add_u32 v38, v38, v65\n"
"v_bfe_u32 v65, v64, 5, 1\n"
"v_lshl_add_u32 v38, v65, 8, v38\n"
"v_bfe_u32 v65, v64, 3, 1\n"
"v_lshl_add_u32 v38, v65, 5, v38\n"
"v_bfe_u32 v65, v64, 0, 2\n"
"v_lshl_add_u32 v38, v65, 3, v38\n"
"v_add_u32 v38, s26, v38\n"
"v_bfe_u32 v65, v64, 5, 1\n"
"v_mul_u32_u24 v70, 36864, v65\n"
"v_bfe_u32 v65, v64, 4, 1\n"
"v_mul_u32_u24 v65, 9216, v65\n"
"v_add_u32 v70, v70, v65\n"
"v_bfe_u32 v65, v64, 3, 1\n"
"v_mul_u32_u24 v65, 4608, v65\n"
"v_add_u32 v70, v70, v65\n"
"v_bfe_u32 v65, v64, 0, 3\n"
"v_lshl_add_u32 v70, v65, 4, v70\n"
"v_bfe_u32 v65, v64, 5, 1\n"
"v_mul_u32_u24 v71, 36864, v65\n"
"v_bfe_u32 v65, v64, 4, 1\n"
"v_mul_u32_u24 v65, 9216, v65\n"
"v_add_u32 v71, v71, v65\n"
"v_bfe_u32 v65, v64, 3, 1\n"
"v_mul_u32_u24 v65, 4608, v65\n"
"v_add_u32 v71, v71, v65\n"
"v_bfe_u32 v65, v64, 0, 3\n"
"v_lshl_add_u32 v71, v65, 4, v71\n"
"v_add_u32 v71, 73728, v71\n"
"s_lshr_b32 s24, s15, 1\n"
"s_lshl_b32 s24, s24, 6\n"
"s_and_b32 s25, s15, 1\n"
"s_lshl_b32 s25, s25, 10\n"
"s_add_u32 s26, s24, s25\n"
"v_bfe_u32 v65, v64, 4, 1\n"
"v_mul_u32_u24 v41, 8320, v65\n"
"v_bfe_u32 v65, v64, 2, 1\n"
"v_mul_u32_u24 v65, 4160, v65\n"
"v_add_u32 v41, v41, v65\n"
"v_bfe_u32 v65, v64, 5, 1\n"
"v_lshl_add_u32 v41, v65, 7, v41\n"
"v_bfe_u32 v65, v64, 3, 1\n"
"v_lshl_add_u32 v41, v65, 5, v41\n"
"v_bfe_u32 v65, v64, 0, 2\n"
"v_lshl_add_u32 v41, v65, 3, v41\n"
"v_add_u32 v41, s26, v41\n"
"v_add_u32 v33, 49152, v32\n"
"v_add_u32 v34, 49152, v33\n"
"v_add_u32 v36, 49152, v35\n"
"v_add_u32 v37, 49152, v36\n"
"v_add_u32 v39, 49152, v38\n"
"v_add_u32 v40, 49152, v39\n"
"v_add_u32 v42, 49152, v41\n"
"v_add_u32 v43, 49152, v42\n"
"s_mul_i32 s66, s15, 4160\n"
"s_lshl_b32 s69, s15, 12\n"
"s_add_u32 s69, s69, 16384\n"
"v_lshlrev_b32 v53, 4, v64\n"
"v_add_u32 v53, s69, v53\n"
"v_add_u32 v44, 65536, v53\n"
"v_add_u32 v45, 65536, v44\n"
"s_add_u32 s24, s62, 0\n"
"s_add_u32 m0, s66, 133120\n"
"s_nop 0\n"
"buffer_load_dwordx4 v66, s[36:39], s24 offen lds\n"
"s_add_u32 m0, s66, 134144\n"
"s_nop 0\n"
"buffer_load_dwordx4 v67, s[36:39], s24 offen lds\n"
"s_add_u32 s24, s63, 0\n"
"s_add_u32 m0, s66, 135200\n"
"s_nop 0\n"
"buffer_load_dwordx4 v66, s[36:39], s24 offen lds\n"
"s_add_u32 m0, s66, 136224\n"
"s_nop 0\n"
"buffer_load_dwordx4 v67, s[36:39], s24 offen lds\n"
"s_add_u32 s24, s62, 0\n"
"s_add_u32 m0, s66, 116480\n"
"s_nop 0\n"
"buffer_load_dwordx4 v66, s[40:43], s24 offen lds\n"
"s_add_u32 m0, s66, 117504\n"
"s_nop 0\n"
"buffer_load_dwordx4 v67, s[40:43], s24 offen lds\n"
"s_add_u32 s24, s63, 0\n"
"s_add_u32 m0, s66, 118560\n"
"s_nop 0\n"
"buffer_load_dwordx4 v66, s[40:43], s24 offen lds\n"
"s_add_u32 m0, s66, 119584\n"
"s_nop 0\n"
"buffer_load_dwordx4 v67, s[40:43], s24 offen lds\n"
"s_add_u32 s24, s62, 524288\n"
"s_add_u32 m0, s66, 99840\n"
"s_nop 0\n"
"buffer_load_dwordx4 v66, s[36:39], s24 offen lds\n"
"s_add_u32 m0, s66, 100864\n"
"s_nop 0\n"
"buffer_load_dwordx4 v67, s[36:39], s24 offen lds\n"
"s_add_u32 s24, s63, 524288\n"
"s_add_u32 m0, s66, 101920\n"
"s_nop 0\n"
"buffer_load_dwordx4 v66, s[36:39], s24 offen lds\n"
"s_add_u32 m0, s66, 102944\n"
"s_nop 0\n"
"buffer_load_dwordx4 v67, s[36:39], s24 offen lds\n"
"s_add_u32 s24, s62, 524288\n"
"s_add_u32 m0, s66, 83200\n"
"s_nop 0\n"
"buffer_load_dwordx4 v66, s[40:43], s24 offen lds\n"
"s_add_u32 m0, s66, 84224\n"
"s_nop 0\n"
"buffer_load_dwordx4 v67, s[40:43], s24 offen lds\n"
"s_add_u32 s24, s63, 524288\n"
"s_add_u32 m0, s66, 85280\n"
"s_nop 0\n"
"buffer_load_dwordx4 v66, s[40:43], s24 offen lds\n"
"s_add_u32 m0, s66, 86304\n"
"s_nop 0\n"
"buffer_load_dwordx4 v67, s[40:43], s24 offen lds\n"
"s_add_u32 s24, s62, 1048576\n"
"s_add_u32 m0, s66, 66560\n"
"s_nop 0\n"
"buffer_load_dwordx4 v66, s[40:43], s24 offen lds\n"
"s_add_u32 m0, s66, 67584\n"
"s_nop 0\n"
"buffer_load_dwordx4 v67, s[40:43], s24 offen lds\n"
"s_add_u32 s24, s63, 1048576\n"
"s_add_u32 m0, s66, 68640\n"
"s_nop 0\n"
"buffer_load_dwordx4 v66, s[40:43], s24 offen lds\n"
"s_add_u32 m0, s66, 69664\n"
"s_nop 0\n"
"buffer_load_dwordx4 v67, s[40:43], s24 offen lds\n"
"s_add_u32 s24, s62, 1572864\n"
"s_add_u32 m0, s66, 49920\n"
"s_nop 0\n"
"buffer_load_dwordx4 v66, s[36:39], s24 offen lds\n"
"s_add_u32 m0, s66, 50944\n"
"s_nop 0\n"
"buffer_load_dwordx4 v67, s[36:39], s24 offen lds\n"
"s_add_u32 s24, s63, 1572864\n"
"s_add_u32 m0, s66, 52000\n"
"s_nop 0\n"
"buffer_load_dwordx4 v66, s[36:39], s24 offen lds\n"
"s_add_u32 m0, s66, 53024\n"
"s_nop 0\n"
"buffer_load_dwordx4 v67, s[36:39], s24 offen lds\n"
"s_add_u32 s24, s62, 1572864\n"
"s_add_u32 m0, s66, 33280\n"
"s_nop 0\n"
"buffer_load_dwordx4 v66, s[40:43], s24 offen lds\n"
"s_add_u32 m0, s66, 34304\n"
"s_nop 0\n"
"buffer_load_dwordx4 v67, s[40:43], s24 offen lds\n"
"s_add_u32 s24, s63, 1572864\n"
"s_add_u32 m0, s66, 35360\n"
"s_nop 0\n"
"buffer_load_dwordx4 v66, s[40:43], s24 offen lds\n"
"s_add_u32 m0, s66, 36384\n"
"s_nop 0\n"
"buffer_load_dwordx4 v67, s[40:43], s24 offen lds\n"
"s_add_u32 s24, s62, 2097152\n"
"s_add_u32 m0, s66, 16640\n"
"s_nop 0\n"
"buffer_load_dwordx4 v66, s[36:39], s24 offen lds\n"
"s_add_u32 m0, s66, 17664\n"
"s_nop 0\n"
"buffer_load_dwordx4 v67, s[36:39], s24 offen lds\n"
"s_add_u32 s24, s63, 2097152\n"
"s_add_u32 m0, s66, 18720\n"
"s_nop 0\n"
"buffer_load_dwordx4 v66, s[36:39], s24 offen lds\n"
"s_add_u32 m0, s66, 19744\n"
"s_nop 0\n"
"buffer_load_dwordx4 v67, s[36:39], s24 offen lds\n"
"s_waitcnt vmcnt(28) lgkmcnt(0)\n"
"s_barrier\n"
"s_add_u32 s24, s62, 2097152\n"
"s_add_u32 m0, s66, 0\n"
"s_nop 0\n"
"buffer_load_dwordx4 v66, s[40:43], s24 offen lds\n"
"s_add_u32 m0, s66, 1024\n"
"s_nop 0\n"
"buffer_load_dwordx4 v67, s[40:43], s24 offen lds\n"
"s_add_u32 s24, s63, 2097152\n"
"s_add_u32 m0, s66, 2080\n"
"s_nop 0\n"
"buffer_load_dwordx4 v66, s[40:43], s24 offen lds\n"
"s_add_u32 m0, s66, 3104\n"
"s_nop 0\n"
"buffer_load_dwordx4 v67, s[40:43], s24 offen lds\n"
"ds_read_b64_tr_b16 a[144:145], v34 offset:34816\n"
"ds_read_b64_tr_b16 a[146:147], v34 offset:35072\n"
"ds_read_b64_tr_b16 a[148:149], v34 offset:35840\n"
"ds_read_b64_tr_b16 a[150:151], v34 offset:36096\n"
"ds_read_b64_tr_b16 a[152:153], v34 offset:34824\n"
"ds_read_b64_tr_b16 a[154:155], v34 offset:35080\n"
"ds_read_b64_tr_b16 a[156:157], v34 offset:35848\n"
"ds_read_b64_tr_b16 a[158:159], v34 offset:36104\n"
"s_waitcnt vmcnt(28) lgkmcnt(0)\n"
"s_barrier\n"
"s_add_u32 s24, s62, 2621440\n"
"s_add_u32 m0, s66, 133120\n"
"s_nop 0\n"
"buffer_load_dwordx4 v66, s[40:43], s24 offen lds\n"
"s_add_u32 m0, s66, 134144\n"
"s_nop 0\n"
"buffer_load_dwordx4 v67, s[40:43], s24 offen lds\n"
"s_add_u32 s24, s63, 2621440\n"
"s_add_u32 m0, s66, 135200\n"
"s_nop 0\n"
"buffer_load_dwordx4 v66, s[40:43], s24 offen lds\n"
"s_add_u32 m0, s66, 136224\n"
"s_nop 0\n"
"buffer_load_dwordx4 v67, s[40:43], s24 offen lds\n"
"ds_read_b64_tr_b16 v[80:81], v37 offset:18176\n"
"ds_read_b64_tr_b16 v[82:83], v37 offset:18432\n"
"ds_read_b64_tr_b16 v[84:85], v37 offset:19200\n"
"ds_read_b64_tr_b16 v[86:87], v37 offset:19456\n"
"ds_read_b64_tr_b16 v[88:89], v37 offset:18208\n"
"ds_read_b64_tr_b16 v[90:91], v37 offset:18464\n"
"ds_read_b64_tr_b16 v[92:93], v37 offset:19232\n"
"ds_read_b64_tr_b16 v[94:95], v37 offset:19488\n"
"s_waitcnt vmcnt(28) lgkmcnt(0)\n"
"s_barrier\n"
"s_add_u32 s24, s62, 3145728\n"
"s_add_u32 m0, s66, 116480\n"
"s_nop 0\n"
"buffer_load_dwordx4 v66, s[36:39], s24 offen lds\n"
"s_add_u32 m0, s66, 117504\n"
"s_nop 0\n"
"buffer_load_dwordx4 v67, s[36:39], s24 offen lds\n"
"s_add_u32 s24, s63, 3145728\n"
"s_add_u32 m0, s66, 118560\n"
"s_nop 0\n"
"buffer_load_dwordx4 v66, s[36:39], s24 offen lds\n"
"s_add_u32 m0, s66, 119584\n"
"s_nop 0\n"
"buffer_load_dwordx4 v67, s[36:39], s24 offen lds\n"
"ds_read_b64_tr_b16 a[160:161], v34 offset:1536\n"
"ds_read_b64_tr_b16 a[162:163], v34 offset:1792\n"
"ds_read_b64_tr_b16 a[164:165], v34 offset:2560\n"
"ds_read_b64_tr_b16 a[166:167], v34 offset:2816\n"
"ds_read_b64_tr_b16 a[168:169], v34 offset:1544\n"
"ds_read_b64_tr_b16 a[170:171], v34 offset:1800\n"
"ds_read_b64_tr_b16 a[172:173], v34 offset:2568\n"
"ds_read_b64_tr_b16 a[174:175], v34 offset:2824\n"
"s_waitcnt vmcnt(28) lgkmcnt(0)\n"
"s_barrier\n"
"s_add_u32 s24, s62, 3145728\n"
"s_add_u32 m0, s66, 99840\n"
"s_nop 0\n"
"buffer_load_dwordx4 v66, s[40:43], s24 offen lds\n"
"s_add_u32 m0, s66, 100864\n"
"s_nop 0\n"
"buffer_load_dwordx4 v67, s[40:43], s24 offen lds\n"
"s_add_u32 s24, s63, 3145728\n"
"s_add_u32 m0, s66, 101920\n"
"s_nop 0\n"
"buffer_load_dwordx4 v66, s[40:43], s24 offen lds\n"
"s_add_u32 m0, s66, 102944\n"
"s_nop 0\n"
"buffer_load_dwordx4 v67, s[40:43], s24 offen lds\n"
"ds_read_b64_tr_b16 v[96:97], v36 offset:34048\n"
"ds_read_b64_tr_b16 v[98:99], v36 offset:34304\n"
"ds_read_b64_tr_b16 v[100:101], v36 offset:35072\n"
"ds_read_b64_tr_b16 v[102:103], v36 offset:35328\n"
"ds_read_b64_tr_b16 v[104:105], v36 offset:34080\n"
"ds_read_b64_tr_b16 v[106:107], v36 offset:34336\n"
"ds_read_b64_tr_b16 v[108:109], v36 offset:35104\n"
"ds_read_b64_tr_b16 v[110:111], v36 offset:35360\n"
"s_waitcnt vmcnt(28) lgkmcnt(0)\n"
"s_barrier\n"
"s_add_u32 s24, s62, 3670016\n"
"s_add_u32 m0, s66, 83200\n"
"s_nop 0\n"
"buffer_load_dwordx4 v66, s[36:39], s24 offen lds\n"
"s_add_u32 m0, s66, 84224\n"
"s_nop 0\n"
"buffer_load_dwordx4 v67, s[36:39], s24 offen lds\n"
"s_add_u32 s24, s63, 3670016\n"
"s_add_u32 m0, s66, 85280\n"
"s_nop 0\n"
"buffer_load_dwordx4 v66, s[36:39], s24 offen lds\n"
"s_add_u32 m0, s66, 86304\n"
"s_nop 0\n"
"buffer_load_dwordx4 v67, s[36:39], s24 offen lds\n"
"ds_read_b64_tr_b16 v[112:113], v36 offset:17408\n"
"ds_read_b64_tr_b16 v[114:115], v36 offset:17664\n"
"ds_read_b64_tr_b16 v[116:117], v36 offset:18432\n"
"ds_read_b64_tr_b16 v[118:119], v36 offset:18688\n"
"ds_read_b64_tr_b16 v[120:121], v36 offset:17440\n"
"ds_read_b64_tr_b16 v[122:123], v36 offset:17696\n"
"ds_read_b64_tr_b16 v[124:125], v36 offset:18464\n"
"ds_read_b64_tr_b16 v[126:127], v36 offset:18720\n"
"s_waitcnt vmcnt(28) lgkmcnt(0)\n"
"s_barrier\n"
"s_add_u32 s24, s62, 4194304\n"
"s_add_u32 m0, s66, 66560\n"
"s_nop 0\n"
"buffer_load_dwordx4 v66, s[36:39], s24 offen lds\n"
"s_add_u32 m0, s66, 67584\n"
"s_nop 0\n"
"buffer_load_dwordx4 v67, s[36:39], s24 offen lds\n"
"s_add_u32 s24, s63, 4194304\n"
"s_add_u32 m0, s66, 68640\n"
"s_nop 0\n"
"buffer_load_dwordx4 v66, s[36:39], s24 offen lds\n"
"s_add_u32 m0, s66, 69664\n"
"s_nop 0\n"
"buffer_load_dwordx4 v67, s[36:39], s24 offen lds\n"
"ds_read_b64_tr_b16 a[176:177], v33 offset:768\n"
"ds_read_b64_tr_b16 a[178:179], v33 offset:1024\n"
"ds_read_b64_tr_b16 a[180:181], v33 offset:1792\n"
"ds_read_b64_tr_b16 a[182:183], v33 offset:2048\n"
"ds_read_b64_tr_b16 a[184:185], v33 offset:776\n"
"ds_read_b64_tr_b16 a[186:187], v33 offset:1032\n"
"ds_read_b64_tr_b16 a[188:189], v33 offset:1800\n"
"ds_read_b64_tr_b16 a[190:191], v33 offset:2056\n"
"s_waitcnt vmcnt(28) lgkmcnt(0)\n"
"s_barrier\n"
"s_add_u32 s24, s62, 0\n"
"s_add_u32 m0, s66, 49920\n"
"s_nop 0\n"
"buffer_load_dwordx4 v66, s[32:35], s24 offen lds\n"
"s_add_u32 m0, s66, 50944\n"
"s_nop 0\n"
"buffer_load_dwordx4 v67, s[32:35], s24 offen lds\n"
"s_add_u32 s24, s63, 0\n"
"s_add_u32 m0, s66, 52000\n"
"s_nop 0\n"
"buffer_load_dwordx4 v66, s[32:35], s24 offen lds\n"
"s_add_u32 m0, s66, 53024\n"
"s_nop 0\n"
"buffer_load_dwordx4 v67, s[32:35], s24 offen lds\n"
"ds_read_b64_tr_b16 v[128:129], v35 offset:33280\n"
"ds_read_b64_tr_b16 v[130:131], v35 offset:33536\n"
"ds_read_b64_tr_b16 v[132:133], v35 offset:34304\n"
"ds_read_b64_tr_b16 v[134:135], v35 offset:34560\n"
"ds_read_b64_tr_b16 v[136:137], v35 offset:33312\n"
"ds_read_b64_tr_b16 v[138:139], v35 offset:33568\n"
"ds_read_b64_tr_b16 v[140:141], v35 offset:34336\n"
"ds_read_b64_tr_b16 v[142:143], v35 offset:34592\n"
"s_waitcnt vmcnt(28) lgkmcnt(0)\n"
"s_barrier\n"
"s_add_u32 s24, s62, 524288\n"
"s_add_u32 m0, s66, 33280\n"
"s_nop 0\n"
"buffer_load_dwordx4 v66, s[32:35], s24 offen lds\n"
"s_add_u32 m0, s66, 34304\n"
"s_nop 0\n"
"buffer_load_dwordx4 v67, s[32:35], s24 offen lds\n"
"s_add_u32 s24, s63, 524288\n"
"s_add_u32 m0, s66, 35360\n"
"s_nop 0\n"
"buffer_load_dwordx4 v66, s[32:35], s24 offen lds\n"
"s_add_u32 m0, s66, 36384\n"
"s_nop 0\n"
"buffer_load_dwordx4 v67, s[32:35], s24 offen lds\n"
"ds_read_b64_tr_b16 a[192:193], v32 offset:16640\n"
"ds_read_b64_tr_b16 a[194:195], v32 offset:16896\n"
"ds_read_b64_tr_b16 a[196:197], v32 offset:17664\n"
"ds_read_b64_tr_b16 a[198:199], v32 offset:17920\n"
"ds_read_b64_tr_b16 a[200:201], v32 offset:16648\n"
"ds_read_b64_tr_b16 a[202:203], v32 offset:16904\n"
"ds_read_b64_tr_b16 a[204:205], v32 offset:17672\n"
"ds_read_b64_tr_b16 a[206:207], v32 offset:17928\n"
"s_waitcnt vmcnt(28) lgkmcnt(0)\n"
"s_barrier\n"
"s_add_u32 s24, s62, 1048576\n"
"s_add_u32 m0, s66, 16640\n"
"s_nop 0\n"
"buffer_load_dwordx4 v66, s[32:35], s24 offen lds\n"
"s_add_u32 m0, s66, 17664\n"
"s_nop 0\n"
"buffer_load_dwordx4 v67, s[32:35], s24 offen lds\n"
"s_add_u32 s24, s63, 1048576\n"
"s_add_u32 m0, s66, 18720\n"
"s_nop 0\n"
"buffer_load_dwordx4 v66, s[32:35], s24 offen lds\n"
"s_add_u32 m0, s66, 19744\n"
"s_nop 0\n"
"buffer_load_dwordx4 v67, s[32:35], s24 offen lds\n"
"ds_read_b64_tr_b16 v[144:145], v35 offset:0\n"
"ds_read_b64_tr_b16 v[146:147], v35 offset:256\n"
"ds_read_b64_tr_b16 v[148:149], v35 offset:1024\n"
"ds_read_b64_tr_b16 v[150:151], v35 offset:1280\n"
"ds_read_b64_tr_b16 v[152:153], v35 offset:32\n"
"ds_read_b64_tr_b16 v[154:155], v35 offset:288\n"
"ds_read_b64_tr_b16 v[156:157], v35 offset:1056\n"
"ds_read_b64_tr_b16 v[158:159], v35 offset:1312\n"
"s_waitcnt vmcnt(28) lgkmcnt(0)\n"
"s_barrier\n"
"s_add_u32 s24, s62, 1572864\n"
"s_add_u32 m0, s66, 0\n"
"s_nop 0\n"
"buffer_load_dwordx4 v66, s[32:35], s24 offen lds\n"
"s_add_u32 m0, s66, 1024\n"
"s_nop 0\n"
"buffer_load_dwordx4 v67, s[32:35], s24 offen lds\n"
"s_add_u32 s24, s63, 1572864\n"
"s_add_u32 m0, s66, 2080\n"
"s_nop 0\n"
"buffer_load_dwordx4 v66, s[32:35], s24 offen lds\n"
"s_add_u32 m0, s66, 3104\n"
"s_nop 0\n"
"buffer_load_dwordx4 v67, s[32:35], s24 offen lds\n"
"ds_read_b64_tr_b16 v[160:161], v37 offset:34816\n"
"ds_read_b64_tr_b16 v[162:163], v37 offset:35072\n"
"ds_read_b64_tr_b16 v[164:165], v37 offset:35840\n"
"ds_read_b64_tr_b16 v[166:167], v37 offset:36096\n"
"ds_read_b64_tr_b16 v[168:169], v37 offset:34848\n"
"ds_read_b64_tr_b16 v[170:171], v37 offset:35104\n"
"ds_read_b64_tr_b16 v[172:173], v37 offset:35872\n"
"ds_read_b64_tr_b16 v[174:175], v37 offset:36128\n"
"s_waitcnt vmcnt(28) lgkmcnt(0)\n"
"s_barrier\n"
"s_add_u32 s24, s62, 2097152\n"
"s_add_u32 m0, s66, 133120\n"
"s_nop 0\n"
"buffer_load_dwordx4 v66, s[32:35], s24 offen lds\n"
"s_add_u32 m0, s66, 134144\n"
"s_nop 0\n"
"buffer_load_dwordx4 v67, s[32:35], s24 offen lds\n"
"s_add_u32 s24, s63, 2097152\n"
"s_add_u32 m0, s66, 135200\n"
"s_nop 0\n"
"buffer_load_dwordx4 v66, s[32:35], s24 offen lds\n"
"s_add_u32 m0, s66, 136224\n"
"s_nop 0\n"
"buffer_load_dwordx4 v67, s[32:35], s24 offen lds\n"
"ds_read_b64_tr_b16 a[208:209], v34 offset:18176\n"
"ds_read_b64_tr_b16 a[210:211], v34 offset:18432\n"
"ds_read_b64_tr_b16 a[212:213], v34 offset:19200\n"
"ds_read_b64_tr_b16 a[214:215], v34 offset:19456\n"
"ds_read_b64_tr_b16 a[216:217], v34 offset:18184\n"
"ds_read_b64_tr_b16 a[218:219], v34 offset:18440\n"
"ds_read_b64_tr_b16 a[220:221], v34 offset:19208\n"
"ds_read_b64_tr_b16 a[222:223], v34 offset:19464\n"
"s_waitcnt vmcnt(28) lgkmcnt(0)\n"
"s_barrier\n"
"s_add_u32 s24, s62, 2621440\n"
"s_add_u32 m0, s66, 116480\n"
"s_nop 0\n"
"buffer_load_dwordx4 v66, s[32:35], s24 offen lds\n"
"s_add_u32 m0, s66, 117504\n"
"s_nop 0\n"
"buffer_load_dwordx4 v67, s[32:35], s24 offen lds\n"
"s_add_u32 s24, s63, 2621440\n"
"s_add_u32 m0, s66, 118560\n"
"s_nop 0\n"
"buffer_load_dwordx4 v66, s[32:35], s24 offen lds\n"
"s_add_u32 m0, s66, 119584\n"
"s_nop 0\n"
"buffer_load_dwordx4 v67, s[32:35], s24 offen lds\n"
"ds_read_b64_tr_b16 v[176:177], v37 offset:1536\n"
"ds_read_b64_tr_b16 v[178:179], v37 offset:1792\n"
"ds_read_b64_tr_b16 v[180:181], v37 offset:2560\n"
"ds_read_b64_tr_b16 v[182:183], v37 offset:2816\n"
"ds_read_b64_tr_b16 v[184:185], v37 offset:1568\n"
"ds_read_b64_tr_b16 v[186:187], v37 offset:1824\n"
"ds_read_b64_tr_b16 v[188:189], v37 offset:2592\n"
"ds_read_b64_tr_b16 v[190:191], v37 offset:2848\n"
"s_waitcnt vmcnt(28) lgkmcnt(0)\n"
"s_barrier\n"
"s_add_u32 s24, s62, 3145728\n"
"s_add_u32 m0, s66, 99840\n"
"s_nop 0\n"
"buffer_load_dwordx4 v66, s[32:35], s24 offen lds\n"
"s_add_u32 m0, s66, 100864\n"
"s_nop 0\n"
"buffer_load_dwordx4 v67, s[32:35], s24 offen lds\n"
"s_add_u32 s24, s63, 3145728\n"
"s_add_u32 m0, s66, 101920\n"
"s_nop 0\n"
"buffer_load_dwordx4 v66, s[32:35], s24 offen lds\n"
"s_add_u32 m0, s66, 102944\n"
"s_nop 0\n"
"buffer_load_dwordx4 v67, s[32:35], s24 offen lds\n"
"ds_read_b64_tr_b16 a[224:225], v33 offset:34048\n"
"ds_read_b64_tr_b16 a[226:227], v33 offset:34304\n"
"ds_read_b64_tr_b16 a[228:229], v33 offset:35072\n"
"ds_read_b64_tr_b16 a[230:231], v33 offset:35328\n"
"ds_read_b64_tr_b16 a[232:233], v33 offset:34056\n"
"ds_read_b64_tr_b16 a[234:235], v33 offset:34312\n"
"ds_read_b64_tr_b16 a[236:237], v33 offset:35080\n"
"ds_read_b64_tr_b16 a[238:239], v33 offset:35336\n"
"s_waitcnt vmcnt(28) lgkmcnt(0)\n"
"s_barrier\n"
"s_add_u32 s24, s62, 3670016\n"
"s_add_u32 m0, s66, 83200\n"
"s_nop 0\n"
"buffer_load_dwordx4 v66, s[32:35], s24 offen lds\n"
"s_add_u32 m0, s66, 84224\n"
"s_nop 0\n"
"buffer_load_dwordx4 v67, s[32:35], s24 offen lds\n"
"s_add_u32 s24, s63, 3670016\n"
"s_add_u32 m0, s66, 85280\n"
"s_nop 0\n"
"buffer_load_dwordx4 v66, s[32:35], s24 offen lds\n"
"s_add_u32 m0, s66, 86304\n"
"s_nop 0\n"
"buffer_load_dwordx4 v67, s[32:35], s24 offen lds\n"
"ds_read_b64_tr_b16 a[240:241], v33 offset:17408\n"
"ds_read_b64_tr_b16 a[242:243], v33 offset:17664\n"
"ds_read_b64_tr_b16 a[244:245], v33 offset:18432\n"
"ds_read_b64_tr_b16 a[246:247], v33 offset:18688\n"
"ds_read_b64_tr_b16 a[248:249], v33 offset:17416\n"
"ds_read_b64_tr_b16 a[250:251], v33 offset:17672\n"
"ds_read_b64_tr_b16 a[252:253], v33 offset:18440\n"
"ds_read_b64_tr_b16 a[254:255], v33 offset:18696\n"
"s_waitcnt vmcnt(28) lgkmcnt(0)\n"
"s_barrier\n"
"s_add_u32 s24, s62, 4194304\n"
"s_add_u32 m0, s66, 66560\n"
"s_nop 0\n"
"buffer_load_dwordx4 v66, s[32:35], s24 offen lds\n"
"s_add_u32 m0, s66, 67584\n"
"s_nop 0\n"
"buffer_load_dwordx4 v67, s[32:35], s24 offen lds\n"
"s_add_u32 s24, s63, 4194304\n"
"s_add_u32 m0, s66, 68640\n"
"s_nop 0\n"
"buffer_load_dwordx4 v66, s[32:35], s24 offen lds\n"
"s_add_u32 m0, s66, 69664\n"
"s_nop 0\n"
"buffer_load_dwordx4 v67, s[32:35], s24 offen lds\n"
"ds_read_b64_tr_b16 a[0:1], v33 offset:768\n"
"ds_read_b64_tr_b16 a[2:3], v33 offset:1024\n"
"ds_read_b64_tr_b16 a[4:5], v33 offset:1792\n"
"ds_read_b64_tr_b16 a[6:7], v33 offset:2048\n"
"ds_read_b64_tr_b16 a[8:9], v33 offset:776\n"
"ds_read_b64_tr_b16 a[10:11], v33 offset:1032\n"
"ds_read_b64_tr_b16 a[12:13], v33 offset:1800\n"
"ds_read_b64_tr_b16 a[14:15], v33 offset:2056\n"
"s_waitcnt vmcnt(28) lgkmcnt(0)\n"
"s_barrier\n"
"s_add_u32 s24, s64, 256\n"
"s_add_u32 m0, s66, 49920\n"
"s_nop 0\n"
"buffer_load_dwordx4 v68, s[44:47], s24 offen lds\n"
"s_add_u32 m0, s66, 50944\n"
"s_nop 0\n"
"buffer_load_dwordx4 v69, s[44:47], s24 offen lds\n"
"s_add_u32 s24, s65, 256\n"
"s_add_u32 m0, s66, 52000\n"
"s_nop 0\n"
"buffer_load_dwordx4 v68, s[44:47], s24 offen lds\n"
"s_add_u32 m0, s66, 53024\n"
"s_nop 0\n"
"buffer_load_dwordx4 v69, s[44:47], s24 offen lds\n"
"ds_read_b64_tr_b16 a[16:17], v32 offset:33280\n"
"ds_read_b64_tr_b16 a[18:19], v32 offset:33536\n"
"ds_read_b64_tr_b16 a[20:21], v32 offset:34304\n"
"ds_read_b64_tr_b16 a[22:23], v32 offset:34560\n"
"ds_read_b64_tr_b16 a[24:25], v32 offset:33288\n"
"ds_read_b64_tr_b16 a[26:27], v32 offset:33544\n"
"ds_read_b64_tr_b16 a[28:29], v32 offset:34312\n"
"ds_read_b64_tr_b16 a[30:31], v32 offset:34568\n"
"s_waitcnt vmcnt(28) lgkmcnt(0)\n"
"s_barrier\n"
"s_add_u32 s24, s64, 73984\n"
"s_add_u32 m0, s66, 33280\n"
"s_nop 0\n"
"buffer_load_dwordx4 v68, s[44:47], s24 offen lds\n"
"s_add_u32 m0, s66, 34304\n"
"s_nop 0\n"
"buffer_load_dwordx4 v69, s[44:47], s24 offen lds\n"
"s_add_u32 s24, s65, 73984\n"
"s_add_u32 m0, s66, 35360\n"
"s_nop 0\n"
"buffer_load_dwordx4 v68, s[44:47], s24 offen lds\n"
"s_add_u32 m0, s66, 36384\n"
"s_nop 0\n"
"buffer_load_dwordx4 v69, s[44:47], s24 offen lds\n"
"ds_read_b64_tr_b16 a[32:33], v32 offset:16640\n"
"ds_read_b64_tr_b16 a[34:35], v32 offset:16896\n"
"ds_read_b64_tr_b16 a[36:37], v32 offset:17664\n"
"ds_read_b64_tr_b16 a[38:39], v32 offset:17920\n"
"ds_read_b64_tr_b16 a[40:41], v32 offset:16648\n"
"ds_read_b64_tr_b16 a[42:43], v32 offset:16904\n"
"ds_read_b64_tr_b16 a[44:45], v32 offset:17672\n"
"ds_read_b64_tr_b16 a[46:47], v32 offset:17928\n"
"s_waitcnt vmcnt(28) lgkmcnt(0)\n"
"s_barrier\n"
"s_add_u32 s24, s64, 768\n"
"s_add_u32 m0, s66, 16640\n"
"s_nop 0\n"
"buffer_load_dwordx4 v68, s[44:47], s24 offen lds\n"
"s_add_u32 m0, s66, 17664\n"
"s_nop 0\n"
"buffer_load_dwordx4 v69, s[44:47], s24 offen lds\n"
"s_add_u32 s24, s65, 768\n"
"s_add_u32 m0, s66, 18720\n"
"s_nop 0\n"
"buffer_load_dwordx4 v68, s[44:47], s24 offen lds\n"
"s_add_u32 m0, s66, 19744\n"
"s_nop 0\n"
"buffer_load_dwordx4 v69, s[44:47], s24 offen lds\n"
"ds_read_b64_tr_b16 a[48:49], v32 offset:0\n"
"ds_read_b64_tr_b16 a[50:51], v32 offset:256\n"
"ds_read_b64_tr_b16 a[52:53], v32 offset:1024\n"
"ds_read_b64_tr_b16 a[54:55], v32 offset:1280\n"
"ds_read_b64_tr_b16 a[56:57], v32 offset:8\n"
"ds_read_b64_tr_b16 a[58:59], v32 offset:264\n"
"ds_read_b64_tr_b16 a[60:61], v32 offset:1032\n"
"ds_read_b64_tr_b16 a[62:63], v32 offset:1288\n"
"s_waitcnt vmcnt(28) lgkmcnt(0)\n"
"s_barrier\n"
"s_add_u32 s24, s64, 74496\n"
"s_add_u32 m0, s66, 0\n"
"s_nop 0\n"
"buffer_load_dwordx4 v68, s[44:47], s24 offen lds\n"
"s_add_u32 m0, s66, 1024\n"
"s_nop 0\n"
"buffer_load_dwordx4 v69, s[44:47], s24 offen lds\n"
"s_add_u32 s24, s65, 74496\n"
"s_add_u32 m0, s66, 2080\n"
"s_nop 0\n"
"buffer_load_dwordx4 v68, s[44:47], s24 offen lds\n"
"s_add_u32 m0, s66, 3104\n"
"s_nop 0\n"
"buffer_load_dwordx4 v69, s[44:47], s24 offen lds\n"
"ds_read_b64_tr_b16 a[64:65], v34 offset:34816\n"
"ds_read_b64_tr_b16 a[66:67], v34 offset:35072\n"
"ds_read_b64_tr_b16 a[68:69], v34 offset:35840\n"
"ds_read_b64_tr_b16 a[70:71], v34 offset:36096\n"
"ds_read_b64_tr_b16 a[72:73], v34 offset:34824\n"
"ds_read_b64_tr_b16 a[74:75], v34 offset:35080\n"
"ds_read_b64_tr_b16 a[76:77], v34 offset:35848\n"
"ds_read_b64_tr_b16 a[78:79], v34 offset:36104\n"
"s_waitcnt vmcnt(28) lgkmcnt(0)\n"
"s_barrier\n"
"s_add_u32 s24, s64, 1024\n"
"s_add_u32 m0, s66, 133120\n"
"s_nop 0\n"
"buffer_load_dwordx4 v70, s[44:47], s24 offen lds\n"
"s_add_u32 m0, s66, 134144\n"
"s_nop 0\n"
"buffer_load_dwordx4 v71, s[44:47], s24 offen lds\n"
"s_add_u32 s24, s65, 1024\n"
"s_add_u32 m0, s66, 135200\n"
"s_nop 0\n"
"buffer_load_dwordx4 v70, s[44:47], s24 offen lds\n"
"s_add_u32 m0, s66, 136224\n"
"s_nop 0\n"
"buffer_load_dwordx4 v71, s[44:47], s24 offen lds\n"
"ds_read_b64_tr_b16 a[80:81], v34 offset:18176\n"
"ds_read_b64_tr_b16 a[82:83], v34 offset:18432\n"
"ds_read_b64_tr_b16 a[84:85], v34 offset:19200\n"
"ds_read_b64_tr_b16 a[86:87], v34 offset:19456\n"
"ds_read_b64_tr_b16 a[88:89], v34 offset:18184\n"
"ds_read_b64_tr_b16 a[90:91], v34 offset:18440\n"
"ds_read_b64_tr_b16 a[92:93], v34 offset:19208\n"
"ds_read_b64_tr_b16 a[94:95], v34 offset:19464\n"
"s_waitcnt vmcnt(28) lgkmcnt(0)\n"
"s_barrier\n"
"s_add_u32 s24, s62, 4194304\n"
"s_add_u32 m0, s66, 116480\n"
"s_nop 0\n"
"buffer_load_dwordx4 v66, s[40:43], s24 offen lds\n"
"s_add_u32 m0, s66, 117504\n"
"s_nop 0\n"
"buffer_load_dwordx4 v67, s[40:43], s24 offen lds\n"
"s_add_u32 s24, s63, 4194304\n"
"s_add_u32 m0, s66, 118560\n"
"s_nop 0\n"
"buffer_load_dwordx4 v66, s[40:43], s24 offen lds\n"
"s_add_u32 m0, s66, 119584\n"
"s_nop 0\n"
"buffer_load_dwordx4 v67, s[40:43], s24 offen lds\n"
"ds_read_b64_tr_b16 a[96:97], v34 offset:1536\n"
"ds_read_b64_tr_b16 a[98:99], v34 offset:1792\n"
"ds_read_b64_tr_b16 a[100:101], v34 offset:2560\n"
"ds_read_b64_tr_b16 a[102:103], v34 offset:2816\n"
"ds_read_b64_tr_b16 a[104:105], v34 offset:1544\n"
"ds_read_b64_tr_b16 a[106:107], v34 offset:1800\n"
"ds_read_b64_tr_b16 a[108:109], v34 offset:2568\n"
"ds_read_b64_tr_b16 a[110:111], v34 offset:2824\n"
"s_waitcnt vmcnt(28) lgkmcnt(0)\n"
"s_barrier\n"
"s_add_u32 s24, s62, 3670016\n"
"s_add_u32 m0, s66, 99840\n"
"s_nop 0\n"
"buffer_load_dwordx4 v66, s[40:43], s24 offen lds\n"
"s_add_u32 m0, s66, 100864\n"
"s_nop 0\n"
"buffer_load_dwordx4 v67, s[40:43], s24 offen lds\n"
"s_add_u32 s24, s63, 3670016\n"
"s_add_u32 m0, s66, 101920\n"
"s_nop 0\n"
"buffer_load_dwordx4 v66, s[40:43], s24 offen lds\n"
"s_add_u32 m0, s66, 102944\n"
"s_nop 0\n"
"buffer_load_dwordx4 v67, s[40:43], s24 offen lds\n"
"ds_read_b64_tr_b16 a[112:113], v33 offset:34048\n"
"ds_read_b64_tr_b16 a[114:115], v33 offset:34304\n"
"ds_read_b64_tr_b16 a[116:117], v33 offset:35072\n"
"ds_read_b64_tr_b16 a[118:119], v33 offset:35328\n"
"ds_read_b64_tr_b16 a[120:121], v33 offset:34056\n"
"ds_read_b64_tr_b16 a[122:123], v33 offset:34312\n"
"ds_read_b64_tr_b16 a[124:125], v33 offset:35080\n"
"ds_read_b64_tr_b16 a[126:127], v33 offset:35336\n"
"s_waitcnt vmcnt(28) lgkmcnt(0)\n"
"s_barrier\n"
"s_add_u32 s24, s64, 74240\n"
"s_add_u32 m0, s66, 83200\n"
"s_nop 0\n"
"buffer_load_dwordx4 v68, s[44:47], s24 offen lds\n"
"s_add_u32 m0, s66, 84224\n"
"s_nop 0\n"
"buffer_load_dwordx4 v69, s[44:47], s24 offen lds\n"
"s_add_u32 s24, s65, 74240\n"
"s_add_u32 m0, s66, 85280\n"
"s_nop 0\n"
"buffer_load_dwordx4 v68, s[44:47], s24 offen lds\n"
"s_add_u32 m0, s66, 86304\n"
"s_nop 0\n"
"buffer_load_dwordx4 v69, s[44:47], s24 offen lds\n"
"ds_read_b64_tr_b16 a[128:129], v33 offset:17408\n"
"ds_read_b64_tr_b16 a[130:131], v33 offset:17664\n"
"ds_read_b64_tr_b16 a[132:133], v33 offset:18432\n"
"ds_read_b64_tr_b16 a[134:135], v33 offset:18688\n"
"ds_read_b64_tr_b16 a[136:137], v33 offset:17416\n"
"ds_read_b64_tr_b16 a[138:139], v33 offset:17672\n"
"ds_read_b64_tr_b16 a[140:141], v33 offset:18440\n"
"ds_read_b64_tr_b16 a[142:143], v33 offset:18696\n"
"s_waitcnt vmcnt(28) lgkmcnt(0)\n"
"s_barrier\n"
"s_add_u32 s24, s64, 512\n"
"s_add_u32 m0, s66, 66560\n"
"s_nop 0\n"
"buffer_load_dwordx4 v68, s[44:47], s24 offen lds\n"
"s_add_u32 m0, s66, 67584\n"
"s_nop 0\n"
"buffer_load_dwordx4 v69, s[44:47], s24 offen lds\n"
"s_add_u32 s24, s65, 512\n"
"s_add_u32 m0, s66, 68640\n"
"s_nop 0\n"
"buffer_load_dwordx4 v68, s[44:47], s24 offen lds\n"
"s_add_u32 m0, s66, 69664\n"
"s_nop 0\n"
"buffer_load_dwordx4 v69, s[44:47], s24 offen lds\n"
"ds_read_b64_tr_b16 v[192:193], v39 offset:768\n"
"ds_read_b64_tr_b16 v[194:195], v39 offset:2848\n"
"ds_read_b64_tr_b16 v[196:197], v39 offset:1280\n"
"ds_read_b64_tr_b16 v[198:199], v39 offset:3360\n"
"ds_read_b64_tr_b16 v[200:201], v39 offset:1792\n"
"ds_read_b64_tr_b16 v[202:203], v39 offset:3872\n"
"ds_read_b64_tr_b16 v[204:205], v39 offset:2304\n"
"ds_read_b64_tr_b16 v[206:207], v39 offset:4384\n"
"s_waitcnt vmcnt(28) lgkmcnt(0)\n"
"s_barrier\n"
"s_add_u32 s24, s64, 73728\n"
"s_add_u32 m0, s66, 49920\n"
"s_nop 0\n"
"buffer_load_dwordx4 v68, s[44:47], s24 offen lds\n"
"s_add_u32 m0, s66, 50944\n"
"s_nop 0\n"
"buffer_load_dwordx4 v69, s[44:47], s24 offen lds\n"
"s_add_u32 s24, s65, 73728\n"
"s_add_u32 m0, s66, 52000\n"
"s_nop 0\n"
"buffer_load_dwordx4 v68, s[44:47], s24 offen lds\n"
"s_add_u32 m0, s66, 53024\n"
"s_nop 0\n"
"buffer_load_dwordx4 v69, s[44:47], s24 offen lds\n"
"ds_read_b64_tr_b16 v[208:209], v38 offset:33280\n"
"ds_read_b64_tr_b16 v[210:211], v38 offset:35360\n"
"ds_read_b64_tr_b16 v[212:213], v38 offset:33792\n"
"ds_read_b64_tr_b16 v[214:215], v38 offset:35872\n"
"ds_read_b64_tr_b16 v[216:217], v38 offset:34304\n"
"ds_read_b64_tr_b16 v[218:219], v38 offset:36384\n"
"ds_read_b64_tr_b16 v[220:221], v38 offset:34816\n"
"ds_read_b64_tr_b16 v[222:223], v38 offset:36896\n"
"s_waitcnt vmcnt(28) lgkmcnt(0)\n"
"s_barrier\n"
"s_add_u32 s24, s64, 0\n"
"s_add_u32 m0, s66, 33280\n"
"s_nop 0\n"
"buffer_load_dwordx4 v68, s[44:47], s24 offen lds\n"
"s_add_u32 m0, s66, 34304\n"
"s_nop 0\n"
"buffer_load_dwordx4 v69, s[44:47], s24 offen lds\n"
"s_add_u32 s24, s65, 0\n"
"s_add_u32 m0, s66, 35360\n"
"s_nop 0\n"
"buffer_load_dwordx4 v68, s[44:47], s24 offen lds\n"
"s_add_u32 m0, s66, 36384\n"
"s_nop 0\n"
"buffer_load_dwordx4 v69, s[44:47], s24 offen lds\n"
"ds_read_b64_tr_b16 v[224:225], v38 offset:16640\n"
"ds_read_b64_tr_b16 v[226:227], v38 offset:18720\n"
"ds_read_b64_tr_b16 v[228:229], v38 offset:17152\n"
"ds_read_b64_tr_b16 v[230:231], v38 offset:19232\n"
"ds_read_b64_tr_b16 v[232:233], v38 offset:17664\n"
"ds_read_b64_tr_b16 v[234:235], v38 offset:19744\n"
"ds_read_b64_tr_b16 v[236:237], v38 offset:18176\n"
"ds_read_b64_tr_b16 v[238:239], v38 offset:20256\n"
"s_waitcnt vmcnt(28) lgkmcnt(0)\n"
"s_barrier\n"
"s_add_u32 s24, s62, 2621440\n"
"s_add_u32 m0, s66, 16640\n"
"s_nop 0\n"
"buffer_load_dwordx4 v66, s[36:39], s24 offen lds\n"
"s_add_u32 m0, s66, 17664\n"
"s_nop 0\n"
"buffer_load_dwordx4 v67, s[36:39], s24 offen lds\n"
"s_add_u32 s24, s63, 2621440\n"
"s_add_u32 m0, s66, 18720\n"
"s_nop 0\n"
"buffer_load_dwordx4 v66, s[36:39], s24 offen lds\n"
"s_add_u32 m0, s66, 19744\n"
"s_nop 0\n"
"buffer_load_dwordx4 v67, s[36:39], s24 offen lds\n"
"ds_read_b64_tr_b16 v[240:241], v38 offset:0\n"
"ds_read_b64_tr_b16 v[242:243], v38 offset:2080\n"
"ds_read_b64_tr_b16 v[244:245], v38 offset:512\n"
"ds_read_b64_tr_b16 v[246:247], v38 offset:2592\n"
"ds_read_b64_tr_b16 v[248:249], v38 offset:1024\n"
"ds_read_b64_tr_b16 v[250:251], v38 offset:3104\n"
"ds_read_b64_tr_b16 v[252:253], v38 offset:1536\n"
"ds_read_b64_tr_b16 v[254:255], v38 offset:3616\n"
"s_waitcnt vmcnt(28) lgkmcnt(0)\n"
"s_barrier\n"
"s_add_u32 s24, s62, 1048576\n"
"s_add_u32 m0, s66, 0\n"
"s_nop 0\n"
"buffer_load_dwordx4 v66, s[36:39], s24 offen lds\n"
"s_add_u32 m0, s66, 1024\n"
"s_nop 0\n"
"buffer_load_dwordx4 v67, s[36:39], s24 offen lds\n"
"s_add_u32 s24, s63, 1048576\n"
"s_add_u32 m0, s66, 2080\n"
"s_nop 0\n"
"buffer_load_dwordx4 v66, s[36:39], s24 offen lds\n"
"s_add_u32 m0, s66, 3104\n"
"s_nop 0\n"
"buffer_load_dwordx4 v67, s[36:39], s24 offen lds\n"
"ds_read_b64_tr_b16 v[16:17], v43 offset:34816\n"
"ds_read_b64_tr_b16 v[18:19], v43 offset:36896\n"
"ds_read_b64_tr_b16 v[20:21], v43 offset:35072\n"
"ds_read_b64_tr_b16 v[22:23], v43 offset:37152\n"
"ds_read_b64_tr_b16 v[24:25], v43 offset:35328\n"
"ds_read_b64_tr_b16 v[26:27], v43 offset:37408\n"
"ds_read_b64_tr_b16 v[28:29], v43 offset:35584\n"
"ds_read_b64_tr_b16 v[30:31], v43 offset:37664\n"
"s_waitcnt vmcnt(28) lgkmcnt(0)\n"
"s_barrier\n"
"ds_write_b128 v45, v[16:19] offset:0\n"
"ds_write_b128 v45, v[20:23] offset:1024\n"
"ds_write_b128 v45, v[24:27] offset:2048\n"
"ds_write_b128 v45, v[28:31] offset:3072\n"
"ds_read_b64_tr_b16 v[0:1], v37 offset:18176\n"
"ds_read_b64_tr_b16 v[2:3], v37 offset:18432\n"
"ds_read_b64_tr_b16 v[4:5], v37 offset:19200\n"
"ds_read_b64_tr_b16 v[6:7], v37 offset:19456\n"
"ds_read_b64_tr_b16 v[8:9], v37 offset:18208\n"
"ds_read_b64_tr_b16 v[10:11], v37 offset:18464\n"
"ds_read_b64_tr_b16 v[12:13], v37 offset:19232\n"
"ds_read_b64_tr_b16 v[14:15], v37 offset:19488\n"
"s_waitcnt vmcnt(24) lgkmcnt(0)\n"
"s_barrier\n"
"ds_write_b128 v44, v[0:3] offset:49152\n"
"ds_write_b128 v44, v[4:7] offset:50176\n"
"ds_write_b128 v44, v[8:11] offset:51200\n"
"ds_write_b128 v44, v[12:15] offset:52224\n"
"ds_read_b64_tr_b16 v[16:17], v37 offset:1536\n"
"ds_read_b64_tr_b16 v[18:19], v37 offset:1792\n"
"ds_read_b64_tr_b16 v[20:21], v37 offset:2560\n"
"ds_read_b64_tr_b16 v[22:23], v37 offset:2816\n"
"ds_read_b64_tr_b16 v[24:25], v37 offset:1568\n"
"ds_read_b64_tr_b16 v[26:27], v37 offset:1824\n"
"ds_read_b64_tr_b16 v[28:29], v37 offset:2592\n"
"ds_read_b64_tr_b16 v[30:31], v37 offset:2848\n"
"s_waitcnt vmcnt(20) lgkmcnt(0)\n"
"s_barrier\n"
"ds_write_b128 v44, v[16:19] offset:32768\n"
"ds_write_b128 v44, v[20:23] offset:33792\n"
"ds_write_b128 v44, v[24:27] offset:34816\n"
"ds_write_b128 v44, v[28:31] offset:35840\n"
"ds_read_b64_tr_b16 v[0:1], v39 offset:34048\n"
"ds_read_b64_tr_b16 v[2:3], v39 offset:36128\n"
"ds_read_b64_tr_b16 v[4:5], v39 offset:34560\n"
"ds_read_b64_tr_b16 v[6:7], v39 offset:36640\n"
"ds_read_b64_tr_b16 v[8:9], v39 offset:35072\n"
"ds_read_b64_tr_b16 v[10:11], v39 offset:37152\n"
"ds_read_b64_tr_b16 v[12:13], v39 offset:35584\n"
"ds_read_b64_tr_b16 v[14:15], v39 offset:37664\n"
"s_waitcnt vmcnt(16) lgkmcnt(0)\n"
"s_barrier\n"
"ds_write_b128 v44, v[0:3] offset:16384\n"
"ds_write_b128 v44, v[4:7] offset:17408\n"
"ds_write_b128 v44, v[8:11] offset:18432\n"
"ds_write_b128 v44, v[12:15] offset:19456\n"
"ds_read_b64_tr_b16 v[16:17], v39 offset:17408\n"
"ds_read_b64_tr_b16 v[18:19], v39 offset:19488\n"
"ds_read_b64_tr_b16 v[20:21], v39 offset:17920\n"
"ds_read_b64_tr_b16 v[22:23], v39 offset:20000\n"
"ds_read_b64_tr_b16 v[24:25], v39 offset:18432\n"
"ds_read_b64_tr_b16 v[26:27], v39 offset:20512\n"
"ds_read_b64_tr_b16 v[28:29], v39 offset:18944\n"
"ds_read_b64_tr_b16 v[30:31], v39 offset:21024\n"
"s_waitcnt vmcnt(12) lgkmcnt(0)\n"
"s_barrier\n"
"ds_write_b128 v44, v[16:19] offset:0\n"
"ds_write_b128 v44, v[20:23] offset:1024\n"
"ds_write_b128 v44, v[24:27] offset:2048\n"
"ds_write_b128 v44, v[28:31] offset:3072\n"
"ds_read_b64_tr_b16 v[0:1], v39 offset:768\n"
"ds_read_b64_tr_b16 v[2:3], v39 offset:2848\n"
"ds_read_b64_tr_b16 v[4:5], v39 offset:1280\n"
"ds_read_b64_tr_b16 v[6:7], v39 offset:3360\n"
"ds_read_b64_tr_b16 v[8:9], v39 offset:1792\n"
"ds_read_b64_tr_b16 v[10:11], v39 offset:3872\n"
"ds_read_b64_tr_b16 v[12:13], v39 offset:2304\n"
"ds_read_b64_tr_b16 v[14:15], v39 offset:4384\n"
"s_waitcnt vmcnt(8) lgkmcnt(0)\n"
"s_barrier\n"
"ds_write_b128 v53, v[0:3] offset:49152\n"
"ds_write_b128 v53, v[4:7] offset:50176\n"
"ds_write_b128 v53, v[8:11] offset:51200\n"
"ds_write_b128 v53, v[12:15] offset:52224\n"
"ds_read_b64_tr_b16 v[16:17], v38 offset:33280\n"
"ds_read_b64_tr_b16 v[18:19], v38 offset:35360\n"
"ds_read_b64_tr_b16 v[20:21], v38 offset:33792\n"
"ds_read_b64_tr_b16 v[22:23], v38 offset:35872\n"
"ds_read_b64_tr_b16 v[24:25], v38 offset:34304\n"
"ds_read_b64_tr_b16 v[26:27], v38 offset:36384\n"
"ds_read_b64_tr_b16 v[28:29], v38 offset:34816\n"
"ds_read_b64_tr_b16 v[30:31], v38 offset:36896\n"
"s_waitcnt vmcnt(4) lgkmcnt(0)\n"
"s_barrier\n"
"ds_write_b128 v53, v[16:19] offset:32768\n"
"ds_write_b128 v53, v[20:23] offset:33792\n"
"ds_write_b128 v53, v[24:27] offset:34816\n"
"ds_write_b128 v53, v[28:31] offset:35840\n"
"ds_read_b64_tr_b16 v[0:1], v32 offset:16640\n"
"ds_read_b64_tr_b16 v[2:3], v32 offset:16896\n"
"ds_read_b64_tr_b16 v[4:5], v32 offset:17664\n"
"ds_read_b64_tr_b16 v[6:7], v32 offset:17920\n"
"ds_read_b64_tr_b16 v[8:9], v32 offset:16648\n"
"ds_read_b64_tr_b16 v[10:11], v32 offset:16904\n"
"ds_read_b64_tr_b16 v[12:13], v32 offset:17672\n"
"ds_read_b64_tr_b16 v[14:15], v32 offset:17928\n"
"s_waitcnt vmcnt(0) lgkmcnt(0)\n"
"s_barrier\n"
"ds_write_b128 v53, v[0:3] offset:16384\n"
"ds_write_b128 v53, v[4:7] offset:17408\n"
"ds_write_b128 v53, v[8:11] offset:18432\n"
"ds_write_b128 v53, v[12:15] offset:19456\n"
"ds_read_b64_tr_b16 v[16:17], v32 offset:0\n"
"ds_read_b64_tr_b16 v[18:19], v32 offset:256\n"
"ds_read_b64_tr_b16 v[20:21], v32 offset:1024\n"
"ds_read_b64_tr_b16 v[22:23], v32 offset:1280\n"
"ds_read_b64_tr_b16 v[24:25], v32 offset:8\n"
"ds_read_b64_tr_b16 v[26:27], v32 offset:264\n"
"ds_read_b64_tr_b16 v[28:29], v32 offset:1032\n"
"ds_read_b64_tr_b16 v[30:31], v32 offset:1288\n"
"s_waitcnt lgkmcnt(0)\n"
"s_barrier\n"
"ds_write_b128 v53, v[16:19] offset:0\n"
"ds_write_b128 v53, v[20:23] offset:1024\n"
"ds_write_b128 v53, v[24:27] offset:2048\n"
"ds_write_b128 v53, v[28:31] offset:3072\n"
"s_mov_b32 s52, 0xff00ff\n"
"s_mov_b32 s53, 0xff00ff\n"
"s_mov_b32 s54, 0xff00ff00\n"
"s_mov_b32 s55, 0xff00ff00\n"
"s_mov_b32 s56, 0x3f803f80\n"
"s_mov_b32 s57, 0x3f803f80\n"
"s_mov_b32 s58, 0x3f003f00\n"
"s_mov_b32 s59, 0x3f003f00\n"
"s_mov_b32 s60, 0x3e0293ee\n"
"s_mov_b32 s61, 0x3e0293ee\n"
"s_mov_b32 s44, 0x30003\n"
"s_mov_b32 s45, 0x30003\n"
"s_mov_b32 s46, 0xc000c\n"
"s_mov_b32 s47, 0xc000c\n"
"s_mov_b32 s48, 0x300030\n"
"s_mov_b32 s49, 0x300030\n"
"s_mov_b32 s50, 0xc000c0\n"
"s_mov_b32 s51, 0xc000c0\n"
"v_mov_b32 v55, 17476\n"
"v_mov_b32 v65, 61166\n"
"v_cndmask_b32 v52, v65, v55, s[52:53]\n"
"s_lshl_b32 s71, s15, 9\n"
"v_bfe_u32 v65, v64, 4, 2\n"
"v_mul_u32_u24 v54, 128, v65\n"
"v_bfe_u32 v65, v64, 0, 3\n"
"v_lshl_add_u32 v54, v65, 4, v54\n"
"v_add_u32 v54, s71, v54\n"
"v_and_b32 v55, 7, v64\n"
"v_bfe_u32 v65, v64, 4, 1\n"
"v_lshlrev_b32 v65, 2, v65\n"
"v_sub_u32 v55, v55, v65\n"
"v_cmp_gt_u32 vcc, 4, v55\n"
"v_bfe_u32 v65, v64, 3, 1\n"
"v_lshl_add_u32 v55, v65, 2, v55\n"
"v_cndmask_b32 v55, -1, v55, vcc\n"
"v_mov_b32 v66, 16256\n"
"v_mov_b32 v67, 1065353216\n"
"v_cmp_eq_u32 vcc, 0, v55\n"
"s_nop 1\n"
"v_cndmask_b32 v56, 0, v66, vcc\n"
"v_cmp_eq_u32 vcc, 1, v55\n"
"s_nop 1\n"
"v_cndmask_b32 v56, v56, v67, vcc\n"
"v_cmp_eq_u32 vcc, 2, v55\n"
"s_nop 1\n"
"v_cndmask_b32 v57, 0, v66, vcc\n"
"v_cmp_eq_u32 vcc, 3, v55\n"
"s_nop 1\n"
"v_cndmask_b32 v57, v57, v67, vcc\n"
"v_cmp_eq_u32 vcc, 4, v55\n"
"s_nop 1\n"
"v_cndmask_b32 v58, 0, v66, vcc\n"
"v_cmp_eq_u32 vcc, 5, v55\n"
"s_nop 1\n"
"v_cndmask_b32 v58, v58, v67, vcc\n"
"v_cmp_eq_u32 vcc, 6, v55\n"
"s_nop 1\n"
"v_cndmask_b32 v59, 0, v66, vcc\n"
"v_cmp_eq_u32 vcc, 7, v55\n"
"s_nop 1\n"
"v_cndmask_b32 v59, v59, v67, vcc\n"
"s_add_u32 s31, s14, 0\n"
"s_lshr_b32 s27, s31, 6\n"
"s_lshl_b32 s27, s27, 13\n"
"s_lshl_b32 s31, s31, 7\n"
"s_add_u32 s31, s31, s27\n"
"s_add_u32 s31, s31, s16\n"
"s_add_u32 s31, s31, 1835008\n"
"s_lshl_b32 s24, s14, 2\n"
"s_add_u32 s24, s24, s15\n"
"s_add_u32 s39, s24, 136\n"
"s_lshr_b32 s27, s39, 6\n"
"s_lshl_b32 s27, s27, 13\n"
"s_lshl_b32 s39, s39, 7\n"
"s_add_u32 s39, s39, s27\n"
"s_add_u32 s39, s39, s16\n"
"s_add_u32 s39, s39, 1835008\n"
"s_lshl_b32 s24, s14, 14\n"
"s_lshl_b32 s26, s15, 11\n"
"s_add_u32 s66, s24, s26\n"
"s_add_u32 s66, s66, s16\n"
"s_sub_u32 s66, s66, s69\n"
"s_add_u32 s70, s26, 8192\n"
"s_mov_b32 m0, s70\n"
"s_sub_u32 s70, s70, s69\n"
"s_lshr_b32 s24, s14, 2\n"
"s_lshl_b32 s24, s24, 13\n"
"s_lshl_b32 s25, s14, 11\n"
"s_add_u32 s67, s24, s25\n"
"s_add_u32 s67, s67, s16\n"
"s_add_u32 s67, s67, 1835008\n"
"s_lshr_b32 s24, s14, 1\n"
"s_lshl_b32 s24, s24, 13\n"
"s_lshl_b32 s25, s14, 12\n"
"s_add_u32 s68, s24, s25\n"
"s_lshl_b32 s25, s15, 10\n"
"s_add_u32 s68, s68, s25\n"
"s_add_u32 s68, s68, s16\n"
"s_add_u32 s68, s68, 1572864\n"
"s_sub_u32 s68, s68, s69\n"
"s_lshl_b32 s24, s15, 15\n"
"s_lshl_b32 s25, s14, 8\n"
"s_add_u32 s88, s24, s25\n"
"s_add_u32 s88, s88, s17\n"
"s_add_u32 s88, s88, 3538944\n"
"s_lshr_b32 s25, s14, 3\n"
"s_lshl_b32 s24, s25, 1\n"
"s_add_u32 s25, s25, 32\n"
"s_and_b32 s26, s14, 6\n"
"s_lshl_b32 s26, s26, 2\n"
"s_add_u32 s24, s24, s26\n"
"s_and_b32 s26, s14, 1\n"
"s_add_u32 s24, s24, s26\n"
"s_cmp_eq_u32 s15, 3\n"
"s_cselect_b32 s24, s24, s25\n"
"s_cselect_b32 s90, 1, 0\n"
"s_and_b32 s26, s14, 7\n"
"s_cmp_eq_u32 s26, 7\n"
"s_cselect_b32 s26, 2, 7\n"
"s_cmp_eq_u32 s15, s26\n"
"s_cselect_b32 s90, 1, s90\n"
"s_lshr_b32 s26, s24, 2\n"
"s_mul_i32 s37, s26, 10\n"
"s_lshr_b32 s37, s37, 2\n"
"s_min_u32 s37, s37, 14\n"
"s_lshr_b32 s26, s24, 3\n"
"s_and_b32 s27, s14, 7\n"
"s_cmp_eq_u32 s27, 7\n"
"s_cselect_b32 s26, 4, s26\n"
"s_sub_u32 s26, s26, 1\n"
"s_max_i32 s26, s26, 0\n"
"s_lshl_b32 s26, s26, 3\n"
"s_lshr_b32 s36, s23, s26\n"
"s_and_b32 s36, s36, 255\n"
"s_cmp_eq_u32 s90, 0\n"
"s_cselect_b32 s91, 2147483648, 0\n"
"s_sub_u32 s26, 3, s15\n"
"s_lshl_b32 s26, s26, 4\n"
"s_add_u32 s31, s26, 4096\n"
"s_lshl_b32 s89, s24, 14\n"
"s_add_u32 s89, s89, s17\n"
"s_add_u32 s89, s89, 3538944\n"
"s_sub_u32 s89, s89, s69\n"
"s_and_b32 s26, s24, 31\n"
"s_lshl_b32 s26, s26, 8\n"
"s_add_u32 s78, s26, s16\n"
"s_sub_u32 s79, s16, s69\n"
"s_lshl_b32 s24, s69, 2\n"
"s_sub_u32 s92, s16, s24\n"
"s_add_u32 s92, s92, 1298432\n"
"s_lshl_b32 s25, s14, 6\n"
"s_add_u32 s93, s25, s16\n"
"s_add_u32 s93, s93, 1310720\n"
"s_mov_b32 s94, 12288\n"
"s_cmp_eq_u32 s15, 2\n"
"s_cselect_b32 s25, 8, 0\n"
"s_add_u32 s93, s93, s25\n"
"s_cmp_eq_u32 s12, 1\n"
"s_cselect_b32 s24, 0, 0\n"
"s_add_u32 s88, s88, s24\n"
"s_add_u32 s89, s89, s24\n"
"s_add_u32 s78, s78, s24\n"
"s_add_u32 s79, s79, s24\n"
"s_add_u32 s92, s92, s24\n"
"s_add_u32 s93, s93, s24\n"
"s_add_u32 s12, s14, 32\n"
"s_lshr_b32 s27, s12, 6\n"
"s_lshl_b32 s27, s27, 13\n"
"s_lshl_b32 s12, s12, 7\n"
"s_add_u32 s12, s12, s27\n"
"s_add_u32 s12, s12, s16\n"
"s_add_u32 s12, s12, 1835008\n"
"s_mov_b32 s16, -1\n"
"s_mov_b32 s28, -1\n"
"s_mov_b32 s80, 131072\n"
"s_mov_b32 s81, 1179648\n"
"s_bfe_u32 s6, s21, 524288\n"
"s_bfe_u32 s7, s21, 524296\n"
"s_lshl_b32 s6, s6, 2\n"
"s_lshl_b32 s7, s7, 2\n"
"s_lshr_b32 s23, s22, 24\n"
"s_and_b32 s20, s22, 255\n"
"s_sub_u32 s20, s20, 16\n"
"s_bfe_u32 s21, s22, 524296\n"
"s_sub_u32 s21, s21, 16\n"
"s_bfe_u32 s22, s22, 524304\n"
"s_mov_b32 s32, 2048\n"
"s_mov_b32 s33, 0\n"
"s_mov_b32 s34, 0\n"
"s_cmp_gt_i32 s16, s28\n"
"s_cbranch_scc1 MK_CHw0x\n"
"s_mov_b32 s29, s30\n"
"MK_CHw0:\n"
"buffer_load_dword v63, off, s[8:11], s12 sc1\n"
"s_add_u32 s29, s29, 1\n"
"s_waitcnt vmcnt(0)\n"
"v_readfirstlane_b32 s16, v63\n"
"s_cmp_gt_i32 s16, s28\n"
"s_cbranch_scc1 MK_CHw0x\n"
"s_cmp_lt_u32 s29, 131072\n"
"s_cbranch_scc1 MK_CHw0\n"
"s_mov_b32 s30, 131072\n"
"MK_CHw0x:\n"
"s_waitcnt lgkmcnt(0)\n"
"s_barrier\n"
"ds_read_b128 v[44:47], v53 offset:0\n"
"ds_read_b128 v[48:51], v53 offset:1024\n"
"s_memrealtime s[18:19]\n"
"s_waitcnt lgkmcnt(0)\n"
"v_not_b32 v64, s18\n"
"s_mov_b64 exec, 1\n"
"buffer_store_dword v64, off, s[8:11], s93 sc1\n"
"s_mov_b64 exec, -1\n"
"s_mov_b32 s5, 100\n"
"s_sub_u32 s95, s18, 100000\n"
"s_mov_b32 s97, 0xc0000000\n"
"s_lshr_b32 s38, s6, 2\n"
"s_add_u32 s38, s38, s20\n"
"s_lshr_b32 s100, s7, 2\n"
"s_add_u32 s100, s100, s21\n"
"s_mov_b32 s35, s18\n"
"s_mov_b32 s26, s18\n"
"s_mov_b32 s42, 0x80000000\n"
"s_mov_b32 s43, 0x80000001\n"
"s_mov_b32 s40, 100\n"
"s_waitcnt lgkmcnt(0)\n"
"s_add_u32 s75, s79, s80\n"
"s_add_u32 s76, s75, 4096\n"
"s_add_u32 s77, s79, s81\n"
"MK_CHbarB:\n"
"s_barrier\n"
"buffer_load_dwordx4 v[0:3], v53, s[8:11], s75 offen offset:0\n"
"s_add_u32 s28, s28, 1\n"
"s_and_b32 s24, s28, 15\n"
"s_lshl_b32 s24, s24, 22\n"
"s_add_u32 s24, s24, 33554432\n"
"s_add_u32 s64, s24, s67\n"
"s_add_u32 s65, s24, s68\n"
"s_add_u32 s72, s24, s89\n"
"s_add_u32 s73, s72, 4096\n"
"s_add_u32 s82, s24, s88\n"
"s_add_u32 s83, s82, 131072\n"
"buffer_load_dwordx4 v[4:7], v53, s[8:11], s75 offen offset:1024\n"
"s_add_u32 s84, s82, 262144\n"
"s_add_u32 s85, s82, 393216\n"
"s_add_u32 s86, s82, 524288\n"
"s_bfe_u32 s87, s28, 65540\n"
"s_add_u32 s62, s24, s66\n"
"s_add_u32 s63, s62, 524288\n"
"s_add_u32 s25, s28, 1\n"
"s_and_b32 s41, s25, 15\n"
"s_lshl_b32 s41, s41, 22\n"
"s_add_u32 s41, s41, 33554432\n"
"buffer_load_dwordx4 v[8:11], v53, s[8:11], s75 offen offset:2048\n"
"s_add_u32 s41, s41, s66\n"
"s_mul_i32 s24, s69, 7\n"
"s_sub_u32 s41, s41, s24\n"
"v_and_b32 v60, 256, v53\n"
"v_mul_u32_u24 v60, 2040, v60\n"
"v_lshl_add_u32 v60, v53, 3, v60\n"
"v_and_b32 v61, 512, v53\n"
"v_lshl_or_b32 v60, v61, 22, v60\n"
"s_lshl_b32 s80, s25, 14\n"
"s_add_u32 s80, s80, 131072\n"
"buffer_load_dwordx4 v[12:15], v53, s[8:11], s75 offen offset:3072\n"
"s_and_b32 s24, s25, 7\n"
"s_lshr_b32 s25, s25, 3\n"
"s_lshl_b32 s25, s25, 4\n"
"s_add_u32 s25, s25, s24\n"
"s_lshl_b32 s25, s25, 10\n"
"s_add_u32 s81, s25, 1179648\n"
"s_cmp_eq_u32 s15, 3\n"
"s_cselect_b32 s74, s80, s81\n"
"s_add_u32 s74, s74, s78\n"
"s_add_u32 s92, s92, s94\n"
"buffer_load_dwordx4 v[16:19], v53, s[8:11], s76 offen offset:0\n"
"s_sub_u32 s94, 16384, s94\n"
"s_add_u32 s93, s93, s94\n"
"s_nop 7\n"
"buffer_load_dwordx4 v[20:23], v53, s[8:11], s76 offen offset:1024\n"
"s_nop 9\n"
"buffer_load_dwordx4 v[24:27], v53, s[8:11], s76 offen offset:2048\n"
"s_nop 9\n"
"buffer_load_dwordx4 v[28:31], v53, s[8:11], s76 offen offset:3072\n"
"v_mov_b64 v[64:65], 0\n"
"v_mov_b64 v[66:67], 0\n"
"v_mov_b64 v[68:69], 0\n"
"v_mov_b64 v[70:71], 0\n"
"v_mov_b64 v[72:73], 0\n"
"v_mov_b64 v[74:75], 0\n"
"v_mov_b64 v[76:77], 0\n"
"v_mov_b64 v[78:79], 0\n"
"buffer_load_dwordx4 v[32:35], v53, s[8:11], s77 offen offset:0\n"
"MK_CHstep:\n"
"buffer_load_dwordx4 v[40:43], v53, s[8:11], s62 offen offset:1024 sc1\n"
"s_waitcnt vmcnt(9)\n"
"v_smfmac_f32_16x16x64_bf16 v[64:67], v[0:3], a[0:7], v52\n"
"v_min_i32 v55, v0, v2\n"
"v_smfmac_f32_16x16x64_bf16 v[68:71], v[0:3], a[144:151], v52\n"
"v_cmp_eq_u32 vcc, s42, v55\n"
"v_smfmac_f32_16x16x64_bf16 v[72:75], v[0:3], a[8:15], v52\n"
"buffer_load_dwordx4 v[36:39], v53, s[8:11], s62 offen offset:0 sc1\n"
"v_smfmac_f32_16x16x64_bf16 v[76:79], v[0:3], a[152:159], v52\n"
"s_cbranch_vccnz MK_CHmiss0\n"
"s_waitcnt vmcnt(9)\n"
"v_smfmac_f32_16x16x64_bf16 v[64:67], v[4:7], a[16:23], v52\n"
"v_min3_i32 v55, v55, v4, v6\n"
"v_smfmac_f32_16x16x64_bf16 v[68:71], v[4:7], a[160:167], v52\n"
"v_smfmac_f32_16x16x64_bf16 v[72:75], v[4:7], a[24:31], v52\n"
"buffer_load_dwordx4 v53, s[8:11], s63 offen offset:0 sc1 lds\n"
"v_smfmac_f32_16x16x64_bf16 v[76:79], v[4:7], a[168:175], v52\n"
"s_waitcnt vmcnt(9)\n"
"v_smfmac_f32_16x16x64_bf16 v[64:67], v[8:11], a[32:39], v52\n"
"v_min3_i32 v55, v55, v8, v10\n"
"v_smfmac_f32_16x16x64_bf16 v[68:71], v[8:11], v[44:51], v52\n"
"ds_read_b128 v[44:47], v53 offset:2048\n"
"ds_read_b128 v[48:51], v53 offset:3072\n"
"v_cmp_eq_u32 vcc, s42, v55\n"
"v_smfmac_f32_16x16x64_bf16 v[72:75], v[8:11], a[40:47], v52\n"
"s_waitcnt vmcnt(8)\n"
"v_smfmac_f32_16x16x64_bf16 v[64:67], v[12:15], a[48:55], v52\n"
"s_cbranch_vccnz MK_CHmiss2\n"
"v_min3_i32 v55, v55, v12, v14\n"
"v_smfmac_f32_16x16x64_bf16 v[68:71], v[12:15], a[176:183], v52\n"
"v_smfmac_f32_16x16x64_bf16 v[72:75], v[12:15], a[56:63], v52\n"
"buffer_load_dwordx4 v53, s[8:11], s63 offen offset:1024 sc1 lds\n"
"v_smfmac_f32_16x16x64_bf16 v[76:79], v[12:15], a[184:191], v52\n"
"s_waitcnt vmcnt(8)\n"
"v_smfmac_f32_16x16x64_bf16 v[64:67], v[16:19], a[64:71], v52\n"
"s_waitcnt lgkmcnt(0)\n"
"v_min3_i32 v55, v55, v16, v18\n"
"v_smfmac_f32_16x16x64_bf16 v[76:79], v[8:11], v[44:51], v52\n"
"ds_read_b128 v[44:47], v53 offset:16384\n"
"ds_read_b128 v[48:51], v53 offset:17408\n"
"v_smfmac_f32_16x16x64_bf16 v[68:71], v[16:19], a[192:199], v52\n"
"s_add_u32 m0, s71, 4096\n"
"v_smfmac_f32_16x16x64_bf16 v[72:75], v[16:19], a[72:79], v52\n"
"buffer_load_dword v60, s[8:11], s41 offen sc1 lds\n"
"v_smfmac_f32_16x16x64_bf16 v[76:79], v[16:19], a[200:207], v52\n"
"s_add_u32 m0, s70, s69\n"
"s_waitcnt vmcnt(8)\n"
"v_smfmac_f32_16x16x64_bf16 v[64:67], v[20:23], a[80:87], v52\n"
"v_min3_i32 v55, v55, v20, v22\n"
"v_smfmac_f32_16x16x64_bf16 v[72:75], v[20:23], a[88:95], v52\n"
"s_waitcnt vmcnt(7)\n"
"v_cmp_eq_u32 vcc, s42, v55\n"
"v_smfmac_f32_16x16x64_bf16 v[64:67], v[24:27], a[96:103], v52\n"
"v_min3_i32 v55, v55, v24, v26\n"
"v_smfmac_f32_16x16x64_bf16 v[68:71], v[24:27], a[208:215], v52\n"
"s_cbranch_vccnz MK_CHmiss5\n"
"v_smfmac_f32_16x16x64_bf16 v[72:75], v[24:27], a[104:111], v52\n"
"s_waitcnt lgkmcnt(0)\n"
"v_smfmac_f32_16x16x64_bf16 v[68:71], v[20:23], v[44:51], v52\n"
"ds_read_b128 v[44:47], v53 offset:18432\n"
"ds_read_b128 v[48:51], v53 offset:19456\n"
"v_smfmac_f32_16x16x64_bf16 v[76:79], v[24:27], a[216:223], v52\n"
"s_waitcnt vmcnt(6)\n"
"v_smfmac_f32_16x16x64_bf16 v[64:67], v[28:31], a[112:119], v52\n"
"v_min3_i32 v55, v55, v28, v30\n"
"v_smfmac_f32_16x16x64_bf16 v[68:71], v[28:31], a[224:231], v52\n"
"s_waitcnt vmcnt(5)\n"
"v_smfmac_f32_16x16x64_bf16 v[64:67], v[32:35], a[128:135], v52\n"
"v_min3_i32 v55, v55, v32, v34\n"
"v_smfmac_f32_16x16x64_bf16 v[68:71], v[32:35], a[240:247], v52\n"
"v_cmp_eq_u32 vcc, s42, v55\n"
"v_smfmac_f32_16x16x64_bf16 v[72:75], v[28:31], a[120:127], v52\n"
"v_smfmac_f32_16x16x64_bf16 v[76:79], v[28:31], a[232:239], v52\n"
"s_cbranch_vccnz MK_CHmiss8\n"
"v_and_or_b32 v55, v52, s32, v54\n"
"v_smfmac_f32_16x16x64_bf16 v[76:79], v[32:35], a[248:255], v52\n"
"s_nop 0\n"
"v_cvt_pk_bf16_f32 v60, v64, v65\n"
"v_smfmac_f32_16x16x64_bf16 v[72:75], v[32:35], a[136:143], v52\n"
"s_waitcnt lgkmcnt(0)\n"
"v_cvt_pk_bf16_f32 v61, v66, v67\n"
"v_smfmac_f32_16x16x64_bf16 v[76:79], v[20:23], v[44:51], v52\n"
"v_cvt_pk_bf16_f32 v62, v68, v69\n"
"v_cvt_pk_bf16_f32 v63, v70, v71\n"
"v_mov_b64 v[44:45], 0\n"
"v_mov_b64 v[46:47], 0\n"
"v_mfma_f32_16x16x32_bf16 v[64:67], v[60:63], v[56:59], 0\n"
"v_cvt_pk_bf16_f32 v68, v72, v73\n"
"v_cvt_pk_bf16_f32 v69, v74, v75\n"
"s_nop 0\n"
"v_cvt_pk_bf16_f32 v70, v76, v77\n"
"v_cvt_pk_bf16_f32 v71, v78, v79\n"
"v_mov_b64 v[48:49], 0\n"
"v_mov_b64 v[50:51], 0\n"
"v_mfma_f32_16x16x32_bf16 v[72:75], v[68:71], v[56:59], 0\n"
"v_cvt_pk_bf16_f32 v60, v64, v65\n"
"v_cvt_pk_bf16_f32 v61, v66, v67\n"
"v_smfmac_f32_16x16x64_bf16 v[44:47], v[0:3], v[80:87], v52\n"
"s_nop 1\n"
"v_smfmac_f32_16x16x64_bf16 v[48:51], v[0:3], v[88:95], v52\n"
"s_nop 1\n"
"v_cvt_pk_bf16_f32 v62, v72, v73\n"
"v_cvt_pk_bf16_f32 v63, v74, v75\n"
"v_and_or_b32 v72, v52, 8, v54\n"
"v_add_u32 v73, s70, v53\n"
"s_waitcnt vmcnt(4)\n"
"v_mfma_f32_16x16x32_bf16 v[76:79], v[40:43], v[60:63], 0\n"
"s_waitcnt vmcnt(3)\n"
"v_mfma_f32_16x16x32_bf16 v[68:71], v[36:39], v[60:63], 0\n"
"s_nop 1\n"
"v_mfma_f32_16x16x32_bf16 v[64:67], v[60:63], v[60:63], 0\n"
"s_nop 1\n"
"v_smfmac_f32_16x16x64_bf16 v[44:47], v[4:7], v[96:103], v52\n"
"s_nop 1\n"
"v_mov_b32_dpp v68, v76 row_ror:8 row_mask:0xf bank_mask:0xc\n"
"v_mov_b32_dpp v69, v77 row_ror:8 row_mask:0xf bank_mask:0xc\n"
"v_mov_b32_dpp v70, v78 row_ror:8 row_mask:0xf bank_mask:0xc\n"
"v_mov_b32_dpp v71, v79 row_ror:8 row_mask:0xf bank_mask:0xc\n"
"ds_write_b128 v55, v[68:71] offset:0\n"
"v_mov_b32_dpp v64, v66 row_ror:8 row_mask:0xf bank_mask:0xc\n"
"v_mov_b32_dpp v65, v67 row_ror:8 row_mask:0xf bank_mask:0xc\n"
"ds_write_b64 v72, v[64:65] offset:4096\n"
"v_smfmac_f32_16x16x64_bf16 v[48:51], v[4:7], v[104:111], v52\n"
"s_mov_b64 exec, s[54:55]\n"
"buffer_store_dwordx4 v[60:63], v54, s[8:11], s64 offen sc1\n"
"s_mov_b64 exec, -1\n"
"v_smfmac_f32_16x16x64_bf16 v[44:47], v[8:11], v[112:119], v52\n"
"s_waitcnt vmcnt(2)\n"
"v_subrev_u32 v62, s71, v72\n"
"v_smfmac_f32_16x16x64_bf16 v[48:51], v[8:11], v[120:127], v52\n"
"ds_read_b128 v[40:43], v73 offset:1024\n"
"ds_read_b128 v[36:39], v73\n"
"v_xor_b32 v76, 512, v55\n"
"v_xor_b32 v72, 1024, v55\n"
"v_xor_b32 v0, 1536, v55\n"
"s_waitcnt lgkmcnt(2)\n"
"s_barrier\n"
"ds_read_b128 v[76:79], v76 offset:0\n"
"ds_read_b128 v[72:75], v72 offset:0\n"
"v_or_b32 v62, 256, v62\n"
"v_smfmac_f32_16x16x64_bf16 v[44:47], v[12:15], v[128:135], v52\n"
"ds_read_b128 v[0:3], v0 offset:0\n"
"ds_read_b64 v[64:65], v62 offset:4096\n"
"v_lshl_or_b32 v55, v53, 2, s91\n"
"v_smfmac_f32_16x16x64_bf16 v[48:51], v[12:15], v[136:143], v52\n"
"ds_read_b64 v[66:67], v62 offset:4608\n"
"ds_read_b64 v[60:61], v62 offset:5120\n"
"ds_read_b64 v[62:63], v62 offset:5632\n"
"v_smfmac_f32_16x16x64_bf16 v[44:47], v[16:19], v[144:151], v52\n"
"v_smfmac_f32_16x16x64_bf16 v[48:51], v[16:19], v[152:159], v52\n"
"s_waitcnt lgkmcnt(7)\n"
"buffer_load_dwordx4 v55, s[8:11], s92 offen sc1 lds\n"
"v_add_u32 v55, 65536, v53\n"
"s_waitcnt lgkmcnt(4)\n"
"v_pk_add_f32 v[68:69], v[68:69], v[76:77]\n"
"v_pk_add_f32 v[70:71], v[70:71], v[78:79]\n"
"v_pk_add_f32 v[0:1], v[72:73], v[0:1]\n"
"v_pk_add_f32 v[2:3], v[74:75], v[2:3]\n"
"v_pk_add_f32 v[68:69], v[68:69], v[0:1]\n"
"v_pk_add_f32 v[70:71], v[70:71], v[2:3]\n"
"s_waitcnt lgkmcnt(0)\n"
"v_pk_add_f32 v[64:65], v[64:65], v[66:67]\n"
"v_pk_add_f32 v[60:61], v[60:61], v[62:63]\n"
"v_max3_f32 v66, v68, v69, v70\n"
"v_pk_add_f32 v[64:65], v[64:65], v[60:61]\n"
"v_max3_f32 v67, v71, v64, v65\n"
"v_max_f32 v66, v66, v67\n"
"v_smfmac_f32_16x16x64_bf16 v[44:47], v[20:23], v[160:167], v52\n"
"ds_read_b128 v[4:7], v55 offset:32768\n"
"ds_read_b128 v[8:11], v55 offset:33792\n"
"v_max_f32_dpp v66, v66, v66 row_ror:8 row_mask:0xf bank_mask:0xf\n"
"v_mov_b32 v67, v66\n"
"v_smfmac_f32_16x16x64_bf16 v[48:51], v[20:23], v[168:175], v52\n"
"ds_read_b128 v[12:15], v55 offset:34816\n"
"ds_read_b128 v[16:19], v55 offset:35840\n"
"v_permlane16_swap_b32 v66, v67\n"
"v_max_f32 v66, v66, v67\n"
"v_mov_b32 v67, v66\n"
"v_smfmac_f32_16x16x64_bf16 v[44:47], v[24:27], v[176:183], v52\n"
"s_nop 0\n"
"v_permlane32_swap_b32 v66, v67\n"
"v_max_f32 v66, v66, v67\n"
"v_mul_f32 v66, s60, v66\n"
"v_pk_fma_f32 v[68:69], v[68:69], s[60:61], v[66:67] op_sel_hi:[1,1,0] neg_lo:[0,0,1] neg_hi:[0,0,1]\n"
"v_pk_fma_f32 v[70:71], v[70:71], s[60:61], v[66:67] op_sel_hi:[1,1,0] neg_lo:[0,0,1] neg_hi:[0,0,1]\n"
"v_pk_fma_f32 v[64:65], v[64:65], s[60:61], v[66:67] op_sel_hi:[1,1,0] neg_lo:[0,0,1] neg_hi:[0,0,1]\n"
"s_waitcnt lgkmcnt(2)\n"
"v_smfmac_f32_16x16x64_bf16 v[48:51], v[24:27], v[184:191], v52\n"
"ds_read_b128 v[20:23], v55 offset:49152\n"
"ds_read_b128 v[24:27], v55 offset:50176\n"
"v_exp_f32 v68, v68\n"
"v_smfmac_f32_16x16x64_bf16 v[44:47], v[28:31], v[4:11], v52\n"
"ds_read_b128 v[4:7], v55 offset:51200\n"
"ds_read_b128 v[8:11], v55 offset:52224\n"
"v_exp_f32 v69, v69\n"
"s_waitcnt lgkmcnt(4)\n"
"v_smfmac_f32_16x16x64_bf16 v[48:51], v[28:31], v[12:19], v52\n"
"v_exp_f32 v70, v70\n"
"v_exp_f32 v71, v71\n"
"v_cvt_pk_bf16_f32 v72, v68, v69\n"
"v_cvt_pk_bf16_f32 v73, v70, v71\n"
"s_waitcnt lgkmcnt(2)\n"
"v_smfmac_f32_16x16x64_bf16 v[44:47], v[32:35], v[20:27], v52\n"
"v_mov_b32_dpp v74, v72 row_ror:8 row_mask:0xf bank_mask:0xf\n"
"v_mov_b32_dpp v75, v73 row_ror:8 row_mask:0xf bank_mask:0xf\n"
"s_waitcnt lgkmcnt(0)\n"
"v_smfmac_f32_16x16x64_bf16 v[48:51], v[32:35], v[4:11], v52\n"
"ds_read_b128 v[0:3], v53 offset:32768\n"
"ds_read_b128 v[4:7], v53 offset:33792\n"
"v_mov_b64 v[60:61], s[56:57]\n"
"v_mfma_f32_16x16x32_bf16 v[68:71], v[36:39], v[72:75], 0\n"
"ds_read_b128 v[8:11], v53 offset:34816\n"
"v_exp_f32 v64, v64\n"
"v_mfma_f32_16x16x32_bf16 v[32:35], v[40:43], v[72:75], 0\n"
"ds_read_b128 v[12:15], v53 offset:35840\n"
"v_exp_f32 v65, v65\n"
"v_mov_b64 v[62:63], s[56:57]\n"
"v_cvt_pk_bf16_f32 v76, v64, v65\n"
"v_cvt_pk_bf16_f32 v44, v44, v45\n"
"v_mfma_f32_16x16x32_bf16 v[64:67], v[60:63], v[72:75], 0\n"
"ds_read_b128 v[16:19], v53 offset:49152\n"
"v_mov_b32_dpp v77, v76 row_ror:8 row_mask:0xf bank_mask:0xf\n"
"v_mov_b64 v[78:79], s[58:59]\n"
"v_cvt_pk_bf16_f32 v45, v46, v47\n"
"v_cvt_pk_bf16_f32 v46, v48, v49\n"
"v_mfma_f32_16x16x16_bf16 v[64:67], v[78:79], v[76:77], v[64:67]\n"
"v_cvt_pk_bf16_f32 v47, v50, v51\n"
"ds_read_b128 v[20:23], v53 offset:50176\n"
"v_mfma_f32_16x16x16_bf16 v[68:71], v[44:45], v[76:77], v[68:71]\n"
"ds_read_b128 v[24:27], v53 offset:51200\n"
"s_cmp_eq_u32 s28, 63\n"
"v_mfma_f32_16x16x16_bf16 v[32:35], v[46:47], v[76:77], v[32:35]\n"
"ds_read_b128 v[28:31], v53 offset:52224\n"
"v_subrev_u32 v61, s71, v54\n"
"v_rcp_f32 v64, v64\n"
"v_mov_b64 v[36:37], 0\n"
"v_mov_b64 v[38:39], 0\n"
"s_mov_b64 exec, s[52:53]\n"
"v_pk_mul_f32 v[68:69], v[68:69], v[64:65] op_sel_hi:[1,0]\n"
"v_pk_mul_f32 v[70:71], v[70:71], v[64:65] op_sel_hi:[1,0]\n"
"v_pk_mul_f32 v[32:33], v[32:33], v[64:65] op_sel_hi:[1,0]\n"
"v_pk_mul_f32 v[34:35], v[34:35], v[64:65] op_sel_hi:[1,0]\n"
"v_cvt_pk_bf16_f32 v68, v68, v69\n"
"v_cvt_pk_bf16_f32 v69, v70, v71\n"
"v_cvt_pk_bf16_f32 v70, v32, v33\n"
"v_cvt_pk_bf16_f32 v71, v34, v35\n"
"ds_write_b128 v54, v[68:71] offset:6144\n"
"s_mov_b64 exec, -1\n"
"buffer_store_dwordx4 v[44:47], v53, s[8:11], s65 offen sc1\n"
"s_cbranch_scc1 MK_CHdone\n"
"v_mov_b64 v[40:41], 0\n"
"v_mov_b64 v[42:43], 0\n"
"v_mov_b64 v[48:49], 0\n"
"v_mov_b64 v[50:51], 0\n"
"s_cmp_lt_u32 s15, 2\n"
"s_waitcnt lgkmcnt(0)\n"
"s_barrier\n"
"ds_read_b128 v[64:67], v61 offset:6144\n"
"ds_read_b128 v[68:71], v61 offset:6656\n"
"ds_read_b128 v[72:75], v61 offset:7168\n"
"ds_read_b128 v[76:79], v61 offset:7680\n"
"s_cbranch_scc0 MK_CHwo4\n"
"v_mov_b64 v[44:45], 0\n"
"v_mov_b64 v[46:47], 0\n"
"v_mov_b64 v[32:33], 0\n"
"v_mov_b64 v[34:35], 0\n"
"s_waitcnt lgkmcnt(3)\n"
"v_smfmac_f32_16x16x64_bf16 v[36:39], v[64:67], v[0:7], v52\n"
"s_waitcnt lgkmcnt(2)\n"
"ds_read_b128 v[0:3], v55 offset:0\n"
"v_subrev_u32 v60, s69, v53\n"
"v_smfmac_f32_16x16x64_bf16 v[36:39], v[68:71], v[8:15], v52\n"
"s_waitcnt lgkmcnt(2)\n"
"ds_read_b128 v[4:7], v55 offset:1024\n"
"v_and_b32 v61, 512, v60\n"
"v_smfmac_f32_16x16x64_bf16 v[36:39], v[72:75], v[16:23], v52\n"
"s_waitcnt lgkmcnt(2)\n"
"ds_read_b128 v[8:11], v55 offset:2048\n"
"v_bfe_u32 v60, v60, 1, 8\n"
"v_smfmac_f32_16x16x64_bf16 v[36:39], v[76:79], v[24:31], v52\n"
"ds_read_b128 v[12:15], v55 offset:3072\n"
"ds_read_b128 v[16:19], v55 offset:16384\n"
"v_lshl_or_b32 v60, v61, 5, v60\n"
"v_smfmac_f32_16x16x64_bf16 v[40:43], v[64:67], v[192:199], v52\n"
"ds_read_b128 v[20:23], v55 offset:17408\n"
"ds_read_b128 v[24:27], v55 offset:18432\n"
"v_add_u32 v63, s69, v55\n"
"v_smfmac_f32_16x16x64_bf16 v[40:43], v[68:71], v[200:207], v52\n"
"ds_read_b128 v[28:31], v55 offset:19456\n"
"v_smfmac_f32_16x16x64_bf16 v[40:43], v[72:75], v[208:215], v52\n"
"s_nop 0\n"
"v_cvt_pk_bf16_f32 v36, v36, v37\n"
"v_smfmac_f32_16x16x64_bf16 v[40:43], v[76:79], v[216:223], v52\n"
"s_waitcnt lgkmcnt(6)\n"
"v_cvt_pk_bf16_f32 v37, v38, v39\n"
"v_smfmac_f32_16x16x64_bf16 v[48:51], v[64:67], v[0:7], v52\n"
"s_waitcnt lgkmcnt(4)\n"
"ds_read_b128 v[0:3], v63 offset:49152\n"
"v_and_or_b32 v36, v36, -2, s87\n"
"v_smfmac_f32_16x16x64_bf16 v[48:51], v[68:71], v[8:15], v52\n"
"buffer_store_dwordx2 v[36:37], v60, s[8:11], s82 offen sc1\n"
"s_waitcnt lgkmcnt(3)\n"
"v_smfmac_f32_16x16x64_bf16 v[48:51], v[72:75], v[16:23], v52\n"
"s_waitcnt lgkmcnt(1)\n"
"ds_read_b128 v[4:7], v63 offset:50176\n"
"v_cvt_pk_bf16_f32 v40, v40, v41\n"
"v_smfmac_f32_16x16x64_bf16 v[48:51], v[76:79], v[24:31], v52\n"
"ds_read_b128 v[8:11], v63 offset:51200\n"
"ds_read_b128 v[12:15], v63 offset:52224\n"
"v_cvt_pk_bf16_f32 v41, v42, v43\n"
"v_smfmac_f32_16x16x64_bf16 v[44:47], v[64:67], v[224:231], v52\n"
"ds_read_b128 v[16:19], v63 offset:53248\n"
"ds_read_b128 v[20:23], v63 offset:54272\n"
"v_and_or_b32 v40, v40, -2, s87\n"
"v_smfmac_f32_16x16x64_bf16 v[44:47], v[68:71], v[232:239], v52\n"
"buffer_store_dwordx2 v[40:41], v60, s[8:11], s83 offen sc1\n"
"ds_read_b128 v[24:27], v63 offset:55296\n"
"v_smfmac_f32_16x16x64_bf16 v[44:47], v[72:75], v[240:247], v52\n"
"ds_read_b128 v[28:31], v63 offset:56320\n"
"v_cvt_pk_bf16_f32 v48, v48, v49\n"
"v_smfmac_f32_16x16x64_bf16 v[44:47], v[76:79], v[248:255], v52\n"
"s_waitcnt lgkmcnt(6)\n"
"v_cvt_pk_bf16_f32 v49, v50, v51\n"
"v_smfmac_f32_16x16x64_bf16 v[32:35], v[64:67], v[0:7], v52\n"
"s_waitcnt lgkmcnt(4)\n"
"v_and_or_b32 v48, v48, -2, s87\n"
"v_smfmac_f32_16x16x64_bf16 v[32:35], v[68:71], v[8:15], v52\n"
"buffer_store_dwordx2 v[48:49], v60, s[8:11], s84 offen sc1\n"
"s_waitcnt lgkmcnt(2)\n"
"v_smfmac_f32_16x16x64_bf16 v[32:35], v[72:75], v[16:23], v52\n"
"s_waitcnt lgkmcnt(0)\n"
"v_cvt_pk_bf16_f32 v44, v44, v45\n"
"v_smfmac_f32_16x16x64_bf16 v[32:35], v[76:79], v[24:31], v52\n"
"v_cvt_pk_bf16_f32 v45, v46, v47\n"
"v_and_or_b32 v44, v44, -2, s87\n"
"buffer_store_dwordx2 v[44:45], v60, s[8:11], s85 offen sc1\n"
"s_nop 4\n"
"v_cvt_pk_bf16_f32 v32, v32, v33\n"
"v_cvt_pk_bf16_f32 v33, v34, v35\n"
"v_and_or_b32 v32, v32, -2, s87\n"
"buffer_store_dwordx2 v[32:33], v60, s[8:11], s86 offen sc1\n"
"s_memrealtime s[18:19]\n"
"s_waitcnt vmcnt(5)\n"
"v_mov_b32 v79, s28\n"
"s_mov_b64 exec, 1\n"
"buffer_store_dword v79, off, s[8:11], s39 sc1\n"
"s_mov_b64 exec, -1\n"
"s_cmp_eq_u32 s15, 0\n"
"s_cbranch_scc0 MK_CHnov\n"
"s_mov_b64 exec, 1\n"
"s_waitcnt lgkmcnt(0)\n"
"v_not_b32 v67, s18\n"
"buffer_store_dword v67, off, s[8:11], s93 offset:2048 sc1\n"
"s_mov_b64 exec, -1\n"
"MK_CHnov:\n"
"s_barrier\n"
"s_branch MK_CHnored\n"
"MK_CHwo4:\n"
"v_mov_b64 v[44:45], 0\n"
"v_mov_b64 v[46:47], 0\n"
"s_waitcnt vmcnt(1)\n"
"s_waitcnt lgkmcnt(3)\n"
"v_smfmac_f32_16x16x64_bf16 v[36:39], v[64:67], v[0:7], v52\n"
"s_waitcnt lgkmcnt(2)\n"
"ds_read_b128 v[0:3], v55 offset:0\n"
"v_subrev_u32 v60, s69, v53\n"
"v_add_u32 v63, s70, v53\n"
"v_smfmac_f32_16x16x64_bf16 v[36:39], v[68:71], v[8:15], v52\n"
"s_waitcnt lgkmcnt(2)\n"
"ds_read_b128 v[4:7], v55 offset:1024\n"
"v_and_b32 v61, 512, v60\n"
"ds_read_b128 v[32:35], v63\n"
"v_smfmac_f32_16x16x64_bf16 v[36:39], v[72:75], v[16:23], v52\n"
"s_waitcnt lgkmcnt(3)\n"
"ds_read_b128 v[8:11], v55 offset:2048\n"
"v_bfe_u32 v60, v60, 1, 8\n"
"v_smfmac_f32_16x16x64_bf16 v[36:39], v[76:79], v[24:31], v52\n"
"ds_read_b128 v[12:15], v55 offset:3072\n"
"ds_read_b128 v[16:19], v55 offset:16384\n"
"v_lshl_or_b32 v60, v61, 5, v60\n"
"v_smfmac_f32_16x16x64_bf16 v[40:43], v[64:67], v[192:199], v52\n"
"ds_read_b128 v[20:23], v55 offset:17408\n"
"ds_read_b128 v[24:27], v55 offset:18432\n"
"v_smfmac_f32_16x16x64_bf16 v[40:43], v[68:71], v[200:207], v52\n"
"ds_read_b128 v[28:31], v55 offset:19456\n"
"v_smfmac_f32_16x16x64_bf16 v[40:43], v[72:75], v[208:215], v52\n"
"s_nop 0\n"
"v_cvt_pk_bf16_f32 v36, v36, v37\n"
"s_waitcnt lgkmcnt(6)\n"
"v_min_u32 v32, v32, v34\n"
"v_smfmac_f32_16x16x64_bf16 v[40:43], v[76:79], v[216:223], v52\n"
"s_nop 0\n"
"v_cvt_pk_bf16_f32 v37, v38, v39\n"
"v_min_u32_dpp v32, v32, v32 row_ror:1 row_mask:0xf bank_mask:0xf\n"
"v_smfmac_f32_16x16x64_bf16 v[48:51], v[64:67], v[0:7], v52\n"
"s_waitcnt lgkmcnt(4)\n"
"v_and_or_b32 v36, v36, -2, s87\n"
"v_min_u32_dpp v32, v32, v32 row_ror:2 row_mask:0xf bank_mask:0xf\n"
"v_smfmac_f32_16x16x64_bf16 v[48:51], v[68:71], v[8:15], v52\n"
"buffer_store_dwordx2 v[36:37], v60, s[8:11], s82 offen sc1\n"
"s_waitcnt lgkmcnt(2)\n"
"v_min_u32_dpp v32, v32, v32 row_ror:4 row_mask:0xf bank_mask:0xf\n"
"v_smfmac_f32_16x16x64_bf16 v[48:51], v[72:75], v[16:23], v52\n"
"s_waitcnt lgkmcnt(0)\n"
"v_cvt_pk_bf16_f32 v40, v40, v41\n"
"v_min_u32_dpp v32, v32, v32 row_ror:8 row_mask:0xf bank_mask:0xf\n"
"v_smfmac_f32_16x16x64_bf16 v[48:51], v[76:79], v[24:31], v52\n"
"s_nop 0\n"
"v_cvt_pk_bf16_f32 v41, v42, v43\n"
"v_readlane_b32 s99, v32, 0\n"
"v_readlane_b32 s24, v32, 16\n"
"v_smfmac_f32_16x16x64_bf16 v[44:47], v[64:67], v[224:231], v52\n"
"s_nop 0\n"
"v_and_or_b32 v40, v40, -2, s87\n"
"v_smfmac_f32_16x16x64_bf16 v[44:47], v[68:71], v[232:239], v52\n"
"buffer_store_dwordx2 v[40:41], v60, s[8:11], s83 offen sc1\n"
"s_min_u32 s99, s99, s24\n"
"s_not_b32 s99, s99\n"
"v_smfmac_f32_16x16x64_bf16 v[44:47], v[72:75], v[240:247], v52\n"
"s_nop 0\n"
"v_cvt_pk_bf16_f32 v48, v48, v49\n"
"s_add_u32 s98, s99, s38\n"
"s_sub_u32 s40, s99, s95\n"
"v_smfmac_f32_16x16x64_bf16 v[44:47], v[76:79], v[248:255], v52\n"
"v_cvt_pk_bf16_f32 v49, v50, v51\n"
"s_add_u32 s95, s98, s100\n"
"v_and_or_b32 v48, v48, -2, s87\n"
"buffer_store_dwordx2 v[48:49], v60, s[8:11], s84 offen sc1\n"
"s_add_u32 s25, s98, s36\n"
"s_nop 2\n"
"v_cvt_pk_bf16_f32 v44, v44, v45\n"
"v_cvt_pk_bf16_f32 v45, v46, v47\n"
"v_and_or_b32 v44, v44, -2, s87\n"
"buffer_store_dwordx2 v[44:45], v60, s[8:11], s85 offen sc1\n"
"v_mov_b32 v76, s31\n"
"v_mov_b32 v77, s95\n"
"ds_write_b32 v76, v77\n"
"s_waitcnt vmcnt(4)\n"
"v_mov_b32 v79, s28\n"
"s_mov_b64 exec, 1\n"
"buffer_store_dword v79, off, s[8:11], s39 sc1\n"
"s_mov_b64 exec, -1\n"
"s_cmp_eq_u32 s90, 0\n"
"s_waitcnt lgkmcnt(0)\n"
"s_barrier\n"
"s_cbranch_scc0 MK_CHisred\n"
"MK_CHnored:\n"
"v_mov_b32 v76, 4096\n"
"ds_read_b32 v76, v76\n"
"ds_read_b128 v[44:47], v53 offset:0\n"
"ds_read_b128 v[48:51], v53 offset:1024\n"
"s_add_u32 s75, s79, s80\n"
"s_add_u32 s76, s75, 4096\n"
"s_add_u32 s77, s79, s81\n"
"s_waitcnt lgkmcnt(0)\n"
"v_readfirstlane_b32 s26, v76\n"
"MK_CHdBc:\n"
"s_memrealtime s[18:19]\n"
"s_waitcnt lgkmcnt(0)\n"
"s_sub_u32 s24, s26, s18\n"
"s_sub_u32 s27, s24, 6\n"
"s_cmp_lt_u32 s27, 1000\n"
"s_cbranch_scc1 MK_CHdBc\n"
"s_sub_u32 s24, s24, 3\n"
"s_cmp_lt_u32 s24, 3\n"
"s_cbranch_scc0 MK_CHdBe\n"
"MK_CHdB:\n"
"s_sub_u32 s24, s24, 1\n"
"s_cbranch_scc0 MK_CHdB\n"
"MK_CHdBe:\n"
"s_branch MK_CHbarB\n"
"MK_CHisred:\n"
"MK_CHdAc:\n"
"s_memrealtime s[18:19]\n"
"s_waitcnt lgkmcnt(0)\n"
"s_sub_u32 s24, s25, s18\n"
"s_sub_u32 s27, s24, 6\n"
"s_cmp_lt_u32 s27, 1000\n"
"s_cbranch_scc1 MK_CHdAc\n"
"s_sub_u32 s24, s24, 3\n"
"s_cmp_lt_u32 s24, 3\n"
"s_cbranch_scc0 MK_CHdAe\n"
"MK_CHdA:\n"
"s_sub_u32 s24, s24, 1\n"
"s_cbranch_scc0 MK_CHdA\n"
"MK_CHdAe:\n"
"buffer_load_dwordx4 v[0:3], v53, s[8:11], s72 offen offset:0 sc1\n"
"buffer_load_dwordx4 v[4:7], v53, s[8:11], s72 offen offset:1024 sc1\n"
"buffer_load_dwordx4 v[8:11], v53, s[8:11], s72 offen offset:2048 sc1\n"
"buffer_load_dwordx4 v[12:15], v53, s[8:11], s72 offen offset:3072 sc1\n"
"buffer_load_dwordx4 v[16:19], v53, s[8:11], s73 offen offset:0 sc1\n"
"buffer_load_dwordx4 v[20:23], v53, s[8:11], s73 offen offset:1024 sc1\n"
"buffer_load_dwordx4 v[24:27], v53, s[8:11], s73 offen offset:2048 sc1\n"
"buffer_load_dwordx4 v[28:31], v53, s[8:11], s73 offen offset:3072 sc1\n"
"buffer_load_dword v62, off, s[8:11], s12 sc1\n"
"ds_read_b128 v[44:47], v53 offset:0\n"
"ds_read_b128 v[48:51], v53 offset:1024\n"
"v_subrev_u32 v73, s69, v53\n"
"v_min_u32 v33, v33, v35\n"
"v_and_b32 v74, 16, v73\n"
"v_mov_b32 v75, 16256\n"
"v_min_u32_dpp v33, v33, v33 row_ror:1 row_mask:0xf bank_mask:0xf\n"
"v_lshlrev_b32 v74, v74, v75\n"
"v_cndmask_b32 v64, 0, v74, s[44:45]\n"
"v_min_u32_dpp v33, v33, v33 row_ror:2 row_mask:0xf bank_mask:0xf\n"
"v_cndmask_b32 v65, 0, v74, s[46:47]\n"
"v_cndmask_b32 v66, 0, v74, s[48:49]\n"
"v_min_u32_dpp v33, v33, v33 row_ror:4 row_mask:0xf bank_mask:0xf\n"
"v_cndmask_b32 v67, 0, v74, s[50:51]\n"
"v_and_b32 v72, 48, v73\n"
"v_min_u32_dpp v33, v33, v33 row_ror:8 row_mask:0xf bank_mask:0xf\n"
"v_and_b32 v74, 64, v73\n"
"v_lshl_or_b32 v72, v74, 1, v72\n"
"v_bfe_u32 v74, v73, 9, 1\n"
"v_lshl_or_b32 v72, v74, 6, v72\n"
"v_bfe_u32 v74, v73, 8, 1\n"
"v_lshl_or_b32 v72, v74, 3, v72\n"
"v_readlane_b32 s101, v32, 32\n"
"v_readlane_b32 s25, v32, 48\n"
"v_readlane_b32 s13, v33, 0\n"
"v_readlane_b32 s24, v33, 16\n"
"s_min_u32 s101, s101, s25\n"
"s_min_u32 s13, s13, s24\n"
"s_not_b32 s101, s101\n"
"s_not_b32 s13, s13\n"
"s_sub_u32 s24, s101, s96\n"
"s_lshr_b32 s25, s6, 2\n"
"s_sub_u32 s24, s24, s25\n"
"s_max_i32 s24, s24, -6\n"
"s_min_i32 s24, s24, 8\n"
"s_cmp_le_i32 s5, 8\n"
"s_cselect_b32 s24, s24, 4\n"
"s_add_u32 s6, s6, s24\n"
"s_sub_u32 s24, s13, s97\n"
"s_lshr_b32 s25, s7, 2\n"
"s_sub_u32 s24, s24, s25\n"
"s_max_i32 s24, s24, -6\n"
"s_min_i32 s24, s24, 8\n"
"s_sub_u32 s25, s97, s101\n"
"s_cmp_ge_i32 s25, 2\n"
"s_cselect_b32 s24, s24, 4\n"
"s_add_u32 s7, s7, s24\n"
"s_mov_b32 s5, s40\n"
"s_mov_b32 s96, s99\n"
"s_mov_b32 s97, s98\n"
"s_lshr_b32 s38, s6, 2\n"
"s_add_u32 s38, s38, s20\n"
"s_lshr_b32 s100, s7, 2\n"
"s_add_u32 s100, s100, s21\n"
"s_add_u32 s75, s79, s80\n"
"s_add_u32 s76, s75, 4096\n"
"s_add_u32 s77, s79, s81\n"
"s_add_u32 s41, s74, 4194304\n"
"s_add_u32 s26, s95, s37\n"
"s_sub_u32 s26, s26, 0\n"
"v_not_b32 v76, s95\n"
"s_mov_b32 s29, s30\n"
"s_waitcnt lgkmcnt(0)\n"
"MK_CHred_sum:\n"
"s_cmp_eq_u32 s87, 1\n"
"s_cbranch_scc1 MK_CHred_odd\n"
"s_waitcnt vmcnt(8)\n"
"v_mfma_f32_16x16x32_bf16 v[68:71], v[0:3], v[64:67], 0\n"
"v_or_b32 v55, v0, v2\n"
"s_waitcnt vmcnt(7)\n"
"v_mfma_f32_16x16x32_bf16 v[68:71], v[4:7], v[64:67], v[68:71]\n"
"v_or3_b32 v55, v55, v4, v6\n"
"s_waitcnt vmcnt(6)\n"
"v_mfma_f32_16x16x32_bf16 v[68:71], v[8:11], v[64:67], v[68:71]\n"
"v_or3_b32 v55, v55, v8, v10\n"
"s_waitcnt vmcnt(5)\n"
"v_mfma_f32_16x16x32_bf16 v[68:71], v[12:15], v[64:67], v[68:71]\n"
"v_or3_b32 v55, v55, v12, v14\n"
"s_waitcnt vmcnt(4)\n"
"v_mfma_f32_16x16x32_bf16 v[68:71], v[16:19], v[64:67], v[68:71]\n"
"v_or3_b32 v55, v55, v16, v18\n"
"s_waitcnt vmcnt(3)\n"
"v_mfma_f32_16x16x32_bf16 v[68:71], v[20:23], v[64:67], v[68:71]\n"
"v_or3_b32 v55, v55, v20, v22\n"
"s_waitcnt vmcnt(2)\n"
"v_mfma_f32_16x16x32_bf16 v[68:71], v[24:27], v[64:67], v[68:71]\n"
"v_or3_b32 v55, v55, v24, v26\n"
"s_waitcnt vmcnt(1)\n"
"s_memrealtime s[18:19]\n"
"v_mfma_f32_16x16x32_bf16 v[68:71], v[28:31], v[64:67], v[68:71]\n"
"v_or3_b32 v55, v55, v28, v30\n"
"v_and_b32 v55, 1, v55\n"
"v_cmp_ne_u32 vcc, s87, v55\n"
"s_waitcnt vmcnt(0)\n"
"v_readfirstlane_b32 s16, v62\n"
"s_mov_b64 exec, s[52:53]\n"
"s_branch MK_CHred_chk\n"
"MK_CHred_odd:\n"
"s_waitcnt vmcnt(8)\n"
"v_mfma_f32_16x16x32_bf16 v[68:71], v[0:3], v[64:67], 0\n"
"v_and_b32 v55, v0, v2\n"
"s_waitcnt vmcnt(7)\n"
"v_mfma_f32_16x16x32_bf16 v[68:71], v[4:7], v[64:67], v[68:71]\n"
"v_bitop3_b32 v55, v55, v4, v6 bitop3:0x80\n"
"s_waitcnt vmcnt(6)\n"
"v_mfma_f32_16x16x32_bf16 v[68:71], v[8:11], v[64:67], v[68:71]\n"
"v_bitop3_b32 v55, v55, v8, v10 bitop3:0x80\n"
"s_waitcnt vmcnt(5)\n"
"v_mfma_f32_16x16x32_bf16 v[68:71], v[12:15], v[64:67], v[68:71]\n"
"v_bitop3_b32 v55, v55, v12, v14 bitop3:0x80\n"
"s_waitcnt vmcnt(4)\n"
"v_mfma_f32_16x16x32_bf16 v[68:71], v[16:19], v[64:67], v[68:71]\n"
"v_bitop3_b32 v55, v55, v16, v18 bitop3:0x80\n"
"s_waitcnt vmcnt(3)\n"
"v_mfma_f32_16x16x32_bf16 v[68:71], v[20:23], v[64:67], v[68:71]\n"
"v_bitop3_b32 v55, v55, v20, v22 bitop3:0x80\n"
"s_waitcnt vmcnt(2)\n"
"v_mfma_f32_16x16x32_bf16 v[68:71], v[24:27], v[64:67], v[68:71]\n"
"v_bitop3_b32 v55, v55, v24, v26 bitop3:0x80\n"
"s_waitcnt vmcnt(1)\n"
"s_memrealtime s[18:19]\n"
"v_mfma_f32_16x16x32_bf16 v[68:71], v[28:31], v[64:67], v[68:71]\n"
"v_bitop3_b32 v55, v55, v28, v30 bitop3:0x80\n"
"v_and_b32 v55, 1, v55\n"
"v_cmp_ne_u32 vcc, s87, v55\n"
"s_waitcnt vmcnt(0)\n"
"v_readfirstlane_b32 s16, v62\n"
"s_mov_b64 exec, s[52:53]\n"
"MK_CHred_chk:\n"
"s_nop 1\n"
"v_cvt_pk_bf16_f32 v68, v68, v69\n"
"v_cvt_pk_bf16_f32 v69, v70, v71\n"
"v_max_i32 v68, s43, v68\n"
"s_cbranch_vccnz MK_CHretryA\n"
"MK_CHred_go:\n"
"buffer_store_dwordx2 v[68:69], v72, s[8:11], s74 offen sc1\n"
"buffer_store_dwordx2 v[68:69], v72, s[8:11], s41 offen sc1\n"
"s_mov_b64 exec, 1\n"
"s_waitcnt lgkmcnt(0)\n"
"s_sub_u32 s27, s18, s37\n"
"v_not_b32 v77, s27\n"
"s_sub_u32 s25, s26, s18\n"
"s_cmp_lt_i32 s25, 0\n"
"s_cbranch_scc1 MK_CHlate\n"
"buffer_store_dwordx2 v[76:77], off, s[8:11], s93 sc1\n"
"s_mov_b64 exec, -1\n"
"s_cmp_gt_i32 s16, s28\n"
"s_cbranch_scc1 MK_CHbarB\n"
"MK_CHkvw:\n"
"s_cmp_gt_i32 s16, s28\n"
"s_cbranch_scc1 MK_CHw2x\n"
"s_mov_b32 s29, s30\n"
"MK_CHw2:\n"
"buffer_load_dword v63, off, s[8:11], s12 sc1\n"
"s_add_u32 s29, s29, 1\n"
"s_waitcnt vmcnt(0)\n"
"v_readfirstlane_b32 s16, v63\n"
"s_cmp_gt_i32 s16, s28\n"
"s_cbranch_scc1 MK_CHw2x\n"
"s_cmp_lt_u32 s29, 131072\n"
"s_cbranch_scc1 MK_CHw2\n"
"s_mov_b32 s30, 131072\n"
"MK_CHw2x:\n"
"s_branch MK_CHbarB\n"
"MK_CHlate:\n"
"s_add_u32 s24, s27, s22\n"
"v_not_b32 v76, s24\n"
"s_add_u32 s26, s27, s23\n"
"buffer_store_dwordx2 v[76:77], off, s[8:11], s93 sc1\n"
"s_mov_b64 exec, -1\n"
"MK_CHdLc:\n"
"s_memrealtime s[18:19]\n"
"s_waitcnt lgkmcnt(0)\n"
"s_sub_u32 s24, s26, s18\n"
"s_sub_u32 s27, s24, 6\n"
"s_cmp_lt_u32 s27, 1000\n"
"s_cbranch_scc1 MK_CHdLc\n"
"s_sub_u32 s24, s24, 3\n"
"s_cmp_lt_u32 s24, 3\n"
"s_cbranch_scc0 MK_CHdLe\n"
"MK_CHdL:\n"
"s_sub_u32 s24, s24, 1\n"
"s_cbranch_scc0 MK_CHdL\n"
"MK_CHdLe:\n"
"s_branch MK_CHkvw\n"
"MK_CHretryA:\n"
"s_add_u32 s33, s33, 1\n"
"s_add_u32 s29, s29, 1\n"
"s_cmp_lt_u32 s29, 131072\n"
"s_cselect_b32 s30, s30, 131072\n"
"s_cbranch_scc0 MK_CHred_go\n"
"s_mov_b64 exec, -1\n"
"s_waitcnt lgkmcnt(0)\n"
"buffer_load_dwordx4 v[0:3], v53, s[8:11], s72 offen offset:0 sc1\n"
"buffer_load_dwordx4 v[4:7], v53, s[8:11], s72 offen offset:1024 sc1\n"
"buffer_load_dwordx4 v[8:11], v53, s[8:11], s72 offen offset:2048 sc1\n"
"buffer_load_dwordx4 v[12:15], v53, s[8:11], s72 offen offset:3072 sc1\n"
"buffer_load_dwordx4 v[16:19], v53, s[8:11], s73 offen offset:0 sc1\n"
"buffer_load_dwordx4 v[20:23], v53, s[8:11], s73 offen offset:1024 sc1\n"
"buffer_load_dwordx4 v[24:27], v53, s[8:11], s73 offen offset:2048 sc1\n"
"buffer_load_dwordx4 v[28:31], v53, s[8:11], s73 offen offset:3072 sc1\n"
"s_waitcnt vmcnt(0)\n"
"s_branch MK_CHred_sum\n"
"MK_CHmiss0:\n"
"s_add_u32 s75, s75, 4194304\n"
"s_add_u32 s76, s76, 4194304\n"
"s_add_u32 s77, s77, 4194304\n"
"buffer_load_dwordx4 v[0:3], v53, s[8:11], s75 offen offset:0 sc1\n"
"s_waitcnt vmcnt(10)\n"
"buffer_load_dwordx4 v[4:7], v53, s[8:11], s75 offen offset:1024 sc1\n"
"s_branch MK_CHredo\n"
"MK_CHmiss2:\n"
"s_add_u32 s75, s75, 4194304\n"
"s_add_u32 s76, s76, 4194304\n"
"s_add_u32 s77, s77, 4194304\n"
"buffer_load_dwordx4 v[4:7], v53, s[8:11], s75 offen offset:1024 sc1\n"
"buffer_load_dwordx4 v[8:11], v53, s[8:11], s75 offen offset:2048 sc1\n"
"buffer_load_dwordx4 v[12:15], v53, s[8:11], s75 offen offset:3072 sc1\n"
"s_branch MK_CHredo\n"
"MK_CHmiss5:\n"
"s_add_u32 s75, s75, 4194304\n"
"s_add_u32 s76, s76, 4194304\n"
"s_add_u32 s77, s77, 4194304\n"
"buffer_load_dwordx4 v[12:15], v53, s[8:11], s75 offen offset:3072 sc1\n"
"buffer_load_dwordx4 v[16:19], v53, s[8:11], s76 offen offset:0 sc1\n"
"buffer_load_dwordx4 v[20:23], v53, s[8:11], s76 offen offset:1024 sc1\n"
"buffer_load_dwordx4 v[24:27], v53, s[8:11], s76 offen offset:2048 sc1\n"
"s_branch MK_CHredo\n"
"MK_CHmiss8:\n"
"s_add_u32 s75, s75, 4194304\n"
"s_add_u32 s76, s76, 4194304\n"
"s_add_u32 s77, s77, 4194304\n"
"buffer_load_dwordx4 v[24:27], v53, s[8:11], s76 offen offset:2048 sc1\n"
"buffer_load_dwordx4 v[28:31], v53, s[8:11], s76 offen offset:3072 sc1\n"
"buffer_load_dwordx4 v[32:35], v53, s[8:11], s77 offen offset:0 sc1\n"
"s_branch MK_CHredo\n"
"MK_CHredo:\n"
"s_add_u32 m0, s70, s69\n"
"s_mov_b32 s29, s30\n"
"s_waitcnt lgkmcnt(1)\n"
"ds_read_b128 v[44:47], v53 offset:0\n"
"s_waitcnt lgkmcnt(1)\n"
"ds_read_b128 v[48:51], v53 offset:1024\n"
"v_mov_b64 v[64:65], 0\n"
"v_mov_b64 v[66:67], 0\n"
"v_mov_b64 v[68:69], 0\n"
"v_mov_b64 v[70:71], 0\n"
"v_mov_b64 v[72:73], 0\n"
"v_mov_b64 v[74:75], 0\n"
"v_mov_b64 v[76:77], 0\n"
"v_mov_b64 v[78:79], 0\n"
"v_readfirstlane_b32 s27, v55\n"
"s_memrealtime s[18:19]\n"
"s_waitcnt lgkmcnt(0)\n"
"s_sub_u32 s24, s18, 6\n"
"s_sub_u32 s25, s93, s94\n"
"s_or_b32 s25, s25, 8\n"
"s_mov_b64 exec, 1\n"
"v_not_b32 v55, s24\n"
"buffer_store_dword v55, off, s[8:11], s25 sc1\n"
"v_mov_b32 v55, s27\n"
"s_mov_b64 exec, -1\n"
"s_waitcnt vmcnt(1) lgkmcnt(0)\n"
"MK_CHagain:\n"
"s_add_u32 s29, s29, 1\n"
"v_min_i32 v55, v0, v2\n"
"v_min3_i32 v55, v55, v4, v6\n"
"v_min3_i32 v55, v55, v8, v10\n"
"v_min3_i32 v55, v55, v12, v14\n"
"v_min3_i32 v55, v55, v16, v18\n"
"v_min3_i32 v55, v55, v20, v22\n"
"v_min3_i32 v55, v55, v24, v26\n"
"v_min3_i32 v55, v55, v28, v30\n"
"v_min3_i32 v55, v55, v32, v34\n"
"v_cmp_eq_u32 vcc, s42, v55\n"
"s_cbranch_vccz MK_CHstep\n"
"v_min_i32 v55, v0, v2\n"
"v_cmp_eq_u32 vcc, s42, v55\n"
"s_cbranch_vccz MK_CHhave0\n"
"buffer_load_dwordx4 v[0:3], v53, s[8:11], s75 offen offset:0 sc1\n"
"MK_CHhave0:\n"
"v_min_i32 v55, v4, v6\n"
"v_cmp_eq_u32 vcc, s42, v55\n"
"s_cbranch_vccz MK_CHhave1\n"
"buffer_load_dwordx4 v[4:7], v53, s[8:11], s75 offen offset:1024 sc1\n"
"MK_CHhave1:\n"
"v_min_i32 v55, v8, v10\n"
"v_cmp_eq_u32 vcc, s42, v55\n"
"s_cbranch_vccz MK_CHhave2\n"
"buffer_load_dwordx4 v[8:11], v53, s[8:11], s75 offen offset:2048 sc1\n"
"MK_CHhave2:\n"
"v_min_i32 v55, v12, v14\n"
"v_cmp_eq_u32 vcc, s42, v55\n"
"s_cbranch_vccz MK_CHhave3\n"
"buffer_load_dwordx4 v[12:15], v53, s[8:11], s75 offen offset:3072 sc1\n"
"MK_CHhave3:\n"
"v_min_i32 v55, v16, v18\n"
"v_cmp_eq_u32 vcc, s42, v55\n"
"s_cbranch_vccz MK_CHhave4\n"
"buffer_load_dwordx4 v[16:19], v53, s[8:11], s76 offen offset:0 sc1\n"
"MK_CHhave4:\n"
"v_min_i32 v55, v20, v22\n"
"v_cmp_eq_u32 vcc, s42, v55\n"
"s_cbranch_vccz MK_CHhave5\n"
"buffer_load_dwordx4 v[20:23], v53, s[8:11], s76 offen offset:1024 sc1\n"
"MK_CHhave5:\n"
"v_min_i32 v55, v24, v26\n"
"v_cmp_eq_u32 vcc, s42, v55\n"
"s_cbranch_vccz MK_CHhave6\n"
"buffer_load_dwordx4 v[24:27], v53, s[8:11], s76 offen offset:2048 sc1\n"
"MK_CHhave6:\n"
"v_min_i32 v55, v28, v30\n"
"v_cmp_eq_u32 vcc, s42, v55\n"
"s_cbranch_vccz MK_CHhave7\n"
"buffer_load_dwordx4 v[28:31], v53, s[8:11], s76 offen offset:3072 sc1\n"
"MK_CHhave7:\n"
"v_min_i32 v55, v32, v34\n"
"v_cmp_eq_u32 vcc, s42, v55\n"
"s_cbranch_vccz MK_CHhave8\n"
"buffer_load_dwordx4 v[32:35], v53, s[8:11], s77 offen offset:0 sc1\n"
"MK_CHhave8:\n"
"s_waitcnt vmcnt(0)\n"
"s_cmp_lt_u32 s29, 131072\n"
"s_cbranch_scc1 MK_CHagain\n"
"s_endpgm\n"
"MK_CHdone:\n"
"s_waitcnt vmcnt(0) lgkmcnt(0)\n"
"v_mov_b32 v55, s28\n"
"s_mov_b64 exec, 1\n"
"buffer_store_dword v55, off, s[8:11], s39 sc1\n"
"s_mov_b64 exec, -1\n"
"s_endpgm\n"
"MK_KV:\n"
"s_memrealtime s[18:19]\n"
"s_mul_i32 s24, s12, 500\n"
"s_add_u32 s24, s24, 0\n"
"s_waitcnt lgkmcnt(0)\n"
"s_add_u32 s24, s24, s18\n"
"MK_KV_dly:\n"
"s_memrealtime s[18:19]\n"
"s_waitcnt lgkmcnt(0)\n"
"s_sub_u32 s25, s24, s18\n"
"s_cmp_gt_i32 s25, 0\n"
"s_cbranch_scc0 MK_KV_dlx\n"
"s_sleep 4\n"
"s_branch MK_KV_dly\n"
"MK_KV_dlx:\n"
"s_load_dwordx2 s[44:45], s[6:7], 24\n"
"s_mov_b32 s46, 4718592\n"
"s_mov_b32 s47, 131072\n"
"s_waitcnt lgkmcnt(0)\n"
"s_and_b32 s45, s45, 65535\n"
"s_load_dwordx2 s[48:49], s[6:7], 32\n"
"s_mov_b32 s50, 4718592\n"
"s_mov_b32 s51, 131072\n"
"s_waitcnt lgkmcnt(0)\n"
"s_and_b32 s49, s49, 65535\n"
"v_mbcnt_lo_u32_b32 v0, -1, 0\n"
"v_mbcnt_hi_u32_b32 v0, -1, v0\n"
"s_lshl_b32 s52, s14, 8\n"
"s_lshr_b32 s24, s15, 1\n"
"s_lshl_b32 s24, s24, 10\n"
"s_and_b32 s25, s15, 1\n"
"s_lshl_b32 s25, s25, 7\n"
"s_add_u32 s53, s24, s25\n"
"s_cmp_ge_u32 s15, 2\n"
"s_cbranch_scc1 MK_KV_lv\n"
"v_bfe_u32 v15, v0, 5, 1\n"
"v_mul_u32_u24 v10, 131072, v15\n"
"v_bfe_u32 v15, v0, 4, 1\n"
"v_lshl_add_u32 v10, v15, 15, v10\n"
"v_bfe_u32 v15, v0, 0, 4\n"
"v_lshl_add_u32 v10, v15, 4, v10\n"
"s_lshr_b32 s24, s15, 1\n"
"s_mul_i32 s24, s24, 65536\n"
"s_and_b32 s25, s15, 1\n"
"s_mul_i32 s25, s25, 16384\n"
"s_add_u32 s24, s24, s25\n"
"s_add_u32 s60, s24, s52\n"
"s_add_u32 s61, s60, 8192\n"
"s_mul_i32 s62, s15, 4160\n"
"v_bfe_u32 v15, v0, 4, 1\n"
"v_mul_u32_u24 v12, 8320, v15\n"
"v_bfe_u32 v15, v0, 2, 2\n"
"v_mul_u32_u24 v15, 2080, v15\n"
"v_add_u32 v12, v12, v15\n"
"v_bfe_u32 v15, v0, 5, 1\n"
"v_lshl_add_u32 v12, v15, 9, v12\n"
"v_bfe_u32 v15, v0, 0, 2\n"
"v_lshl_add_u32 v12, v15, 4, v12\n"
"v_add_u32 v12, s53, v12\n"
"v_add_u32 v13, 49152, v12\n"
"v_add_u32 v14, 49152, v13\n"
"s_add_u32 s24, s60, 0\n"
"s_add_u32 m0, s62, 0\n"
"s_nop 0\n"
"buffer_load_dwordx4 v10, s[44:47], s24 offen lds\n"
"s_add_u32 m0, s62, 1024\n"
"s_nop 0\n"
"buffer_load_dwordx4 v10, s[48:51], s24 offen lds\n"
"s_add_u32 s24, s61, 0\n"
"s_add_u32 m0, s62, 2080\n"
"s_nop 0\n"
"buffer_load_dwordx4 v10, s[44:47], s24 offen lds\n"
"s_add_u32 m0, s62, 3104\n"
"s_nop 0\n"
"buffer_load_dwordx4 v10, s[48:51], s24 offen lds\n"
"s_add_u32 s24, s60, 262144\n"
"s_add_u32 m0, s62, 16640\n"
"s_nop 0\n"
"buffer_load_dwordx4 v10, s[44:47], s24 offen lds\n"
"s_add_u32 m0, s62, 17664\n"
"s_nop 0\n"
"buffer_load_dwordx4 v10, s[48:51], s24 offen lds\n"
"s_add_u32 s24, s61, 262144\n"
"s_add_u32 m0, s62, 18720\n"
"s_nop 0\n"
"buffer_load_dwordx4 v10, s[44:47], s24 offen lds\n"
"s_add_u32 m0, s62, 19744\n"
"s_nop 0\n"
"buffer_load_dwordx4 v10, s[48:51], s24 offen lds\n"
"s_add_u32 s24, s60, 524288\n"
"s_add_u32 m0, s62, 33280\n"
"s_nop 0\n"
"buffer_load_dwordx4 v10, s[44:47], s24 offen lds\n"
"s_add_u32 m0, s62, 34304\n"
"s_nop 0\n"
"buffer_load_dwordx4 v10, s[48:51], s24 offen lds\n"
"s_add_u32 s24, s61, 524288\n"
"s_add_u32 m0, s62, 35360\n"
"s_nop 0\n"
"buffer_load_dwordx4 v10, s[44:47], s24 offen lds\n"
"s_add_u32 m0, s62, 36384\n"
"s_nop 0\n"
"buffer_load_dwordx4 v10, s[48:51], s24 offen lds\n"
"s_add_u32 s24, s60, 786432\n"
"s_add_u32 m0, s62, 49920\n"
"s_nop 0\n"
"buffer_load_dwordx4 v10, s[44:47], s24 offen lds\n"
"s_add_u32 m0, s62, 50944\n"
"s_nop 0\n"
"buffer_load_dwordx4 v10, s[48:51], s24 offen lds\n"
"s_add_u32 s24, s61, 786432\n"
"s_add_u32 m0, s62, 52000\n"
"s_nop 0\n"
"buffer_load_dwordx4 v10, s[44:47], s24 offen lds\n"
"s_add_u32 m0, s62, 53024\n"
"s_nop 0\n"
"buffer_load_dwordx4 v10, s[48:51], s24 offen lds\n"
"s_add_u32 s24, s60, 1048576\n"
"s_add_u32 m0, s62, 66560\n"
"s_nop 0\n"
"buffer_load_dwordx4 v10, s[44:47], s24 offen lds\n"
"s_add_u32 m0, s62, 67584\n"
"s_nop 0\n"
"buffer_load_dwordx4 v10, s[48:51], s24 offen lds\n"
"s_add_u32 s24, s61, 1048576\n"
"s_add_u32 m0, s62, 68640\n"
"s_nop 0\n"
"buffer_load_dwordx4 v10, s[44:47], s24 offen lds\n"
"s_add_u32 m0, s62, 69664\n"
"s_nop 0\n"
"buffer_load_dwordx4 v10, s[48:51], s24 offen lds\n"
"s_add_u32 s24, s60, 1310720\n"
"s_add_u32 m0, s62, 83200\n"
"s_nop 0\n"
"buffer_load_dwordx4 v10, s[44:47], s24 offen lds\n"
"s_add_u32 m0, s62, 84224\n"
"s_nop 0\n"
"buffer_load_dwordx4 v10, s[48:51], s24 offen lds\n"
"s_add_u32 s24, s61, 1310720\n"
"s_add_u32 m0, s62, 85280\n"
"s_nop 0\n"
"buffer_load_dwordx4 v10, s[44:47], s24 offen lds\n"
"s_add_u32 m0, s62, 86304\n"
"s_nop 0\n"
"buffer_load_dwordx4 v10, s[48:51], s24 offen lds\n"
"s_add_u32 s24, s60, 1572864\n"
"s_add_u32 m0, s62, 99840\n"
"s_nop 0\n"
"buffer_load_dwordx4 v10, s[44:47], s24 offen lds\n"
"s_add_u32 m0, s62, 100864\n"
"s_nop 0\n"
"buffer_load_dwordx4 v10, s[48:51], s24 offen lds\n"
"s_add_u32 s24, s61, 1572864\n"
"s_add_u32 m0, s62, 101920\n"
"s_nop 0\n"
"buffer_load_dwordx4 v10, s[44:47], s24 offen lds\n"
"s_add_u32 m0, s62, 102944\n"
"s_nop 0\n"
"buffer_load_dwordx4 v10, s[48:51], s24 offen lds\n"
"s_add_u32 s24, s60, 1835008\n"
"s_add_u32 m0, s62, 116480\n"
"s_nop 0\n"
"buffer_load_dwordx4 v10, s[44:47], s24 offen lds\n"
"s_add_u32 m0, s62, 117504\n"
"s_nop 0\n"
"buffer_load_dwordx4 v10, s[48:51], s24 offen lds\n"
"s_add_u32 s24, s61, 1835008\n"
"s_add_u32 m0, s62, 118560\n"
"s_nop 0\n"
"buffer_load_dwordx4 v10, s[44:47], s24 offen lds\n"
"s_add_u32 m0, s62, 119584\n"
"s_nop 0\n"
"buffer_load_dwordx4 v10, s[48:51], s24 offen lds\n"
"s_waitcnt vmcnt(28) lgkmcnt(0)\n"
"s_barrier\n"
"s_add_u32 s24, s60, 2097152\n"
"s_add_u32 m0, s62, 133120\n"
"s_nop 0\n"
"buffer_load_dwordx4 v10, s[44:47], s24 offen lds\n"
"s_add_u32 m0, s62, 134144\n"
"s_nop 0\n"
"buffer_load_dwordx4 v10, s[48:51], s24 offen lds\n"
"s_add_u32 s24, s61, 2097152\n"
"s_add_u32 m0, s62, 135200\n"
"s_nop 0\n"
"buffer_load_dwordx4 v10, s[44:47], s24 offen lds\n"
"s_add_u32 m0, s62, 136224\n"
"s_nop 0\n"
"buffer_load_dwordx4 v10, s[48:51], s24 offen lds\n"
"ds_read_b64_tr_b16 a[0:1], v12 offset:0\n"
"ds_read_b64_tr_b16 a[2:3], v12 offset:256\n"
"ds_read_b64_tr_b16 a[4:5], v12 offset:8\n"
"ds_read_b64_tr_b16 a[6:7], v12 offset:264\n"
"ds_read_b64_tr_b16 a[8:9], v12 offset:64\n"
"ds_read_b64_tr_b16 a[10:11], v12 offset:320\n"
"ds_read_b64_tr_b16 a[12:13], v12 offset:72\n"
"ds_read_b64_tr_b16 a[14:15], v12 offset:328\n"
"s_waitcnt vmcnt(28) lgkmcnt(0)\n"
"s_barrier\n"
"s_add_u32 s24, s60, 2359296\n"
"s_add_u32 m0, s62, 0\n"
"s_nop 0\n"
"buffer_load_dwordx4 v10, s[44:47], s24 offen lds\n"
"s_add_u32 m0, s62, 1024\n"
"s_nop 0\n"
"buffer_load_dwordx4 v10, s[48:51], s24 offen lds\n"
"s_add_u32 s24, s61, 2359296\n"
"s_add_u32 m0, s62, 2080\n"
"s_nop 0\n"
"buffer_load_dwordx4 v10, s[44:47], s24 offen lds\n"
"s_add_u32 m0, s62, 3104\n"
"s_nop 0\n"
"buffer_load_dwordx4 v10, s[48:51], s24 offen lds\n"
"ds_read_b64_tr_b16 a[16:17], v12 offset:16640\n"
"ds_read_b64_tr_b16 a[18:19], v12 offset:16896\n"
"ds_read_b64_tr_b16 a[20:21], v12 offset:16648\n"
"ds_read_b64_tr_b16 a[22:23], v12 offset:16904\n"
"ds_read_b64_tr_b16 a[24:25], v12 offset:16704\n"
"ds_read_b64_tr_b16 a[26:27], v12 offset:16960\n"
"ds_read_b64_tr_b16 a[28:29], v12 offset:16712\n"
"ds_read_b64_tr_b16 a[30:31], v12 offset:16968\n"
"s_waitcnt vmcnt(28) lgkmcnt(0)\n"
"s_barrier\n"
"s_add_u32 s24, s60, 2621440\n"
"s_add_u32 m0, s62, 16640\n"
"s_nop 0\n"
"buffer_load_dwordx4 v10, s[44:47], s24 offen lds\n"
"s_add_u32 m0, s62, 17664\n"
"s_nop 0\n"
"buffer_load_dwordx4 v10, s[48:51], s24 offen lds\n"
"s_add_u32 s24, s61, 2621440\n"
"s_add_u32 m0, s62, 18720\n"
"s_nop 0\n"
"buffer_load_dwordx4 v10, s[44:47], s24 offen lds\n"
"s_add_u32 m0, s62, 19744\n"
"s_nop 0\n"
"buffer_load_dwordx4 v10, s[48:51], s24 offen lds\n"
"ds_read_b64_tr_b16 a[32:33], v12 offset:33280\n"
"ds_read_b64_tr_b16 a[34:35], v12 offset:33536\n"
"ds_read_b64_tr_b16 a[36:37], v12 offset:33288\n"
"ds_read_b64_tr_b16 a[38:39], v12 offset:33544\n"
"ds_read_b64_tr_b16 a[40:41], v12 offset:33344\n"
"ds_read_b64_tr_b16 a[42:43], v12 offset:33600\n"
"ds_read_b64_tr_b16 a[44:45], v12 offset:33352\n"
"ds_read_b64_tr_b16 a[46:47], v12 offset:33608\n"
"s_waitcnt vmcnt(28) lgkmcnt(0)\n"
"s_barrier\n"
"s_add_u32 s24, s60, 2883584\n"
"s_add_u32 m0, s62, 33280\n"
"s_nop 0\n"
"buffer_load_dwordx4 v10, s[44:47], s24 offen lds\n"
"s_add_u32 m0, s62, 34304\n"
"s_nop 0\n"
"buffer_load_dwordx4 v10, s[48:51], s24 offen lds\n"
"s_add_u32 s24, s61, 2883584\n"
"s_add_u32 m0, s62, 35360\n"
"s_nop 0\n"
"buffer_load_dwordx4 v10, s[44:47], s24 offen lds\n"
"s_add_u32 m0, s62, 36384\n"
"s_nop 0\n"
"buffer_load_dwordx4 v10, s[48:51], s24 offen lds\n"
"ds_read_b64_tr_b16 a[48:49], v13 offset:768\n"
"ds_read_b64_tr_b16 a[50:51], v13 offset:1024\n"
"ds_read_b64_tr_b16 a[52:53], v13 offset:776\n"
"ds_read_b64_tr_b16 a[54:55], v13 offset:1032\n"
"ds_read_b64_tr_b16 a[56:57], v13 offset:832\n"
"ds_read_b64_tr_b16 a[58:59], v13 offset:1088\n"
"ds_read_b64_tr_b16 a[60:61], v13 offset:840\n"
"ds_read_b64_tr_b16 a[62:63], v13 offset:1096\n"
"s_waitcnt vmcnt(28) lgkmcnt(0)\n"
"s_barrier\n"
"s_add_u32 s24, s60, 3145728\n"
"s_add_u32 m0, s62, 49920\n"
"s_nop 0\n"
"buffer_load_dwordx4 v10, s[44:47], s24 offen lds\n"
"s_add_u32 m0, s62, 50944\n"
"s_nop 0\n"
"buffer_load_dwordx4 v10, s[48:51], s24 offen lds\n"
"s_add_u32 s24, s61, 3145728\n"
"s_add_u32 m0, s62, 52000\n"
"s_nop 0\n"
"buffer_load_dwordx4 v10, s[44:47], s24 offen lds\n"
"s_add_u32 m0, s62, 53024\n"
"s_nop 0\n"
"buffer_load_dwordx4 v10, s[48:51], s24 offen lds\n"
"ds_read_b64_tr_b16 a[64:65], v13 offset:17408\n"
"ds_read_b64_tr_b16 a[66:67], v13 offset:17664\n"
"ds_read_b64_tr_b16 a[68:69], v13 offset:17416\n"
"ds_read_b64_tr_b16 a[70:71], v13 offset:17672\n"
"ds_read_b64_tr_b16 a[72:73], v13 offset:17472\n"
"ds_read_b64_tr_b16 a[74:75], v13 offset:17728\n"
"ds_read_b64_tr_b16 a[76:77], v13 offset:17480\n"
"ds_read_b64_tr_b16 a[78:79], v13 offset:17736\n"
"s_waitcnt vmcnt(28) lgkmcnt(0)\n"
"s_barrier\n"
"s_add_u32 s24, s60, 3407872\n"
"s_add_u32 m0, s62, 66560\n"
"s_nop 0\n"
"buffer_load_dwordx4 v10, s[44:47], s24 offen lds\n"
"s_add_u32 m0, s62, 67584\n"
"s_nop 0\n"
"buffer_load_dwordx4 v10, s[48:51], s24 offen lds\n"
"s_add_u32 s24, s61, 3407872\n"
"s_add_u32 m0, s62, 68640\n"
"s_nop 0\n"
"buffer_load_dwordx4 v10, s[44:47], s24 offen lds\n"
"s_add_u32 m0, s62, 69664\n"
"s_nop 0\n"
"buffer_load_dwordx4 v10, s[48:51], s24 offen lds\n"
"ds_read_b64_tr_b16 a[80:81], v13 offset:34048\n"
"ds_read_b64_tr_b16 a[82:83], v13 offset:34304\n"
"ds_read_b64_tr_b16 a[84:85], v13 offset:34056\n"
"ds_read_b64_tr_b16 a[86:87], v13 offset:34312\n"
"ds_read_b64_tr_b16 a[88:89], v13 offset:34112\n"
"ds_read_b64_tr_b16 a[90:91], v13 offset:34368\n"
"ds_read_b64_tr_b16 a[92:93], v13 offset:34120\n"
"ds_read_b64_tr_b16 a[94:95], v13 offset:34376\n"
"s_waitcnt vmcnt(28) lgkmcnt(0)\n"
"s_barrier\n"
"s_add_u32 s24, s60, 3670016\n"
"s_add_u32 m0, s62, 83200\n"
"s_nop 0\n"
"buffer_load_dwordx4 v10, s[44:47], s24 offen lds\n"
"s_add_u32 m0, s62, 84224\n"
"s_nop 0\n"
"buffer_load_dwordx4 v10, s[48:51], s24 offen lds\n"
"s_add_u32 s24, s61, 3670016\n"
"s_add_u32 m0, s62, 85280\n"
"s_nop 0\n"
"buffer_load_dwordx4 v10, s[44:47], s24 offen lds\n"
"s_add_u32 m0, s62, 86304\n"
"s_nop 0\n"
"buffer_load_dwordx4 v10, s[48:51], s24 offen lds\n"
"ds_read_b64_tr_b16 a[96:97], v14 offset:1536\n"
"ds_read_b64_tr_b16 a[98:99], v14 offset:1792\n"
"ds_read_b64_tr_b16 a[100:101], v14 offset:1544\n"
"ds_read_b64_tr_b16 a[102:103], v14 offset:1800\n"
"ds_read_b64_tr_b16 a[104:105], v14 offset:1600\n"
"ds_read_b64_tr_b16 a[106:107], v14 offset:1856\n"
"ds_read_b64_tr_b16 a[108:109], v14 offset:1608\n"
"ds_read_b64_tr_b16 a[110:111], v14 offset:1864\n"
"s_waitcnt vmcnt(28) lgkmcnt(0)\n"
"s_barrier\n"
"s_add_u32 s24, s60, 3932160\n"
"s_add_u32 m0, s62, 99840\n"
"s_nop 0\n"
"buffer_load_dwordx4 v10, s[44:47], s24 offen lds\n"
"s_add_u32 m0, s62, 100864\n"
"s_nop 0\n"
"buffer_load_dwordx4 v10, s[48:51], s24 offen lds\n"
"s_add_u32 s24, s61, 3932160\n"
"s_add_u32 m0, s62, 101920\n"
"s_nop 0\n"
"buffer_load_dwordx4 v10, s[44:47], s24 offen lds\n"
"s_add_u32 m0, s62, 102944\n"
"s_nop 0\n"
"buffer_load_dwordx4 v10, s[48:51], s24 offen lds\n"
"ds_read_b64_tr_b16 a[112:113], v14 offset:18176\n"
"ds_read_b64_tr_b16 a[114:115], v14 offset:18432\n"
"ds_read_b64_tr_b16 a[116:117], v14 offset:18184\n"
"ds_read_b64_tr_b16 a[118:119], v14 offset:18440\n"
"ds_read_b64_tr_b16 a[120:121], v14 offset:18240\n"
"ds_read_b64_tr_b16 a[122:123], v14 offset:18496\n"
"ds_read_b64_tr_b16 a[124:125], v14 offset:18248\n"
"ds_read_b64_tr_b16 a[126:127], v14 offset:18504\n"
"s_waitcnt vmcnt(28) lgkmcnt(0)\n"
"s_barrier\n"
"s_add_u32 s24, s60, 4194304\n"
"s_add_u32 m0, s62, 116480\n"
"s_nop 0\n"
"buffer_load_dwordx4 v10, s[44:47], s24 offen lds\n"
"s_add_u32 m0, s62, 117504\n"
"s_nop 0\n"
"buffer_load_dwordx4 v10, s[48:51], s24 offen lds\n"
"s_add_u32 s24, s61, 4194304\n"
"s_add_u32 m0, s62, 118560\n"
"s_nop 0\n"
"buffer_load_dwordx4 v10, s[44:47], s24 offen lds\n"
"s_add_u32 m0, s62, 119584\n"
"s_nop 0\n"
"buffer_load_dwordx4 v10, s[48:51], s24 offen lds\n"
"ds_read_b64_tr_b16 a[128:129], v14 offset:34816\n"
"ds_read_b64_tr_b16 a[130:131], v14 offset:35072\n"
"ds_read_b64_tr_b16 a[132:133], v14 offset:34824\n"
"ds_read_b64_tr_b16 a[134:135], v14 offset:35080\n"
"ds_read_b64_tr_b16 a[136:137], v14 offset:34880\n"
"ds_read_b64_tr_b16 a[138:139], v14 offset:35136\n"
"ds_read_b64_tr_b16 a[140:141], v14 offset:34888\n"
"ds_read_b64_tr_b16 a[142:143], v14 offset:35144\n"
"s_waitcnt vmcnt(28) lgkmcnt(0)\n"
"s_barrier\n"
"s_add_u32 s24, s60, 4456448\n"
"s_add_u32 m0, s62, 133120\n"
"s_nop 0\n"
"buffer_load_dwordx4 v10, s[44:47], s24 offen lds\n"
"s_add_u32 m0, s62, 134144\n"
"s_nop 0\n"
"buffer_load_dwordx4 v10, s[48:51], s24 offen lds\n"
"s_add_u32 s24, s61, 4456448\n"
"s_add_u32 m0, s62, 135200\n"
"s_nop 0\n"
"buffer_load_dwordx4 v10, s[44:47], s24 offen lds\n"
"s_add_u32 m0, s62, 136224\n"
"s_nop 0\n"
"buffer_load_dwordx4 v10, s[48:51], s24 offen lds\n"
"ds_read_b64_tr_b16 a[144:145], v12 offset:0\n"
"ds_read_b64_tr_b16 a[146:147], v12 offset:256\n"
"ds_read_b64_tr_b16 a[148:149], v12 offset:8\n"
"ds_read_b64_tr_b16 a[150:151], v12 offset:264\n"
"ds_read_b64_tr_b16 a[152:153], v12 offset:64\n"
"ds_read_b64_tr_b16 a[154:155], v12 offset:320\n"
"ds_read_b64_tr_b16 a[156:157], v12 offset:72\n"
"ds_read_b64_tr_b16 a[158:159], v12 offset:328\n"
"s_waitcnt vmcnt(28) lgkmcnt(0)\n"
"s_barrier\n"
"ds_read_b64_tr_b16 a[160:161], v12 offset:16640\n"
"ds_read_b64_tr_b16 a[162:163], v12 offset:16896\n"
"ds_read_b64_tr_b16 a[164:165], v12 offset:16648\n"
"ds_read_b64_tr_b16 a[166:167], v12 offset:16904\n"
"ds_read_b64_tr_b16 a[168:169], v12 offset:16704\n"
"ds_read_b64_tr_b16 a[170:171], v12 offset:16960\n"
"ds_read_b64_tr_b16 a[172:173], v12 offset:16712\n"
"ds_read_b64_tr_b16 a[174:175], v12 offset:16968\n"
"s_waitcnt vmcnt(24) lgkmcnt(0)\n"
"s_barrier\n"
"ds_read_b64_tr_b16 a[176:177], v12 offset:33280\n"
"ds_read_b64_tr_b16 a[178:179], v12 offset:33536\n"
"ds_read_b64_tr_b16 a[180:181], v12 offset:33288\n"
"ds_read_b64_tr_b16 a[182:183], v12 offset:33544\n"
"ds_read_b64_tr_b16 a[184:185], v12 offset:33344\n"
"ds_read_b64_tr_b16 a[186:187], v12 offset:33600\n"
"ds_read_b64_tr_b16 a[188:189], v12 offset:33352\n"
"ds_read_b64_tr_b16 a[190:191], v12 offset:33608\n"
"s_waitcnt vmcnt(20) lgkmcnt(0)\n"
"s_barrier\n"
"ds_read_b64_tr_b16 a[192:193], v13 offset:768\n"
"ds_read_b64_tr_b16 a[194:195], v13 offset:1024\n"
"ds_read_b64_tr_b16 a[196:197], v13 offset:776\n"
"ds_read_b64_tr_b16 a[198:199], v13 offset:1032\n"
"ds_read_b64_tr_b16 a[200:201], v13 offset:832\n"
"ds_read_b64_tr_b16 a[202:203], v13 offset:1088\n"
"ds_read_b64_tr_b16 a[204:205], v13 offset:840\n"
"ds_read_b64_tr_b16 a[206:207], v13 offset:1096\n"
"s_waitcnt vmcnt(16) lgkmcnt(0)\n"
"s_barrier\n"
"ds_read_b64_tr_b16 a[208:209], v13 offset:17408\n"
"ds_read_b64_tr_b16 a[210:211], v13 offset:17664\n"
"ds_read_b64_tr_b16 a[212:213], v13 offset:17416\n"
"ds_read_b64_tr_b16 a[214:215], v13 offset:17672\n"
"ds_read_b64_tr_b16 a[216:217], v13 offset:17472\n"
"ds_read_b64_tr_b16 a[218:219], v13 offset:17728\n"
"ds_read_b64_tr_b16 a[220:221], v13 offset:17480\n"
"ds_read_b64_tr_b16 a[222:223], v13 offset:17736\n"
"s_waitcnt vmcnt(12) lgkmcnt(0)\n"
"s_barrier\n"
"ds_read_b64_tr_b16 a[224:225], v13 offset:34048\n"
"ds_read_b64_tr_b16 a[226:227], v13 offset:34304\n"
"ds_read_b64_tr_b16 a[228:229], v13 offset:34056\n"
"ds_read_b64_tr_b16 a[230:231], v13 offset:34312\n"
"ds_read_b64_tr_b16 a[232:233], v13 offset:34112\n"
"ds_read_b64_tr_b16 a[234:235], v13 offset:34368\n"
"ds_read_b64_tr_b16 a[236:237], v13 offset:34120\n"
"ds_read_b64_tr_b16 a[238:239], v13 offset:34376\n"
"s_waitcnt vmcnt(8) lgkmcnt(0)\n"
"s_barrier\n"
"ds_read_b64_tr_b16 a[240:241], v14 offset:1536\n"
"ds_read_b64_tr_b16 a[242:243], v14 offset:1792\n"
"ds_read_b64_tr_b16 a[244:245], v14 offset:1544\n"
"ds_read_b64_tr_b16 a[246:247], v14 offset:1800\n"
"ds_read_b64_tr_b16 a[248:249], v14 offset:1600\n"
"ds_read_b64_tr_b16 a[250:251], v14 offset:1856\n"
"ds_read_b64_tr_b16 a[252:253], v14 offset:1608\n"
"ds_read_b64_tr_b16 a[254:255], v14 offset:1864\n"
"s_waitcnt vmcnt(4) lgkmcnt(0)\n"
"s_barrier\n"
"ds_read_b64_tr_b16 v[252:253], v14 offset:18176\n"
"ds_read_b64_tr_b16 v[254:255], v14 offset:18432\n"
"ds_read_b64_tr_b16 v[248:249], v14 offset:18184\n"
"ds_read_b64_tr_b16 v[250:251], v14 offset:18440\n"
"ds_read_b64_tr_b16 v[244:245], v14 offset:18240\n"
"ds_read_b64_tr_b16 v[246:247], v14 offset:18496\n"
"ds_read_b64_tr_b16 v[240:241], v14 offset:18248\n"
"ds_read_b64_tr_b16 v[242:243], v14 offset:18504\n"
"s_waitcnt vmcnt(0) lgkmcnt(0)\n"
"s_barrier\n"
"ds_read_b64_tr_b16 v[236:237], v14 offset:34816\n"
"ds_read_b64_tr_b16 v[238:239], v14 offset:35072\n"
"ds_read_b64_tr_b16 v[232:233], v14 offset:34824\n"
"ds_read_b64_tr_b16 v[234:235], v14 offset:35080\n"
"ds_read_b64_tr_b16 v[228:229], v14 offset:34880\n"
"ds_read_b64_tr_b16 v[230:231], v14 offset:35136\n"
"ds_read_b64_tr_b16 v[224:225], v14 offset:34888\n"
"ds_read_b64_tr_b16 v[226:227], v14 offset:35144\n"
"s_waitcnt lgkmcnt(0)\n"
"s_barrier\n"
"s_branch MK_KV_l\n"
"MK_KV_lv:\n"
"v_bfe_u32 v15, v0, 5, 1\n"
"v_mul_u32_u24 v10, 131072, v15\n"
"v_bfe_u32 v15, v0, 4, 1\n"
"v_lshl_add_u32 v10, v15, 15, v10\n"
"v_bfe_u32 v15, v0, 0, 4\n"
"v_lshl_add_u32 v10, v15, 4, v10\n"
"s_lshr_b32 s24, s15, 1\n"
"s_mul_i32 s24, s24, 65536\n"
"s_and_b32 s25, s15, 1\n"
"s_mul_i32 s25, s25, 16384\n"
"s_add_u32 s24, s24, s25\n"
"s_add_u32 s60, s24, s52\n"
"s_add_u32 s61, s60, 8192\n"
"s_mul_i32 s62, s15, 4160\n"
"v_bfe_u32 v15, v0, 4, 1\n"
"v_mul_u32_u24 v12, 8320, v15\n"
"v_bfe_u32 v15, v0, 2, 2\n"
"v_mul_u32_u24 v15, 2080, v15\n"
"v_add_u32 v12, v12, v15\n"
"v_bfe_u32 v15, v0, 5, 1\n"
"v_lshl_add_u32 v12, v15, 9, v12\n"
"v_bfe_u32 v15, v0, 0, 2\n"
"v_lshl_add_u32 v12, v15, 3, v12\n"
"v_add_u32 v12, s53, v12\n"
"v_add_u32 v13, 49152, v12\n"
"v_add_u32 v14, 49152, v13\n"
"s_add_u32 s24, s60, 0\n"
"s_add_u32 m0, s62, 0\n"
"s_nop 0\n"
"buffer_load_dwordx4 v10, s[44:47], s24 offen lds\n"
"s_add_u32 m0, s62, 1024\n"
"s_nop 0\n"
"buffer_load_dwordx4 v10, s[48:51], s24 offen lds\n"
"s_add_u32 s24, s61, 0\n"
"s_add_u32 m0, s62, 2080\n"
"s_nop 0\n"
"buffer_load_dwordx4 v10, s[44:47], s24 offen lds\n"
"s_add_u32 m0, s62, 3104\n"
"s_nop 0\n"
"buffer_load_dwordx4 v10, s[48:51], s24 offen lds\n"
"s_add_u32 s24, s60, 262144\n"
"s_add_u32 m0, s62, 16640\n"
"s_nop 0\n"
"buffer_load_dwordx4 v10, s[44:47], s24 offen lds\n"
"s_add_u32 m0, s62, 17664\n"
"s_nop 0\n"
"buffer_load_dwordx4 v10, s[48:51], s24 offen lds\n"
"s_add_u32 s24, s61, 262144\n"
"s_add_u32 m0, s62, 18720\n"
"s_nop 0\n"
"buffer_load_dwordx4 v10, s[44:47], s24 offen lds\n"
"s_add_u32 m0, s62, 19744\n"
"s_nop 0\n"
"buffer_load_dwordx4 v10, s[48:51], s24 offen lds\n"
"s_add_u32 s24, s60, 524288\n"
"s_add_u32 m0, s62, 33280\n"
"s_nop 0\n"
"buffer_load_dwordx4 v10, s[44:47], s24 offen lds\n"
"s_add_u32 m0, s62, 34304\n"
"s_nop 0\n"
"buffer_load_dwordx4 v10, s[48:51], s24 offen lds\n"
"s_add_u32 s24, s61, 524288\n"
"s_add_u32 m0, s62, 35360\n"
"s_nop 0\n"
"buffer_load_dwordx4 v10, s[44:47], s24 offen lds\n"
"s_add_u32 m0, s62, 36384\n"
"s_nop 0\n"
"buffer_load_dwordx4 v10, s[48:51], s24 offen lds\n"
"s_add_u32 s24, s60, 786432\n"
"s_add_u32 m0, s62, 49920\n"
"s_nop 0\n"
"buffer_load_dwordx4 v10, s[44:47], s24 offen lds\n"
"s_add_u32 m0, s62, 50944\n"
"s_nop 0\n"
"buffer_load_dwordx4 v10, s[48:51], s24 offen lds\n"
"s_add_u32 s24, s61, 786432\n"
"s_add_u32 m0, s62, 52000\n"
"s_nop 0\n"
"buffer_load_dwordx4 v10, s[44:47], s24 offen lds\n"
"s_add_u32 m0, s62, 53024\n"
"s_nop 0\n"
"buffer_load_dwordx4 v10, s[48:51], s24 offen lds\n"
"s_add_u32 s24, s60, 1048576\n"
"s_add_u32 m0, s62, 66560\n"
"s_nop 0\n"
"buffer_load_dwordx4 v10, s[44:47], s24 offen lds\n"
"s_add_u32 m0, s62, 67584\n"
"s_nop 0\n"
"buffer_load_dwordx4 v10, s[48:51], s24 offen lds\n"
"s_add_u32 s24, s61, 1048576\n"
"s_add_u32 m0, s62, 68640\n"
"s_nop 0\n"
"buffer_load_dwordx4 v10, s[44:47], s24 offen lds\n"
"s_add_u32 m0, s62, 69664\n"
"s_nop 0\n"
"buffer_load_dwordx4 v10, s[48:51], s24 offen lds\n"
"s_add_u32 s24, s60, 1310720\n"
"s_add_u32 m0, s62, 83200\n"
"s_nop 0\n"
"buffer_load_dwordx4 v10, s[44:47], s24 offen lds\n"
"s_add_u32 m0, s62, 84224\n"
"s_nop 0\n"
"buffer_load_dwordx4 v10, s[48:51], s24 offen lds\n"
"s_add_u32 s24, s61, 1310720\n"
"s_add_u32 m0, s62, 85280\n"
"s_nop 0\n"
"buffer_load_dwordx4 v10, s[44:47], s24 offen lds\n"
"s_add_u32 m0, s62, 86304\n"
"s_nop 0\n"
"buffer_load_dwordx4 v10, s[48:51], s24 offen lds\n"
"s_add_u32 s24, s60, 1572864\n"
"s_add_u32 m0, s62, 99840\n"
"s_nop 0\n"
"buffer_load_dwordx4 v10, s[44:47], s24 offen lds\n"
"s_add_u32 m0, s62, 100864\n"
"s_nop 0\n"
"buffer_load_dwordx4 v10, s[48:51], s24 offen lds\n"
"s_add_u32 s24, s61, 1572864\n"
"s_add_u32 m0, s62, 101920\n"
"s_nop 0\n"
"buffer_load_dwordx4 v10, s[44:47], s24 offen lds\n"
"s_add_u32 m0, s62, 102944\n"
"s_nop 0\n"
"buffer_load_dwordx4 v10, s[48:51], s24 offen lds\n"
"s_add_u32 s24, s60, 1835008\n"
"s_add_u32 m0, s62, 116480\n"
"s_nop 0\n"
"buffer_load_dwordx4 v10, s[44:47], s24 offen lds\n"
"s_add_u32 m0, s62, 117504\n"
"s_nop 0\n"
"buffer_load_dwordx4 v10, s[48:51], s24 offen lds\n"
"s_add_u32 s24, s61, 1835008\n"
"s_add_u32 m0, s62, 118560\n"
"s_nop 0\n"
"buffer_load_dwordx4 v10, s[44:47], s24 offen lds\n"
"s_add_u32 m0, s62, 119584\n"
"s_nop 0\n"
"buffer_load_dwordx4 v10, s[48:51], s24 offen lds\n"
"s_waitcnt vmcnt(28) lgkmcnt(0)\n"
"s_barrier\n"
"s_add_u32 s24, s60, 2097152\n"
"s_add_u32 m0, s62, 133120\n"
"s_nop 0\n"
"buffer_load_dwordx4 v10, s[44:47], s24 offen lds\n"
"s_add_u32 m0, s62, 134144\n"
"s_nop 0\n"
"buffer_load_dwordx4 v10, s[48:51], s24 offen lds\n"
"s_add_u32 s24, s61, 2097152\n"
"s_add_u32 m0, s62, 135200\n"
"s_nop 0\n"
"buffer_load_dwordx4 v10, s[44:47], s24 offen lds\n"
"s_add_u32 m0, s62, 136224\n"
"s_nop 0\n"
"buffer_load_dwordx4 v10, s[48:51], s24 offen lds\n"
"ds_read_b64_tr_b16 a[0:1], v12 offset:0\n"
"ds_read_b64_tr_b16 a[2:3], v12 offset:256\n"
"ds_read_b64_tr_b16 a[4:5], v12 offset:32\n"
"ds_read_b64_tr_b16 a[6:7], v12 offset:288\n"
"ds_read_b64_tr_b16 a[8:9], v12 offset:64\n"
"ds_read_b64_tr_b16 a[10:11], v12 offset:320\n"
"ds_read_b64_tr_b16 a[12:13], v12 offset:96\n"
"ds_read_b64_tr_b16 a[14:15], v12 offset:352\n"
"s_waitcnt vmcnt(28) lgkmcnt(0)\n"
"s_barrier\n"
"s_add_u32 s24, s60, 2359296\n"
"s_add_u32 m0, s62, 0\n"
"s_nop 0\n"
"buffer_load_dwordx4 v10, s[44:47], s24 offen lds\n"
"s_add_u32 m0, s62, 1024\n"
"s_nop 0\n"
"buffer_load_dwordx4 v10, s[48:51], s24 offen lds\n"
"s_add_u32 s24, s61, 2359296\n"
"s_add_u32 m0, s62, 2080\n"
"s_nop 0\n"
"buffer_load_dwordx4 v10, s[44:47], s24 offen lds\n"
"s_add_u32 m0, s62, 3104\n"
"s_nop 0\n"
"buffer_load_dwordx4 v10, s[48:51], s24 offen lds\n"
"ds_read_b64_tr_b16 a[16:17], v12 offset:16640\n"
"ds_read_b64_tr_b16 a[18:19], v12 offset:16896\n"
"ds_read_b64_tr_b16 a[20:21], v12 offset:16672\n"
"ds_read_b64_tr_b16 a[22:23], v12 offset:16928\n"
"ds_read_b64_tr_b16 a[24:25], v12 offset:16704\n"
"ds_read_b64_tr_b16 a[26:27], v12 offset:16960\n"
"ds_read_b64_tr_b16 a[28:29], v12 offset:16736\n"
"ds_read_b64_tr_b16 a[30:31], v12 offset:16992\n"
"s_waitcnt vmcnt(28) lgkmcnt(0)\n"
"s_barrier\n"
"s_add_u32 s24, s60, 2621440\n"
"s_add_u32 m0, s62, 16640\n"
"s_nop 0\n"
"buffer_load_dwordx4 v10, s[44:47], s24 offen lds\n"
"s_add_u32 m0, s62, 17664\n"
"s_nop 0\n"
"buffer_load_dwordx4 v10, s[48:51], s24 offen lds\n"
"s_add_u32 s24, s61, 2621440\n"
"s_add_u32 m0, s62, 18720\n"
"s_nop 0\n"
"buffer_load_dwordx4 v10, s[44:47], s24 offen lds\n"
"s_add_u32 m0, s62, 19744\n"
"s_nop 0\n"
"buffer_load_dwordx4 v10, s[48:51], s24 offen lds\n"
"ds_read_b64_tr_b16 a[32:33], v12 offset:33280\n"
"ds_read_b64_tr_b16 a[34:35], v12 offset:33536\n"
"ds_read_b64_tr_b16 a[36:37], v12 offset:33312\n"
"ds_read_b64_tr_b16 a[38:39], v12 offset:33568\n"
"ds_read_b64_tr_b16 a[40:41], v12 offset:33344\n"
"ds_read_b64_tr_b16 a[42:43], v12 offset:33600\n"
"ds_read_b64_tr_b16 a[44:45], v12 offset:33376\n"
"ds_read_b64_tr_b16 a[46:47], v12 offset:33632\n"
"s_waitcnt vmcnt(28) lgkmcnt(0)\n"
"s_barrier\n"
"s_add_u32 s24, s60, 2883584\n"
"s_add_u32 m0, s62, 33280\n"
"s_nop 0\n"
"buffer_load_dwordx4 v10, s[44:47], s24 offen lds\n"
"s_add_u32 m0, s62, 34304\n"
"s_nop 0\n"
"buffer_load_dwordx4 v10, s[48:51], s24 offen lds\n"
"s_add_u32 s24, s61, 2883584\n"
"s_add_u32 m0, s62, 35360\n"
"s_nop 0\n"
"buffer_load_dwordx4 v10, s[44:47], s24 offen lds\n"
"s_add_u32 m0, s62, 36384\n"
"s_nop 0\n"
"buffer_load_dwordx4 v10, s[48:51], s24 offen lds\n"
"ds_read_b64_tr_b16 a[48:49], v13 offset:768\n"
"ds_read_b64_tr_b16 a[50:51], v13 offset:1024\n"
"ds_read_b64_tr_b16 a[52:53], v13 offset:800\n"
"ds_read_b64_tr_b16 a[54:55], v13 offset:1056\n"
"ds_read_b64_tr_b16 a[56:57], v13 offset:832\n"
"ds_read_b64_tr_b16 a[58:59], v13 offset:1088\n"
"ds_read_b64_tr_b16 a[60:61], v13 offset:864\n"
"ds_read_b64_tr_b16 a[62:63], v13 offset:1120\n"
"s_waitcnt vmcnt(28) lgkmcnt(0)\n"
"s_barrier\n"
"s_add_u32 s24, s60, 3145728\n"
"s_add_u32 m0, s62, 49920\n"
"s_nop 0\n"
"buffer_load_dwordx4 v10, s[44:47], s24 offen lds\n"
"s_add_u32 m0, s62, 50944\n"
"s_nop 0\n"
"buffer_load_dwordx4 v10, s[48:51], s24 offen lds\n"
"s_add_u32 s24, s61, 3145728\n"
"s_add_u32 m0, s62, 52000\n"
"s_nop 0\n"
"buffer_load_dwordx4 v10, s[44:47], s24 offen lds\n"
"s_add_u32 m0, s62, 53024\n"
"s_nop 0\n"
"buffer_load_dwordx4 v10, s[48:51], s24 offen lds\n"
"ds_read_b64_tr_b16 a[64:65], v13 offset:17408\n"
"ds_read_b64_tr_b16 a[66:67], v13 offset:17664\n"
"ds_read_b64_tr_b16 a[68:69], v13 offset:17440\n"
"ds_read_b64_tr_b16 a[70:71], v13 offset:17696\n"
"ds_read_b64_tr_b16 a[72:73], v13 offset:17472\n"
"ds_read_b64_tr_b16 a[74:75], v13 offset:17728\n"
"ds_read_b64_tr_b16 a[76:77], v13 offset:17504\n"
"ds_read_b64_tr_b16 a[78:79], v13 offset:17760\n"
"s_waitcnt vmcnt(28) lgkmcnt(0)\n"
"s_barrier\n"
"s_add_u32 s24, s60, 3407872\n"
"s_add_u32 m0, s62, 66560\n"
"s_nop 0\n"
"buffer_load_dwordx4 v10, s[44:47], s24 offen lds\n"
"s_add_u32 m0, s62, 67584\n"
"s_nop 0\n"
"buffer_load_dwordx4 v10, s[48:51], s24 offen lds\n"
"s_add_u32 s24, s61, 3407872\n"
"s_add_u32 m0, s62, 68640\n"
"s_nop 0\n"
"buffer_load_dwordx4 v10, s[44:47], s24 offen lds\n"
"s_add_u32 m0, s62, 69664\n"
"s_nop 0\n"
"buffer_load_dwordx4 v10, s[48:51], s24 offen lds\n"
"ds_read_b64_tr_b16 a[80:81], v13 offset:34048\n"
"ds_read_b64_tr_b16 a[82:83], v13 offset:34304\n"
"ds_read_b64_tr_b16 a[84:85], v13 offset:34080\n"
"ds_read_b64_tr_b16 a[86:87], v13 offset:34336\n"
"ds_read_b64_tr_b16 a[88:89], v13 offset:34112\n"
"ds_read_b64_tr_b16 a[90:91], v13 offset:34368\n"
"ds_read_b64_tr_b16 a[92:93], v13 offset:34144\n"
"ds_read_b64_tr_b16 a[94:95], v13 offset:34400\n"
"s_waitcnt vmcnt(28) lgkmcnt(0)\n"
"s_barrier\n"
"s_add_u32 s24, s60, 3670016\n"
"s_add_u32 m0, s62, 83200\n"
"s_nop 0\n"
"buffer_load_dwordx4 v10, s[44:47], s24 offen lds\n"
"s_add_u32 m0, s62, 84224\n"
"s_nop 0\n"
"buffer_load_dwordx4 v10, s[48:51], s24 offen lds\n"
"s_add_u32 s24, s61, 3670016\n"
"s_add_u32 m0, s62, 85280\n"
"s_nop 0\n"
"buffer_load_dwordx4 v10, s[44:47], s24 offen lds\n"
"s_add_u32 m0, s62, 86304\n"
"s_nop 0\n"
"buffer_load_dwordx4 v10, s[48:51], s24 offen lds\n"
"ds_read_b64_tr_b16 a[96:97], v14 offset:1536\n"
"ds_read_b64_tr_b16 a[98:99], v14 offset:1792\n"
"ds_read_b64_tr_b16 a[100:101], v14 offset:1568\n"
"ds_read_b64_tr_b16 a[102:103], v14 offset:1824\n"
"ds_read_b64_tr_b16 a[104:105], v14 offset:1600\n"
"ds_read_b64_tr_b16 a[106:107], v14 offset:1856\n"
"ds_read_b64_tr_b16 a[108:109], v14 offset:1632\n"
"ds_read_b64_tr_b16 a[110:111], v14 offset:1888\n"
"s_waitcnt vmcnt(28) lgkmcnt(0)\n"
"s_barrier\n"
"s_add_u32 s24, s60, 3932160\n"
"s_add_u32 m0, s62, 99840\n"
"s_nop 0\n"
"buffer_load_dwordx4 v10, s[44:47], s24 offen lds\n"
"s_add_u32 m0, s62, 100864\n"
"s_nop 0\n"
"buffer_load_dwordx4 v10, s[48:51], s24 offen lds\n"
"s_add_u32 s24, s61, 3932160\n"
"s_add_u32 m0, s62, 101920\n"
"s_nop 0\n"
"buffer_load_dwordx4 v10, s[44:47], s24 offen lds\n"
"s_add_u32 m0, s62, 102944\n"
"s_nop 0\n"
"buffer_load_dwordx4 v10, s[48:51], s24 offen lds\n"
"ds_read_b64_tr_b16 a[112:113], v14 offset:18176\n"
"ds_read_b64_tr_b16 a[114:115], v14 offset:18432\n"
"ds_read_b64_tr_b16 a[116:117], v14 offset:18208\n"
"ds_read_b64_tr_b16 a[118:119], v14 offset:18464\n"
"ds_read_b64_tr_b16 a[120:121], v14 offset:18240\n"
"ds_read_b64_tr_b16 a[122:123], v14 offset:18496\n"
"ds_read_b64_tr_b16 a[124:125], v14 offset:18272\n"
"ds_read_b64_tr_b16 a[126:127], v14 offset:18528\n"
"s_waitcnt vmcnt(28) lgkmcnt(0)\n"
"s_barrier\n"
"s_add_u32 s24, s60, 4194304\n"
"s_add_u32 m0, s62, 116480\n"
"s_nop 0\n"
"buffer_load_dwordx4 v10, s[44:47], s24 offen lds\n"
"s_add_u32 m0, s62, 117504\n"
"s_nop 0\n"
"buffer_load_dwordx4 v10, s[48:51], s24 offen lds\n"
"s_add_u32 s24, s61, 4194304\n"
"s_add_u32 m0, s62, 118560\n"
"s_nop 0\n"
"buffer_load_dwordx4 v10, s[44:47], s24 offen lds\n"
"s_add_u32 m0, s62, 119584\n"
"s_nop 0\n"
"buffer_load_dwordx4 v10, s[48:51], s24 offen lds\n"
"ds_read_b64_tr_b16 a[128:129], v14 offset:34816\n"
"ds_read_b64_tr_b16 a[130:131], v14 offset:35072\n"
"ds_read_b64_tr_b16 a[132:133], v14 offset:34848\n"
"ds_read_b64_tr_b16 a[134:135], v14 offset:35104\n"
"ds_read_b64_tr_b16 a[136:137], v14 offset:34880\n"
"ds_read_b64_tr_b16 a[138:139], v14 offset:35136\n"
"ds_read_b64_tr_b16 a[140:141], v14 offset:34912\n"
"ds_read_b64_tr_b16 a[142:143], v14 offset:35168\n"
"s_waitcnt vmcnt(28) lgkmcnt(0)\n"
"s_barrier\n"
"s_add_u32 s24, s60, 4456448\n"
"s_add_u32 m0, s62, 133120\n"
"s_nop 0\n"
"buffer_load_dwordx4 v10, s[44:47], s24 offen lds\n"
"s_add_u32 m0, s62, 134144\n"
"s_nop 0\n"
"buffer_load_dwordx4 v10, s[48:51], s24 offen lds\n"
"s_add_u32 s24, s61, 4456448\n"
"s_add_u32 m0, s62, 135200\n"
"s_nop 0\n"
"buffer_load_dwordx4 v10, s[44:47], s24 offen lds\n"
"s_add_u32 m0, s62, 136224\n"
"s_nop 0\n"
"buffer_load_dwordx4 v10, s[48:51], s24 offen lds\n"
"ds_read_b64_tr_b16 a[144:145], v12 offset:0\n"
"ds_read_b64_tr_b16 a[146:147], v12 offset:256\n"
"ds_read_b64_tr_b16 a[148:149], v12 offset:32\n"
"ds_read_b64_tr_b16 a[150:151], v12 offset:288\n"
"ds_read_b64_tr_b16 a[152:153], v12 offset:64\n"
"ds_read_b64_tr_b16 a[154:155], v12 offset:320\n"
"ds_read_b64_tr_b16 a[156:157], v12 offset:96\n"
"ds_read_b64_tr_b16 a[158:159], v12 offset:352\n"
"s_waitcnt vmcnt(28) lgkmcnt(0)\n"
"s_barrier\n"
"ds_read_b64_tr_b16 a[160:161], v12 offset:16640\n"
"ds_read_b64_tr_b16 a[162:163], v12 offset:16896\n"
"ds_read_b64_tr_b16 a[164:165], v12 offset:16672\n"
"ds_read_b64_tr_b16 a[166:167], v12 offset:16928\n"
"ds_read_b64_tr_b16 a[168:169], v12 offset:16704\n"
"ds_read_b64_tr_b16 a[170:171], v12 offset:16960\n"
"ds_read_b64_tr_b16 a[172:173], v12 offset:16736\n"
"ds_read_b64_tr_b16 a[174:175], v12 offset:16992\n"
"s_waitcnt vmcnt(24) lgkmcnt(0)\n"
"s_barrier\n"
"ds_read_b64_tr_b16 a[176:177], v12 offset:33280\n"
"ds_read_b64_tr_b16 a[178:179], v12 offset:33536\n"
"ds_read_b64_tr_b16 a[180:181], v12 offset:33312\n"
"ds_read_b64_tr_b16 a[182:183], v12 offset:33568\n"
"ds_read_b64_tr_b16 a[184:185], v12 offset:33344\n"
"ds_read_b64_tr_b16 a[186:187], v12 offset:33600\n"
"ds_read_b64_tr_b16 a[188:189], v12 offset:33376\n"
"ds_read_b64_tr_b16 a[190:191], v12 offset:33632\n"
"s_waitcnt vmcnt(20) lgkmcnt(0)\n"
"s_barrier\n"
"ds_read_b64_tr_b16 a[192:193], v13 offset:768\n"
"ds_read_b64_tr_b16 a[194:195], v13 offset:1024\n"
"ds_read_b64_tr_b16 a[196:197], v13 offset:800\n"
"ds_read_b64_tr_b16 a[198:199], v13 offset:1056\n"
"ds_read_b64_tr_b16 a[200:201], v13 offset:832\n"
"ds_read_b64_tr_b16 a[202:203], v13 offset:1088\n"
"ds_read_b64_tr_b16 a[204:205], v13 offset:864\n"
"ds_read_b64_tr_b16 a[206:207], v13 offset:1120\n"
"s_waitcnt vmcnt(16) lgkmcnt(0)\n"
"s_barrier\n"
"ds_read_b64_tr_b16 a[208:209], v13 offset:17408\n"
"ds_read_b64_tr_b16 a[210:211], v13 offset:17664\n"
"ds_read_b64_tr_b16 a[212:213], v13 offset:17440\n"
"ds_read_b64_tr_b16 a[214:215], v13 offset:17696\n"
"ds_read_b64_tr_b16 a[216:217], v13 offset:17472\n"
"ds_read_b64_tr_b16 a[218:219], v13 offset:17728\n"
"ds_read_b64_tr_b16 a[220:221], v13 offset:17504\n"
"ds_read_b64_tr_b16 a[222:223], v13 offset:17760\n"
"s_waitcnt vmcnt(12) lgkmcnt(0)\n"
"s_barrier\n"
"ds_read_b64_tr_b16 a[224:225], v13 offset:34048\n"
"ds_read_b64_tr_b16 a[226:227], v13 offset:34304\n"
"ds_read_b64_tr_b16 a[228:229], v13 offset:34080\n"
"ds_read_b64_tr_b16 a[230:231], v13 offset:34336\n"
"ds_read_b64_tr_b16 a[232:233], v13 offset:34112\n"
"ds_read_b64_tr_b16 a[234:235], v13 offset:34368\n"
"ds_read_b64_tr_b16 a[236:237], v13 offset:34144\n"
"ds_read_b64_tr_b16 a[238:239], v13 offset:34400\n"
"s_waitcnt vmcnt(8) lgkmcnt(0)\n"
"s_barrier\n"
"ds_read_b64_tr_b16 a[240:241], v14 offset:1536\n"
"ds_read_b64_tr_b16 a[242:243], v14 offset:1792\n"
"ds_read_b64_tr_b16 a[244:245], v14 offset:1568\n"
"ds_read_b64_tr_b16 a[246:247], v14 offset:1824\n"
"ds_read_b64_tr_b16 a[248:249], v14 offset:1600\n"
"ds_read_b64_tr_b16 a[250:251], v14 offset:1856\n"
"ds_read_b64_tr_b16 a[252:253], v14 offset:1632\n"
"ds_read_b64_tr_b16 a[254:255], v14 offset:1888\n"
"s_waitcnt vmcnt(4) lgkmcnt(0)\n"
"s_barrier\n"
"ds_read_b64_tr_b16 v[252:253], v14 offset:18176\n"
"ds_read_b64_tr_b16 v[254:255], v14 offset:18432\n"
"ds_read_b64_tr_b16 v[248:249], v14 offset:18208\n"
"ds_read_b64_tr_b16 v[250:251], v14 offset:18464\n"
"ds_read_b64_tr_b16 v[244:245], v14 offset:18240\n"
"ds_read_b64_tr_b16 v[246:247], v14 offset:18496\n"
"ds_read_b64_tr_b16 v[240:241], v14 offset:18272\n"
"ds_read_b64_tr_b16 v[242:243], v14 offset:18528\n"
"s_waitcnt vmcnt(0) lgkmcnt(0)\n"
"s_barrier\n"
"ds_read_b64_tr_b16 v[236:237], v14 offset:34816\n"
"ds_read_b64_tr_b16 v[238:239], v14 offset:35072\n"
"ds_read_b64_tr_b16 v[232:233], v14 offset:34848\n"
"ds_read_b64_tr_b16 v[234:235], v14 offset:35104\n"
"ds_read_b64_tr_b16 v[228:229], v14 offset:34880\n"
"ds_read_b64_tr_b16 v[230:231], v14 offset:35136\n"
"ds_read_b64_tr_b16 v[224:225], v14 offset:34912\n"
"ds_read_b64_tr_b16 v[226:227], v14 offset:35168\n"
"s_waitcnt lgkmcnt(0)\n"
"s_barrier\n"
"MK_KV_l:\n"
"s_load_dwordx2 s[44:45], s[6:7], 16\n"
"s_mov_b32 s46, 4718592\n"
"s_mov_b32 s47, 131072\n"
"s_waitcnt lgkmcnt(0)\n"
"s_and_b32 s45, s45, 65535\n"
"s_add_u32 s52, s52, 128\n"
"s_lshl_b32 s53, s15, 5\n"
"v_bfe_u32 v12, v0, 5, 1\n"
"v_mul_u32_u24 v10, 262144, v12\n"
"v_bfe_u32 v12, v0, 4, 1\n"
"v_lshl_add_u32 v10, v12, 17, v10\n"
"v_bfe_u32 v12, v0, 3, 1\n"
"v_lshl_add_u32 v10, v12, 15, v10\n"
"v_bfe_u32 v12, v0, 0, 3\n"
"v_lshl_add_u32 v10, v12, 4, v10\n"
"s_lshr_b32 s24, s15, 1\n"
"s_lshl_b32 s24, s24, 16\n"
"s_and_b32 s25, s15, 1\n"
"s_lshl_b32 s25, s25, 14\n"
"s_add_u32 s24, s24, s25\n"
"s_add_u32 s60, s24, s52\n"
"s_add_u32 s61, s60, 8192\n"
"s_mul_i32 s62, s15, 18496\n"
"s_add_u32 m0, s62, 0\n"
"s_add_u32 s24, s60, 0\n"
"buffer_load_dwordx4 v10, s[44:47], s24 offen lds\n"
"s_add_u32 m0, s62, 1024\n"
"s_add_u32 s24, s60, 524288\n"
"buffer_load_dwordx4 v10, s[44:47], s24 offen lds\n"
"s_add_u32 m0, s62, 2048\n"
"s_add_u32 s24, s60, 1048576\n"
"buffer_load_dwordx4 v10, s[44:47], s24 offen lds\n"
"s_add_u32 m0, s62, 3072\n"
"s_add_u32 s24, s60, 1572864\n"
"buffer_load_dwordx4 v10, s[44:47], s24 offen lds\n"
"s_add_u32 m0, s62, 4096\n"
"s_add_u32 s24, s60, 2097152\n"
"buffer_load_dwordx4 v10, s[44:47], s24 offen lds\n"
"s_add_u32 m0, s62, 5120\n"
"s_add_u32 s24, s60, 2621440\n"
"buffer_load_dwordx4 v10, s[44:47], s24 offen lds\n"
"s_add_u32 m0, s62, 6144\n"
"s_add_u32 s24, s60, 3145728\n"
"buffer_load_dwordx4 v10, s[44:47], s24 offen lds\n"
"s_add_u32 m0, s62, 7168\n"
"s_add_u32 s24, s60, 3670016\n"
"buffer_load_dwordx4 v10, s[44:47], s24 offen lds\n"
"s_add_u32 m0, s62, 8192\n"
"s_add_u32 s24, s60, 4194304\n"
"buffer_load_dwordx4 v10, s[44:47], s24 offen lds\n"
"s_add_u32 m0, s62, 9248\n"
"s_add_u32 s24, s61, 0\n"
"buffer_load_dwordx4 v10, s[44:47], s24 offen lds\n"
"s_add_u32 m0, s62, 10272\n"
"s_add_u32 s24, s61, 524288\n"
"buffer_load_dwordx4 v10, s[44:47], s24 offen lds\n"
"s_add_u32 m0, s62, 11296\n"
"s_add_u32 s24, s61, 1048576\n"
"buffer_load_dwordx4 v10, s[44:47], s24 offen lds\n"
"s_add_u32 m0, s62, 12320\n"
"s_add_u32 s24, s61, 1572864\n"
"buffer_load_dwordx4 v10, s[44:47], s24 offen lds\n"
"s_add_u32 m0, s62, 13344\n"
"s_add_u32 s24, s61, 2097152\n"
"buffer_load_dwordx4 v10, s[44:47], s24 offen lds\n"
"s_add_u32 m0, s62, 14368\n"
"s_add_u32 s24, s61, 2621440\n"
"buffer_load_dwordx4 v10, s[44:47], s24 offen lds\n"
"s_add_u32 m0, s62, 15392\n"
"s_add_u32 s24, s61, 3145728\n"
"buffer_load_dwordx4 v10, s[44:47], s24 offen lds\n"
"s_add_u32 m0, s62, 16416\n"
"s_add_u32 s24, s61, 3670016\n"
"buffer_load_dwordx4 v10, s[44:47], s24 offen lds\n"
"s_add_u32 m0, s62, 17440\n"
"s_add_u32 s24, s61, 4194304\n"
"buffer_load_dwordx4 v10, s[44:47], s24 offen lds\n"
"v_bfe_u32 v12, v0, 4, 1\n"
"v_mul_u32_u24 v11, 36992, v12\n"
"v_bfe_u32 v12, v0, 2, 2\n"
"v_mul_u32_u24 v12, 9248, v12\n"
"v_add_u32 v11, v11, v12\n"
"v_bfe_u32 v12, v0, 5, 1\n"
"v_lshl_add_u32 v11, v12, 8, v11\n"
"v_bfe_u32 v12, v0, 0, 2\n"
"v_lshl_add_u32 v11, v12, 3, v11\n"
"v_add_u32 v11, s53, v11\n"
"s_waitcnt vmcnt(0)\n"
"s_barrier\n"
"ds_read_b64_tr_b16 v[220:221], v11 offset:0\n"
"ds_read_b64_tr_b16 v[222:223], v11 offset:128\n"
"ds_read_b64_tr_b16 v[216:217], v11 offset:512\n"
"ds_read_b64_tr_b16 v[218:219], v11 offset:640\n"
"ds_read_b64_tr_b16 v[212:213], v11 offset:1024\n"
"ds_read_b64_tr_b16 v[214:215], v11 offset:1152\n"
"ds_read_b64_tr_b16 v[208:209], v11 offset:1536\n"
"ds_read_b64_tr_b16 v[210:211], v11 offset:1664\n"
"ds_read_b64_tr_b16 v[204:205], v11 offset:2048\n"
"ds_read_b64_tr_b16 v[206:207], v11 offset:2176\n"
"ds_read_b64_tr_b16 v[200:201], v11 offset:2560\n"
"ds_read_b64_tr_b16 v[202:203], v11 offset:2688\n"
"ds_read_b64_tr_b16 v[196:197], v11 offset:3072\n"
"ds_read_b64_tr_b16 v[198:199], v11 offset:3200\n"
"ds_read_b64_tr_b16 v[192:193], v11 offset:3584\n"
"ds_read_b64_tr_b16 v[194:195], v11 offset:3712\n"
"ds_read_b64_tr_b16 v[188:189], v11 offset:4096\n"
"ds_read_b64_tr_b16 v[190:191], v11 offset:4224\n"
"ds_read_b64_tr_b16 v[184:185], v11 offset:4608\n"
"ds_read_b64_tr_b16 v[186:187], v11 offset:4736\n"
"ds_read_b64_tr_b16 v[180:181], v11 offset:5120\n"
"ds_read_b64_tr_b16 v[182:183], v11 offset:5248\n"
"ds_read_b64_tr_b16 v[176:177], v11 offset:5632\n"
"ds_read_b64_tr_b16 v[178:179], v11 offset:5760\n"
"ds_read_b64_tr_b16 v[172:173], v11 offset:6144\n"
"ds_read_b64_tr_b16 v[174:175], v11 offset:6272\n"
"ds_read_b64_tr_b16 v[168:169], v11 offset:6656\n"
"ds_read_b64_tr_b16 v[170:171], v11 offset:6784\n"
"ds_read_b64_tr_b16 v[164:165], v11 offset:7168\n"
"ds_read_b64_tr_b16 v[166:167], v11 offset:7296\n"
"ds_read_b64_tr_b16 v[160:161], v11 offset:7680\n"
"ds_read_b64_tr_b16 v[162:163], v11 offset:7808\n"
"ds_read_b64_tr_b16 v[156:157], v11 offset:8192\n"
"ds_read_b64_tr_b16 v[158:159], v11 offset:8320\n"
"ds_read_b64_tr_b16 v[152:153], v11 offset:8704\n"
"ds_read_b64_tr_b16 v[154:155], v11 offset:8832\n"
"s_waitcnt lgkmcnt(0)\n"
"s_barrier\n"
"s_lshl_b32 s39, s15, 10\n"
"v_lshlrev_b32 v4, 4, v0\n"
"v_add_u32 v4, s39, v4\n"
"v_bfe_u32 v3, v0, 4, 2\n"
"v_mul_u32_u24 v5, 512, v3\n"
"v_bfe_u32 v3, v0, 0, 4\n"
"v_lshl_add_u32 v5, v3, 4, v5\n"
"s_mov_b64 s[32:33], s[8:9]\n"
"s_mov_b32 s35, s11\n"
"s_mov_b32 s36, 0\n"
"s_mov_b32 s37, 7\n"
"s_mov_b32 s28, 0\n"
"s_mov_b32 s41, 36864\n"
"s_mov_b32 s43, -1\n"
"v_mov_b32 v72, 163776\n"
"v_mov_b32 v73, 0\n"
"v_mov_b32 v74, 0\n"
"ds_write_b32 v72, v73\n"
"s_waitcnt lgkmcnt(0)\n"
"s_barrier\n"
"s_add_u32 s31, s14, 32\n"
"s_lshr_b32 s27, s31, 6\n"
"s_lshl_b32 s27, s27, 13\n"
"s_lshl_b32 s31, s31, 7\n"
"s_add_u32 s31, s31, s27\n"
"s_add_u32 s31, s31, s16\n"
"s_add_u32 s31, s31, 1835008\n"
"s_mov_b32 s54, 0\n"
"s_mov_b32 s55, -1\n"
"s_cmp_eq_u32 s12, 0\n"
"s_cselect_b32 s24, 7, 3\n"
"v_lshrrev_b32 v11, 2, v0\n"
"v_and_b32 v11, s24, v11\n"
"v_add_u32 v11, 128, v11\n"
"v_bfe_u32 v3, v0, 3, 2\n"
"v_add_u32 v3, 124, v3\n"
"v_cndmask_b32 v11, v11, v3, s[54:55]\n"
"v_lshrrev_b32 v10, 6, v11\n"
"v_lshlrev_b32 v10, 13, v10\n"
"v_lshl_add_u32 v1, v11, 7, v10\n"
"v_add_u32 v1, 1835008, v1\n"
"v_add_u32 v1, s16, v1\n"
"v_mov_b32 v3, 15\n"
"v_cndmask_b32 v2, 0, v3, s[54:55]\n"
"s_lshl_b32 s52, s14, 14\n"
"s_add_u32 s52, s52, s16\n"
"s_lshl_b32 s56, s15, 10\n"
"s_add_u32 s56, s56, s52\n"
"s_add_u32 s56, s56, 1048576\n"
"s_and_b32 s25, s15, 1\n"
"s_lshl_b32 s25, s25, 12\n"
"s_add_u32 s52, s52, s25\n"
"s_lshl_b32 s25, s28, 10\n"
"s_and_b32 s25, s25, 8192\n"
"s_xor_b32 s25, s25, s16\n"
"s_lshl_b32 s38, s28, 18\n"
"s_add_u32 s38, s38, s25\n"
"s_add_u32 s38, s38, 8388608\n"
"v_lshlrev_b32 v6, 4, v0\n"
"s_cmp_ge_u32 s15, 2\n"
"s_cbranch_scc1 MK_KV_v\n"
"MK_KV_wait:\n"
"s_mov_b32 s29, s30\n"
"buffer_load_dword v3, v1, s[8:11], 0 offen sc1\n"
"s_waitcnt vmcnt(0)\n"
"s_mov_b64 exec, 1\n"
"ds_add_rtn_u32 v74, v72, v73\n"
"s_mov_b64 exec, -1\n"
"s_waitcnt lgkmcnt(0)\n"
"v_and_b32 v75, 3, v74\n"
"v_cmp_eq_u32 vcc, s37, v75\n"
"v_mov_b32 v76, s43\n"
"s_nop 0\n"
"v_cndmask_b32 v75, -1, 0, vcc\n"
"buffer_store_dword v76, v75, s[8:11], s31 offen sc1\n"
"s_mov_b32 s37, 7\n"
"v_mov_b32 v73, 0\n"
"s_branch MK_KV_poll_in\n"
"MK_KV_poll:\n"
"buffer_load_dword v3, v1, s[8:11], 0 offen sc1\n"
"s_add_u32 s29, s29, 1\n"
"s_waitcnt vmcnt(0)\n"
"MK_KV_poll_in:\n"
"v_add_u32 v3, v3, v2\n"
"v_cmp_gt_i32 vcc, s28, v3\n"
"s_cbranch_vccz MK_KV_poll_ok\n"
"s_cmp_lt_u32 s29, 131072\n"
"s_cbranch_scc1 MK_KV_poll\n"
"s_mov_b32 s30, 131072\n"
"MK_KV_poll_ok:\n"
"s_add_u32 m0, s39, 0\n"
"s_add_u32 s26, s38, 0\n"
"buffer_load_dwordx4 v4, s[8:11], s26 offen sc1 lds\n"
"s_add_u32 m0, s39, 4096\n"
"s_add_u32 s26, s38, 4096\n"
"buffer_load_dwordx4 v4, s[8:11], s26 offen sc1 lds\n"
"s_add_u32 m0, s39, 8192\n"
"s_add_u32 s26, s38, 16384\n"
"buffer_load_dwordx4 v4, s[8:11], s26 offen sc1 lds\n"
"s_waitcnt vmcnt(0)\n"
"s_barrier\n"
"ds_read_b128 v[48:51], v5 offset:0\n"
"ds_read_b128 v[52:55], v5 offset:256\n"
"ds_read_b128 v[56:59], v5 offset:2048\n"
"ds_read_b128 v[60:63], v5 offset:2304\n"
"MK_KV_body:\n"
"s_waitcnt lgkmcnt(2)\n"
"v_mfma_f32_16x16x32_bf16 v[80:83], v[220:223], v[48:51], 0\n"
"ds_read_b128 v[64:67], v5 offset:4096\n"
"s_add_u32 m0, s39, 12288\n"
"s_add_u32 s26, s38, 20480\n"
"v_mfma_f32_16x16x32_bf16 v[16:19], a[0:3], v[48:51], 0\n"
"ds_read_b128 v[68:71], v5 offset:4352\n"
"s_and_b32 s40, s28, 15\n"
"s_lshl_b32 s40, s40, 22\n"
"v_mfma_f32_16x16x32_bf16 v[24:27], a[4:7], v[48:51], 0\n"
"buffer_load_dwordx4 v4, s[8:11], s26 offen sc1 lds\n"
"v_mfma_f32_16x16x32_bf16 v[32:35], a[8:11], v[48:51], 0\n"
"s_add_u32 s40, s40, 33554432\n"
"s_add_u32 s57, s40, s56\n"
"v_mfma_f32_16x16x32_bf16 v[40:43], a[12:15], v[48:51], 0\n"
"s_add_u32 s40, s40, s52\n"
"v_mfma_f32_16x16x32_bf16 v[84:87], v[220:223], v[52:55], 0\n"
"v_mfma_f32_16x16x32_bf16 v[20:23], a[0:3], v[52:55], 0\n"
"s_add_u32 m0, s39, 16384\n"
"s_add_u32 s26, s38, 32768\n"
"v_mfma_f32_16x16x32_bf16 v[28:31], a[4:7], v[52:55], 0\n"
"buffer_load_dwordx4 v4, s[8:11], s26 offen sc1 lds\n"
"v_mfma_f32_16x16x32_bf16 v[36:39], a[8:11], v[52:55], 0\n"
"v_mfma_f32_16x16x32_bf16 v[44:47], a[12:15], v[52:55], 0\n"
"s_waitcnt lgkmcnt(2)\n"
"v_mfma_f32_16x16x32_bf16 v[80:83], v[216:219], v[56:59], v[80:83]\n"
"ds_read_b128 v[48:51], v5 offset:6144\n"
"v_mfma_f32_16x16x32_bf16 v[16:19], a[16:19], v[56:59], v[16:19]\n"
"ds_read_b128 v[52:55], v5 offset:6400\n"
"s_add_u32 m0, s39, 20480\n"
"s_add_u32 s26, s38, 36864\n"
"v_mfma_f32_16x16x32_bf16 v[24:27], a[20:23], v[56:59], v[24:27]\n"
"buffer_load_dwordx4 v4, s[8:11], s26 offen sc1 lds\n"
"v_mfma_f32_16x16x32_bf16 v[32:35], a[24:27], v[56:59], v[32:35]\n"
"v_mfma_f32_16x16x32_bf16 v[40:43], a[28:31], v[56:59], v[40:43]\n"
"v_mfma_f32_16x16x32_bf16 v[84:87], v[216:219], v[60:63], v[84:87]\n"
"v_mfma_f32_16x16x32_bf16 v[20:23], a[16:19], v[60:63], v[20:23]\n"
"s_add_u32 m0, s39, 24576\n"
"s_add_u32 s26, s38, 49152\n"
"v_mfma_f32_16x16x32_bf16 v[28:31], a[20:23], v[60:63], v[28:31]\n"
"buffer_load_dwordx4 v4, s[8:11], s26 offen sc1 lds\n"
"v_mfma_f32_16x16x32_bf16 v[36:39], a[24:27], v[60:63], v[36:39]\n"
"v_mfma_f32_16x16x32_bf16 v[44:47], a[28:31], v[60:63], v[44:47]\n"
"s_waitcnt lgkmcnt(2)\n"
"v_mfma_f32_16x16x32_bf16 v[80:83], v[212:215], v[64:67], v[80:83]\n"
"ds_read_b128 v[56:59], v5 offset:8192\n"
"v_mfma_f32_16x16x32_bf16 v[16:19], a[32:35], v[64:67], v[16:19]\n"
"ds_read_b128 v[60:63], v5 offset:8448\n"
"s_add_u32 m0, s39, 28672\n"
"s_add_u32 s26, s38, 53248\n"
"v_mfma_f32_16x16x32_bf16 v[24:27], a[36:39], v[64:67], v[24:27]\n"
"buffer_load_dwordx4 v4, s[8:11], s26 offen sc1 lds\n"
"v_mfma_f32_16x16x32_bf16 v[32:35], a[40:43], v[64:67], v[32:35]\n"
"v_mfma_f32_16x16x32_bf16 v[40:43], a[44:47], v[64:67], v[40:43]\n"
"v_mfma_f32_16x16x32_bf16 v[84:87], v[212:215], v[68:71], v[84:87]\n"
"v_mfma_f32_16x16x32_bf16 v[20:23], a[32:35], v[68:71], v[20:23]\n"
"s_add_u32 m0, s39, 32768\n"
"s_add_u32 s26, s38, 65536\n"
"v_mfma_f32_16x16x32_bf16 v[28:31], a[36:39], v[68:71], v[28:31]\n"
"buffer_load_dwordx4 v4, s[8:11], s26 offen sc1 lds\n"
"v_mfma_f32_16x16x32_bf16 v[36:39], a[40:43], v[68:71], v[36:39]\n"
"v_mfma_f32_16x16x32_bf16 v[44:47], a[44:47], v[68:71], v[44:47]\n"
"s_waitcnt lgkmcnt(2)\n"
"v_mfma_f32_16x16x32_bf16 v[80:83], v[208:211], v[48:51], v[80:83]\n"
"ds_read_b128 v[64:67], v5 offset:10240\n"
"v_mfma_f32_16x16x32_bf16 v[16:19], a[48:51], v[48:51], v[16:19]\n"
"ds_read_b128 v[68:71], v5 offset:10496\n"
"v_mfma_f32_16x16x32_bf16 v[24:27], a[52:55], v[48:51], v[24:27]\n"
"v_mfma_f32_16x16x32_bf16 v[32:35], a[56:59], v[48:51], v[32:35]\n"
"v_mfma_f32_16x16x32_bf16 v[40:43], a[60:63], v[48:51], v[40:43]\n"
"v_mfma_f32_16x16x32_bf16 v[84:87], v[208:211], v[52:55], v[84:87]\n"
"v_mfma_f32_16x16x32_bf16 v[20:23], a[48:51], v[52:55], v[20:23]\n"
"v_mfma_f32_16x16x32_bf16 v[28:31], a[52:55], v[52:55], v[28:31]\n"
"v_mfma_f32_16x16x32_bf16 v[36:39], a[56:59], v[52:55], v[36:39]\n"
"v_mfma_f32_16x16x32_bf16 v[44:47], a[60:63], v[52:55], v[44:47]\n"
"s_waitcnt lgkmcnt(2)\n"
"v_mfma_f32_16x16x32_bf16 v[80:83], v[204:207], v[56:59], v[80:83]\n"
"s_waitcnt vmcnt(4)\n"
"s_barrier\n"
"v_mfma_f32_16x16x32_bf16 v[16:19], a[64:67], v[56:59], v[16:19]\n"
"ds_read_b128 v[48:51], v5 offset:12288\n"
"ds_read_b128 v[52:55], v5 offset:12544\n"
"v_mfma_f32_16x16x32_bf16 v[24:27], a[68:71], v[56:59], v[24:27]\n"
"s_mov_b64 exec, 1\n"
"ds_add_rtn_u32 v74, v72, v73\n"
"s_mov_b64 exec, -1\n"
"v_mfma_f32_16x16x32_bf16 v[32:35], a[72:75], v[56:59], v[32:35]\n"
"v_mfma_f32_16x16x32_bf16 v[40:43], a[76:79], v[56:59], v[40:43]\n"
"v_mfma_f32_16x16x32_bf16 v[84:87], v[204:207], v[60:63], v[84:87]\n"
"v_mfma_f32_16x16x32_bf16 v[20:23], a[64:67], v[60:63], v[20:23]\n"
"v_mfma_f32_16x16x32_bf16 v[28:31], a[68:71], v[60:63], v[28:31]\n"
"v_mfma_f32_16x16x32_bf16 v[36:39], a[72:75], v[60:63], v[36:39]\n"
"s_waitcnt lgkmcnt(0)\n"
"v_and_b32 v75, 3, v74\n"
"v_cmp_eq_u32 vcc, s37, v75\n"
"v_mfma_f32_16x16x32_bf16 v[44:47], a[76:79], v[60:63], v[44:47]\n"
"v_mov_b32 v76, s43\n"
"s_nop 0\n"
"v_mfma_f32_16x16x32_bf16 v[80:83], v[200:203], v[64:67], v[80:83]\n"
"ds_read_b128 v[56:59], v5 offset:14336\n"
"v_cndmask_b32 v75, -1, 0, vcc\n"
"v_mfma_f32_16x16x32_bf16 v[16:19], a[80:83], v[64:67], v[16:19]\n"
"ds_read_b128 v[60:63], v5 offset:14592\n"
"v_mfma_f32_16x16x32_bf16 v[24:27], a[84:87], v[64:67], v[24:27]\n"
"buffer_store_dword v76, v75, s[8:11], s31 offen sc1\n"
"v_mfma_f32_16x16x32_bf16 v[32:35], a[88:91], v[64:67], v[32:35]\n"
"v_mfma_f32_16x16x32_bf16 v[40:43], a[92:95], v[64:67], v[40:43]\n"
"v_mfma_f32_16x16x32_bf16 v[84:87], v[200:203], v[68:71], v[84:87]\n"
"v_mfma_f32_16x16x32_bf16 v[20:23], a[80:83], v[68:71], v[20:23]\n"
"v_mfma_f32_16x16x32_bf16 v[28:31], a[84:87], v[68:71], v[28:31]\n"
"v_mfma_f32_16x16x32_bf16 v[36:39], a[88:91], v[68:71], v[36:39]\n"
"v_mfma_f32_16x16x32_bf16 v[44:47], a[92:95], v[68:71], v[44:47]\n"
"v_mfma_f32_16x16x32_bf16 v[80:83], v[196:199], v[48:51], v[80:83]\n"
"ds_read_b128 v[64:67], v5 offset:16384\n"
"v_mfma_f32_16x16x32_bf16 v[16:19], a[96:99], v[48:51], v[16:19]\n"
"ds_read_b128 v[68:71], v5 offset:16640\n"
"v_mfma_f32_16x16x32_bf16 v[24:27], a[100:103], v[48:51], v[24:27]\n"
"v_mfma_f32_16x16x32_bf16 v[32:35], a[104:107], v[48:51], v[32:35]\n"
"v_mfma_f32_16x16x32_bf16 v[40:43], a[108:111], v[48:51], v[40:43]\n"
"v_mfma_f32_16x16x32_bf16 v[84:87], v[196:199], v[52:55], v[84:87]\n"
"v_mfma_f32_16x16x32_bf16 v[20:23], a[96:99], v[52:55], v[20:23]\n"
"v_mfma_f32_16x16x32_bf16 v[28:31], a[100:103], v[52:55], v[28:31]\n"
"v_mfma_f32_16x16x32_bf16 v[36:39], a[104:107], v[52:55], v[36:39]\n"
"v_mfma_f32_16x16x32_bf16 v[44:47], a[108:111], v[52:55], v[44:47]\n"
"s_waitcnt lgkmcnt(2)\n"
"v_mfma_f32_16x16x32_bf16 v[80:83], v[192:195], v[56:59], v[80:83]\n"
"ds_read_b128 v[48:51], v5 offset:18432\n"
"v_mfma_f32_16x16x32_bf16 v[16:19], a[112:115], v[56:59], v[16:19]\n"
"ds_read_b128 v[52:55], v5 offset:18688\n"
"v_mfma_f32_16x16x32_bf16 v[24:27], a[116:119], v[56:59], v[24:27]\n"
"v_mfma_f32_16x16x32_bf16 v[32:35], a[120:123], v[56:59], v[32:35]\n"
"v_mfma_f32_16x16x32_bf16 v[40:43], a[124:127], v[56:59], v[40:43]\n"
"v_mfma_f32_16x16x32_bf16 v[84:87], v[192:195], v[60:63], v[84:87]\n"
"v_mfma_f32_16x16x32_bf16 v[20:23], a[112:115], v[60:63], v[20:23]\n"
"v_mfma_f32_16x16x32_bf16 v[28:31], a[116:119], v[60:63], v[28:31]\n"
"v_mfma_f32_16x16x32_bf16 v[36:39], a[120:123], v[60:63], v[36:39]\n"
"v_mfma_f32_16x16x32_bf16 v[44:47], a[124:127], v[60:63], v[44:47]\n"
"s_waitcnt lgkmcnt(2)\n"
"v_mfma_f32_16x16x32_bf16 v[80:83], v[188:191], v[64:67], v[80:83]\n"
"s_waitcnt vmcnt(1)\n"
"s_barrier\n"
"v_mfma_f32_16x16x32_bf16 v[16:19], a[128:131], v[64:67], v[16:19]\n"
"ds_read_b128 v[56:59], v5 offset:20480\n"
"ds_read_b128 v[60:63], v5 offset:20736\n"
"v_mfma_f32_16x16x32_bf16 v[24:27], a[132:135], v[64:67], v[24:27]\n"
"buffer_load_dword v3, v1, s[8:11], 0 offen sc1\n"
"v_mfma_f32_16x16x32_bf16 v[32:35], a[136:139], v[64:67], v[32:35]\n"
"s_add_u32 s42, s28, 1\n"
"s_add_u32 s39, s39, s41\n"
"s_lshl_b32 s25, s42, 10\n"
"v_mfma_f32_16x16x32_bf16 v[40:43], a[140:143], v[64:67], v[40:43]\n"
"s_and_b32 s25, s25, 8192\n"
"s_xor_b32 s25, s25, s16\n"
"s_lshl_b32 s38, s42, 18\n"
"v_mfma_f32_16x16x32_bf16 v[84:87], v[188:191], v[68:71], v[84:87]\n"
"s_add_u32 s38, s38, s25\n"
"s_add_u32 s38, s38, 8388608\n"
"v_mfma_f32_16x16x32_bf16 v[20:23], a[128:131], v[68:71], v[20:23]\n"
"v_mfma_f32_16x16x32_bf16 v[28:31], a[132:135], v[68:71], v[28:31]\n"
"v_mfma_f32_16x16x32_bf16 v[36:39], a[136:139], v[68:71], v[36:39]\n"
"v_mfma_f32_16x16x32_bf16 v[44:47], a[140:143], v[68:71], v[44:47]\n"
"s_waitcnt lgkmcnt(2)\n"
"v_mfma_f32_16x16x32_bf16 v[80:83], v[184:187], v[48:51], v[80:83]\n"
"ds_read_b128 v[64:67], v5 offset:22528\n"
"v_mfma_f32_16x16x32_bf16 v[16:19], a[144:147], v[48:51], v[16:19]\n"
"ds_read_b128 v[68:71], v5 offset:22784\n"
"v_mfma_f32_16x16x32_bf16 v[24:27], a[148:151], v[48:51], v[24:27]\n"
"v_mfma_f32_16x16x32_bf16 v[32:35], a[152:155], v[48:51], v[32:35]\n"
"v_mfma_f32_16x16x32_bf16 v[40:43], a[156:159], v[48:51], v[40:43]\n"
"v_mfma_f32_16x16x32_bf16 v[84:87], v[184:187], v[52:55], v[84:87]\n"
"v_mfma_f32_16x16x32_bf16 v[20:23], a[144:147], v[52:55], v[20:23]\n"
"v_mfma_f32_16x16x32_bf16 v[28:31], a[148:151], v[52:55], v[28:31]\n"
"v_mfma_f32_16x16x32_bf16 v[36:39], a[152:155], v[52:55], v[36:39]\n"
"v_mfma_f32_16x16x32_bf16 v[44:47], a[156:159], v[52:55], v[44:47]\n"
"s_waitcnt lgkmcnt(2)\n"
"v_mfma_f32_16x16x32_bf16 v[80:83], v[180:183], v[56:59], v[80:83]\n"
"ds_read_b128 v[48:51], v5 offset:24576\n"
"v_mfma_f32_16x16x32_bf16 v[16:19], a[160:163], v[56:59], v[16:19]\n"
"ds_read_b128 v[52:55], v5 offset:24832\n"
"v_mfma_f32_16x16x32_bf16 v[24:27], a[164:167], v[56:59], v[24:27]\n"
"v_mfma_f32_16x16x32_bf16 v[32:35], a[168:171], v[56:59], v[32:35]\n"
"v_mfma_f32_16x16x32_bf16 v[40:43], a[172:175], v[56:59], v[40:43]\n"
"v_mfma_f32_16x16x32_bf16 v[84:87], v[180:183], v[60:63], v[84:87]\n"
"v_mfma_f32_16x16x32_bf16 v[20:23], a[160:163], v[60:63], v[20:23]\n"
"v_mfma_f32_16x16x32_bf16 v[28:31], a[164:167], v[60:63], v[28:31]\n"
"v_mfma_f32_16x16x32_bf16 v[36:39], a[168:171], v[60:63], v[36:39]\n"
"v_mfma_f32_16x16x32_bf16 v[44:47], a[172:175], v[60:63], v[44:47]\n"
"s_waitcnt lgkmcnt(2)\n"
"v_mfma_f32_16x16x32_bf16 v[80:83], v[176:179], v[64:67], v[80:83]\n"
"ds_read_b128 v[56:59], v5 offset:26624\n"
"ds_read_b128 v[60:63], v5 offset:26880\n"
"v_mfma_f32_16x16x32_bf16 v[16:19], a[176:179], v[64:67], v[16:19]\n"
"s_waitcnt vmcnt(0)\n"
"v_add_u32 v3, v3, v2\n"
"v_cmp_gt_i32 s[76:77], s42, v3\n"
"v_mfma_f32_16x16x32_bf16 v[24:27], a[180:183], v[64:67], v[24:27]\n"
"v_mfma_f32_16x16x32_bf16 v[32:35], a[184:187], v[64:67], v[32:35]\n"
"s_lshr_b32 s27, s42, 6\n"
"s_or_b32 s76, s76, s27\n"
"v_mfma_f32_16x16x32_bf16 v[40:43], a[188:191], v[64:67], v[40:43]\n"
"s_cmp_eq_u64 s[76:77], 0\n"
"s_cselect_b32 s36, 1, 0\n"
"s_cselect_b32 s34, s10, 0\n"
"v_mfma_f32_16x16x32_bf16 v[84:87], v[176:179], v[68:71], v[84:87]\n"
"s_add_u32 m0, s39, 0\n"
"s_add_u32 s26, s38, 0\n"
"v_mfma_f32_16x16x32_bf16 v[20:23], a[176:179], v[68:71], v[20:23]\n"
"buffer_load_dwordx4 v4, s[32:35], s26 offen sc1 lds\n"
"v_mfma_f32_16x16x32_bf16 v[28:31], a[180:183], v[68:71], v[28:31]\n"
"v_mfma_f32_16x16x32_bf16 v[36:39], a[184:187], v[68:71], v[36:39]\n"
"v_mfma_f32_16x16x32_bf16 v[44:47], a[188:191], v[68:71], v[44:47]\n"
"s_waitcnt lgkmcnt(2)\n"
"v_mfma_f32_16x16x32_bf16 v[80:83], v[172:175], v[48:51], v[80:83]\n"
"ds_read_b128 v[64:67], v5 offset:28672\n"
"s_add_u32 m0, s39, 4096\n"
"s_add_u32 s26, s38, 4096\n"
"v_mfma_f32_16x16x32_bf16 v[16:19], a[192:195], v[48:51], v[16:19]\n"
"ds_read_b128 v[68:71], v5 offset:28928\n"
"v_mfma_f32_16x16x32_bf16 v[24:27], a[196:199], v[48:51], v[24:27]\n"
"buffer_load_dwordx4 v4, s[32:35], s26 offen sc1 lds\n"
"v_mfma_f32_16x16x32_bf16 v[32:35], a[200:203], v[48:51], v[32:35]\n"
"v_mfma_f32_16x16x32_bf16 v[40:43], a[204:207], v[48:51], v[40:43]\n"
"v_mfma_f32_16x16x32_bf16 v[84:87], v[172:175], v[52:55], v[84:87]\n"
"v_mfma_f32_16x16x32_bf16 v[20:23], a[192:195], v[52:55], v[20:23]\n"
"s_add_u32 m0, s39, 8192\n"
"s_add_u32 s26, s38, 16384\n"
"v_mfma_f32_16x16x32_bf16 v[28:31], a[196:199], v[52:55], v[28:31]\n"
"buffer_load_dwordx4 v4, s[32:35], s26 offen sc1 lds\n"
"v_mfma_f32_16x16x32_bf16 v[36:39], a[200:203], v[52:55], v[36:39]\n"
"v_mfma_f32_16x16x32_bf16 v[44:47], a[204:207], v[52:55], v[44:47]\n"
"s_waitcnt lgkmcnt(2)\n"
"v_mfma_f32_16x16x32_bf16 v[80:83], v[168:171], v[56:59], v[80:83]\n"
"ds_read_b128 v[48:51], v5 offset:30720\n"
"v_mfma_f32_16x16x32_bf16 v[16:19], a[208:211], v[56:59], v[16:19]\n"
"ds_read_b128 v[52:55], v5 offset:30976\n"
"v_mfma_f32_16x16x32_bf16 v[24:27], a[212:215], v[56:59], v[24:27]\n"
"v_mfma_f32_16x16x32_bf16 v[32:35], a[216:219], v[56:59], v[32:35]\n"
"v_mfma_f32_16x16x32_bf16 v[40:43], a[220:223], v[56:59], v[40:43]\n"
"v_mfma_f32_16x16x32_bf16 v[84:87], v[168:171], v[60:63], v[84:87]\n"
"v_mfma_f32_16x16x32_bf16 v[20:23], a[208:211], v[60:63], v[20:23]\n"
"v_mfma_f32_16x16x32_bf16 v[28:31], a[212:215], v[60:63], v[28:31]\n"
"v_mfma_f32_16x16x32_bf16 v[36:39], a[216:219], v[60:63], v[36:39]\n"
"v_mfma_f32_16x16x32_bf16 v[44:47], a[220:223], v[60:63], v[44:47]\n"
"s_waitcnt lgkmcnt(2)\n"
"v_mfma_f32_16x16x32_bf16 v[80:83], v[164:167], v[64:67], v[80:83]\n"
"ds_read_b128 v[56:59], v5 offset:32768\n"
"v_mfma_f32_16x16x32_bf16 v[16:19], a[224:227], v[64:67], v[16:19]\n"
"ds_read_b128 v[60:63], v5 offset:33024\n"
"v_mfma_f32_16x16x32_bf16 v[24:27], a[228:231], v[64:67], v[24:27]\n"
"v_mfma_f32_16x16x32_bf16 v[32:35], a[232:235], v[64:67], v[32:35]\n"
"v_mfma_f32_16x16x32_bf16 v[40:43], a[236:239], v[64:67], v[40:43]\n"
"v_mfma_f32_16x16x32_bf16 v[84:87], v[164:167], v[68:71], v[84:87]\n"
"v_mfma_f32_16x16x32_bf16 v[20:23], a[224:227], v[68:71], v[20:23]\n"
"v_mfma_f32_16x16x32_bf16 v[28:31], a[228:231], v[68:71], v[28:31]\n"
"v_mfma_f32_16x16x32_bf16 v[36:39], a[232:235], v[68:71], v[36:39]\n"
"v_mfma_f32_16x16x32_bf16 v[44:47], a[236:239], v[68:71], v[44:47]\n"
"s_waitcnt lgkmcnt(2)\n"
"v_mfma_f32_16x16x32_bf16 v[80:83], v[160:163], v[48:51], v[80:83]\n"
"ds_read_b128 v[64:67], v5 offset:34816\n"
"v_mfma_f32_16x16x32_bf16 v[84:87], v[160:163], v[52:55], v[84:87]\n"
"ds_read_b128 v[68:71], v5 offset:35072\n"
"s_waitcnt lgkmcnt(3)\n"
"v_mfma_f32_16x16x32_bf16 v[80:83], v[156:159], v[56:59], v[80:83]\n"
"s_waitcnt lgkmcnt(2)\n"
"v_mfma_f32_16x16x32_bf16 v[84:87], v[156:159], v[60:63], v[84:87]\n"
"s_waitcnt lgkmcnt(1)\n"
"v_mfma_f32_16x16x32_bf16 v[80:83], v[152:155], v[64:67], v[80:83]\n"
"s_waitcnt lgkmcnt(0)\n"
"v_mfma_f32_16x16x32_bf16 v[84:87], v[152:155], v[68:71], v[84:87]\n"
"v_mfma_f32_16x16x32_bf16 v[16:19], a[240:243], v[48:51], v[16:19]\n"
"v_add_u32 v5, s41, v5\n"
"s_sub_u32 s41, 0, s41\n"
"s_mov_b32 s37, 3\n"
"v_mfma_f32_16x16x32_bf16 v[24:27], a[244:247], v[48:51], v[24:27]\n"
"v_mov_b32 v73, 1\n"
"s_mov_b32 s43, s28\n"
"s_mov_b32 s28, s42\n"
"v_mfma_f32_16x16x32_bf16 v[16:19], v[252:255], v[56:59], v[16:19]\n"
"v_cvt_pk_bf16_f32 v80, v80, v81\n"
"v_cvt_pk_bf16_f32 v81, v82, v83\n"
"v_mfma_f32_16x16x32_bf16 v[24:27], v[248:251], v[56:59], v[24:27]\n"
"v_cvt_pk_bf16_f32 v82, v84, v85\n"
"v_cvt_pk_bf16_f32 v83, v86, v87\n"
"v_mfma_f32_16x16x32_bf16 v[16:19], v[236:239], v[64:67], v[16:19]\n"
"v_permlane16_swap_b32 v80, v82\n"
"v_mfma_f32_16x16x32_bf16 v[24:27], v[232:235], v[64:67], v[24:27]\n"
"v_permlane16_swap_b32 v81, v83\n"
"v_mfma_f32_16x16x32_bf16 v[20:23], a[240:243], v[52:55], v[20:23]\n"
"buffer_store_dwordx4 v[80:83], v6, s[8:11], s57 offen sc1\n"
"v_mfma_f32_16x16x32_bf16 v[28:31], a[244:247], v[52:55], v[28:31]\n"
"s_nop 1\n"
"v_mfma_f32_16x16x32_bf16 v[20:23], v[252:255], v[60:63], v[20:23]\n"
"v_cvt_pk_bf16_f32 v16, v16, v17\n"
"v_cvt_pk_bf16_f32 v17, v18, v19\n"
"v_mfma_f32_16x16x32_bf16 v[28:31], v[248:251], v[60:63], v[28:31]\n"
"v_cvt_pk_bf16_f32 v18, v24, v25\n"
"v_cvt_pk_bf16_f32 v19, v26, v27\n"
"v_mfma_f32_16x16x32_bf16 v[20:23], v[236:239], v[68:71], v[20:23]\n"
"buffer_store_dwordx4 v[16:19], v6, s[8:11], s40 offen offset:0 sc1\n"
"v_mfma_f32_16x16x32_bf16 v[28:31], v[232:235], v[68:71], v[28:31]\n"
"v_mfma_f32_16x16x32_bf16 v[32:35], a[248:251], v[48:51], v[32:35]\n"
"s_nop 1\n"
"v_mfma_f32_16x16x32_bf16 v[40:43], a[252:255], v[48:51], v[40:43]\n"
"s_nop 1\n"
"v_mfma_f32_16x16x32_bf16 v[32:35], v[244:247], v[56:59], v[32:35]\n"
"v_cvt_pk_bf16_f32 v20, v20, v21\n"
"v_cvt_pk_bf16_f32 v21, v22, v23\n"
"v_mfma_f32_16x16x32_bf16 v[40:43], v[240:243], v[56:59], v[40:43]\n"
"v_cvt_pk_bf16_f32 v22, v28, v29\n"
"v_cvt_pk_bf16_f32 v23, v30, v31\n"
"v_mfma_f32_16x16x32_bf16 v[32:35], v[228:231], v[64:67], v[32:35]\n"
"buffer_store_dwordx4 v[20:23], v6, s[8:11], s40 offen offset:1024 sc1\n"
"v_mfma_f32_16x16x32_bf16 v[40:43], v[224:227], v[64:67], v[40:43]\n"
"v_mfma_f32_16x16x32_bf16 v[36:39], a[248:251], v[52:55], v[36:39]\n"
"s_nop 1\n"
"v_mfma_f32_16x16x32_bf16 v[44:47], a[252:255], v[52:55], v[44:47]\n"
"s_nop 1\n"
"v_mfma_f32_16x16x32_bf16 v[36:39], v[244:247], v[60:63], v[36:39]\n"
"v_cvt_pk_bf16_f32 v32, v32, v33\n"
"v_cvt_pk_bf16_f32 v33, v34, v35\n"
"v_mfma_f32_16x16x32_bf16 v[44:47], v[240:243], v[60:63], v[44:47]\n"
"v_cvt_pk_bf16_f32 v34, v40, v41\n"
"v_cvt_pk_bf16_f32 v35, v42, v43\n"
"v_mfma_f32_16x16x32_bf16 v[36:39], v[228:231], v[68:71], v[36:39]\n"
"buffer_store_dwordx4 v[32:35], v6, s[8:11], s40 offen offset:2048 sc1\n"
"v_mfma_f32_16x16x32_bf16 v[44:47], v[224:227], v[68:71], v[44:47]\n"
"s_cmp_eq_u32 s36, 1\n"
"s_cbranch_scc0 MK_KV_np\n"
"s_waitcnt vmcnt(4)\n"
"s_barrier\n"
"ds_read_b128 v[48:51], v5 offset:0\n"
"ds_read_b128 v[52:55], v5 offset:256\n"
"ds_read_b128 v[56:59], v5 offset:2048\n"
"ds_read_b128 v[60:63], v5 offset:2304\n"
"v_cvt_pk_bf16_f32 v36, v36, v37\n"
"v_cvt_pk_bf16_f32 v37, v38, v39\n"
"v_cvt_pk_bf16_f32 v38, v44, v45\n"
"v_cvt_pk_bf16_f32 v39, v46, v47\n"
"buffer_store_dwordx4 v[36:39], v6, s[8:11], s40 offen offset:3072 sc1\n"
"s_branch MK_KV_body\n"
"MK_KV_np:\n"
"s_nop 3\n"
"v_cvt_pk_bf16_f32 v36, v36, v37\n"
"v_cvt_pk_bf16_f32 v37, v38, v39\n"
"v_cvt_pk_bf16_f32 v38, v44, v45\n"
"v_cvt_pk_bf16_f32 v39, v46, v47\n"
"buffer_store_dwordx4 v[36:39], v6, s[8:11], s40 offen offset:3072 sc1\n"
"s_cmp_lt_u32 s28, 64\n"
"s_cbranch_scc1 MK_KV_wait\n"
"s_waitcnt vmcnt(0)\n"
"s_barrier\n"
"s_cmp_eq_u32 s15, 0\n"
"s_cbranch_scc0 MK_pf1\n"
"v_mov_b32 v3, 63\n"
"s_mov_b64 exec, 1\n"
"buffer_store_dword v3, off, s[8:11], s31 sc1\n"
"s_mov_b64 exec, -1\n"
"MK_pf1:\n"
"s_endpgm\n"
"MK_KV_v:\n"
"s_add_u32 s52, s52, 524288\n"
"MK_VV_wait:\n"
"s_mov_b32 s29, s30\n"
"buffer_load_dword v3, v1, s[8:11], 0 offen sc1\n"
"s_waitcnt vmcnt(0)\n"
"s_mov_b64 exec, 1\n"
"ds_add_rtn_u32 v74, v72, v73\n"
"s_mov_b64 exec, -1\n"
"s_waitcnt lgkmcnt(0)\n"
"v_and_b32 v75, 3, v74\n"
"v_cmp_eq_u32 vcc, s37, v75\n"
"v_mov_b32 v76, s43\n"
"s_nop 0\n"
"v_cndmask_b32 v75, -1, 0, vcc\n"
"buffer_store_dword v76, v75, s[8:11], s31 offen sc1\n"
"s_mov_b32 s37, 7\n"
"v_mov_b32 v73, 0\n"
"s_branch MK_VV_poll_in\n"
"MK_VV_poll:\n"
"buffer_load_dword v3, v1, s[8:11], 0 offen sc1\n"
"s_add_u32 s29, s29, 1\n"
"s_waitcnt vmcnt(0)\n"
"MK_VV_poll_in:\n"
"v_add_u32 v3, v3, v2\n"
"v_cmp_gt_i32 vcc, s28, v3\n"
"s_cbranch_vccz MK_VV_poll_ok\n"
"s_cmp_lt_u32 s29, 131072\n"
"s_cbranch_scc1 MK_VV_poll\n"
"s_mov_b32 s30, 131072\n"
"MK_VV_poll_ok:\n"
"s_add_u32 m0, s39, 0\n"
"s_add_u32 s26, s38, 0\n"
"buffer_load_dwordx4 v4, s[8:11], s26 offen sc1 lds\n"
"s_add_u32 m0, s39, 4096\n"
"s_add_u32 s26, s38, 4096\n"
"buffer_load_dwordx4 v4, s[8:11], s26 offen sc1 lds\n"
"s_add_u32 m0, s39, 8192\n"
"s_add_u32 s26, s38, 16384\n"
"buffer_load_dwordx4 v4, s[8:11], s26 offen sc1 lds\n"
"s_waitcnt vmcnt(0)\n"
"s_barrier\n"
"ds_read_b128 v[48:51], v5 offset:0\n"
"ds_read_b128 v[52:55], v5 offset:256\n"
"ds_read_b128 v[56:59], v5 offset:2048\n"
"ds_read_b128 v[60:63], v5 offset:2304\n"
"MK_VV_body:\n"
"s_waitcnt lgkmcnt(2)\n"
"v_mfma_f32_16x16x32_bf16 v[80:83], v[220:223], v[48:51], 0\n"
"ds_read_b128 v[64:67], v5 offset:4096\n"
"s_add_u32 m0, s39, 12288\n"
"s_add_u32 s26, s38, 20480\n"
"v_mfma_f32_16x16x32_bf16 v[16:19], v[48:51], a[0:3], 0\n"
"ds_read_b128 v[68:71], v5 offset:4352\n"
"s_and_b32 s40, s28, 15\n"
"s_lshl_b32 s40, s40, 22\n"
"v_mfma_f32_16x16x32_bf16 v[24:27], v[48:51], a[4:7], 0\n"
"buffer_load_dwordx4 v4, s[8:11], s26 offen sc1 lds\n"
"v_mfma_f32_16x16x32_bf16 v[32:35], v[48:51], a[8:11], 0\n"
"s_add_u32 s40, s40, 33554432\n"
"s_add_u32 s57, s40, s56\n"
"v_mfma_f32_16x16x32_bf16 v[40:43], v[48:51], a[12:15], 0\n"
"s_add_u32 s40, s40, s52\n"
"v_mfma_f32_16x16x32_bf16 v[84:87], v[220:223], v[52:55], 0\n"
"v_mfma_f32_16x16x32_bf16 v[20:23], v[52:55], a[0:3], 0\n"
"s_add_u32 m0, s39, 16384\n"
"s_add_u32 s26, s38, 32768\n"
"v_mfma_f32_16x16x32_bf16 v[28:31], v[52:55], a[4:7], 0\n"
"buffer_load_dwordx4 v4, s[8:11], s26 offen sc1 lds\n"
"v_mfma_f32_16x16x32_bf16 v[36:39], v[52:55], a[8:11], 0\n"
"v_mfma_f32_16x16x32_bf16 v[44:47], v[52:55], a[12:15], 0\n"
"s_waitcnt lgkmcnt(2)\n"
"v_mfma_f32_16x16x32_bf16 v[80:83], v[216:219], v[56:59], v[80:83]\n"
"ds_read_b128 v[48:51], v5 offset:6144\n"
"v_mfma_f32_16x16x32_bf16 v[16:19], v[56:59], a[16:19], v[16:19]\n"
"ds_read_b128 v[52:55], v5 offset:6400\n"
"s_add_u32 m0, s39, 20480\n"
"s_add_u32 s26, s38, 36864\n"
"v_mfma_f32_16x16x32_bf16 v[24:27], v[56:59], a[20:23], v[24:27]\n"
"buffer_load_dwordx4 v4, s[8:11], s26 offen sc1 lds\n"
"v_mfma_f32_16x16x32_bf16 v[32:35], v[56:59], a[24:27], v[32:35]\n"
"v_mfma_f32_16x16x32_bf16 v[40:43], v[56:59], a[28:31], v[40:43]\n"
"v_mfma_f32_16x16x32_bf16 v[84:87], v[216:219], v[60:63], v[84:87]\n"
"v_mfma_f32_16x16x32_bf16 v[20:23], v[60:63], a[16:19], v[20:23]\n"
"s_add_u32 m0, s39, 24576\n"
"s_add_u32 s26, s38, 49152\n"
"v_mfma_f32_16x16x32_bf16 v[28:31], v[60:63], a[20:23], v[28:31]\n"
"buffer_load_dwordx4 v4, s[8:11], s26 offen sc1 lds\n"
"v_mfma_f32_16x16x32_bf16 v[36:39], v[60:63], a[24:27], v[36:39]\n"
"v_mfma_f32_16x16x32_bf16 v[44:47], v[60:63], a[28:31], v[44:47]\n"
"s_waitcnt lgkmcnt(2)\n"
"v_mfma_f32_16x16x32_bf16 v[80:83], v[212:215], v[64:67], v[80:83]\n"
"ds_read_b128 v[56:59], v5 offset:8192\n"
"v_mfma_f32_16x16x32_bf16 v[16:19], v[64:67], a[32:35], v[16:19]\n"
"ds_read_b128 v[60:63], v5 offset:8448\n"
"s_add_u32 m0, s39, 28672\n"
"s_add_u32 s26, s38, 53248\n"
"v_mfma_f32_16x16x32_bf16 v[24:27], v[64:67], a[36:39], v[24:27]\n"
"buffer_load_dwordx4 v4, s[8:11], s26 offen sc1 lds\n"
"v_mfma_f32_16x16x32_bf16 v[32:35], v[64:67], a[40:43], v[32:35]\n"
"v_mfma_f32_16x16x32_bf16 v[40:43], v[64:67], a[44:47], v[40:43]\n"
"v_mfma_f32_16x16x32_bf16 v[84:87], v[212:215], v[68:71], v[84:87]\n"
"v_mfma_f32_16x16x32_bf16 v[20:23], v[68:71], a[32:35], v[20:23]\n"
"s_add_u32 m0, s39, 32768\n"
"s_add_u32 s26, s38, 65536\n"
"v_mfma_f32_16x16x32_bf16 v[28:31], v[68:71], a[36:39], v[28:31]\n"
"buffer_load_dwordx4 v4, s[8:11], s26 offen sc1 lds\n"
"v_mfma_f32_16x16x32_bf16 v[36:39], v[68:71], a[40:43], v[36:39]\n"
"v_mfma_f32_16x16x32_bf16 v[44:47], v[68:71], a[44:47], v[44:47]\n"
"s_waitcnt lgkmcnt(2)\n"
"v_mfma_f32_16x16x32_bf16 v[80:83], v[208:211], v[48:51], v[80:83]\n"
"ds_read_b128 v[64:67], v5 offset:10240\n"
"v_mfma_f32_16x16x32_bf16 v[16:19], v[48:51], a[48:51], v[16:19]\n"
"ds_read_b128 v[68:71], v5 offset:10496\n"
"v_mfma_f32_16x16x32_bf16 v[24:27], v[48:51], a[52:55], v[24:27]\n"
"v_mfma_f32_16x16x32_bf16 v[32:35], v[48:51], a[56:59], v[32:35]\n"
"v_mfma_f32_16x16x32_bf16 v[40:43], v[48:51], a[60:63], v[40:43]\n"
"v_mfma_f32_16x16x32_bf16 v[84:87], v[208:211], v[52:55], v[84:87]\n"
"v_mfma_f32_16x16x32_bf16 v[20:23], v[52:55], a[48:51], v[20:23]\n"
"v_mfma_f32_16x16x32_bf16 v[28:31], v[52:55], a[52:55], v[28:31]\n"
"v_mfma_f32_16x16x32_bf16 v[36:39], v[52:55], a[56:59], v[36:39]\n"
"v_mfma_f32_16x16x32_bf16 v[44:47], v[52:55], a[60:63], v[44:47]\n"
"s_waitcnt lgkmcnt(2)\n"
"v_mfma_f32_16x16x32_bf16 v[80:83], v[204:207], v[56:59], v[80:83]\n"
"s_waitcnt vmcnt(4)\n"
"s_barrier\n"
"v_mfma_f32_16x16x32_bf16 v[16:19], v[56:59], a[64:67], v[16:19]\n"
"ds_read_b128 v[48:51], v5 offset:12288\n"
"ds_read_b128 v[52:55], v5 offset:12544\n"
"v_mfma_f32_16x16x32_bf16 v[24:27], v[56:59], a[68:71], v[24:27]\n"
"s_mov_b64 exec, 1\n"
"ds_add_rtn_u32 v74, v72, v73\n"
"s_mov_b64 exec, -1\n"
"v_mfma_f32_16x16x32_bf16 v[32:35], v[56:59], a[72:75], v[32:35]\n"
"v_mfma_f32_16x16x32_bf16 v[40:43], v[56:59], a[76:79], v[40:43]\n"
"v_mfma_f32_16x16x32_bf16 v[84:87], v[204:207], v[60:63], v[84:87]\n"
"v_mfma_f32_16x16x32_bf16 v[20:23], v[60:63], a[64:67], v[20:23]\n"
"v_mfma_f32_16x16x32_bf16 v[28:31], v[60:63], a[68:71], v[28:31]\n"
"v_mfma_f32_16x16x32_bf16 v[36:39], v[60:63], a[72:75], v[36:39]\n"
"s_waitcnt lgkmcnt(0)\n"
"v_and_b32 v75, 3, v74\n"
"v_cmp_eq_u32 vcc, s37, v75\n"
"v_mfma_f32_16x16x32_bf16 v[44:47], v[60:63], a[76:79], v[44:47]\n"
"v_mov_b32 v76, s43\n"
"s_nop 0\n"
"v_mfma_f32_16x16x32_bf16 v[80:83], v[200:203], v[64:67], v[80:83]\n"
"ds_read_b128 v[56:59], v5 offset:14336\n"
"v_cndmask_b32 v75, -1, 0, vcc\n"
"v_mfma_f32_16x16x32_bf16 v[16:19], v[64:67], a[80:83], v[16:19]\n"
"ds_read_b128 v[60:63], v5 offset:14592\n"
"v_mfma_f32_16x16x32_bf16 v[24:27], v[64:67], a[84:87], v[24:27]\n"
"buffer_store_dword v76, v75, s[8:11], s31 offen sc1\n"
"v_mfma_f32_16x16x32_bf16 v[32:35], v[64:67], a[88:91], v[32:35]\n"
"v_mfma_f32_16x16x32_bf16 v[40:43], v[64:67], a[92:95], v[40:43]\n"
"v_mfma_f32_16x16x32_bf16 v[84:87], v[200:203], v[68:71], v[84:87]\n"
"v_mfma_f32_16x16x32_bf16 v[20:23], v[68:71], a[80:83], v[20:23]\n"
"v_mfma_f32_16x16x32_bf16 v[28:31], v[68:71], a[84:87], v[28:31]\n"
"v_mfma_f32_16x16x32_bf16 v[36:39], v[68:71], a[88:91], v[36:39]\n"
"v_mfma_f32_16x16x32_bf16 v[44:47], v[68:71], a[92:95], v[44:47]\n"
"v_mfma_f32_16x16x32_bf16 v[80:83], v[196:199], v[48:51], v[80:83]\n"
"ds_read_b128 v[64:67], v5 offset:16384\n"
"v_mfma_f32_16x16x32_bf16 v[16:19], v[48:51], a[96:99], v[16:19]\n"
"ds_read_b128 v[68:71], v5 offset:16640\n"
"v_mfma_f32_16x16x32_bf16 v[24:27], v[48:51], a[100:103], v[24:27]\n"
"v_mfma_f32_16x16x32_bf16 v[32:35], v[48:51], a[104:107], v[32:35]\n"
"v_mfma_f32_16x16x32_bf16 v[40:43], v[48:51], a[108:111], v[40:43]\n"
"v_mfma_f32_16x16x32_bf16 v[84:87], v[196:199], v[52:55], v[84:87]\n"
"v_mfma_f32_16x16x32_bf16 v[20:23], v[52:55], a[96:99], v[20:23]\n"
"v_mfma_f32_16x16x32_bf16 v[28:31], v[52:55], a[100:103], v[28:31]\n"
"v_mfma_f32_16x16x32_bf16 v[36:39], v[52:55], a[104:107], v[36:39]\n"
"v_mfma_f32_16x16x32_bf16 v[44:47], v[52:55], a[108:111], v[44:47]\n"
"s_waitcnt lgkmcnt(2)\n"
"v_mfma_f32_16x16x32_bf16 v[80:83], v[192:195], v[56:59], v[80:83]\n"
"ds_read_b128 v[48:51], v5 offset:18432\n"
"v_mfma_f32_16x16x32_bf16 v[16:19], v[56:59], a[112:115], v[16:19]\n"
"ds_read_b128 v[52:55], v5 offset:18688\n"
"v_mfma_f32_16x16x32_bf16 v[24:27], v[56:59], a[116:119], v[24:27]\n"
"v_mfma_f32_16x16x32_bf16 v[32:35], v[56:59], a[120:123], v[32:35]\n"
"v_mfma_f32_16x16x32_bf16 v[40:43], v[56:59], a[124:127], v[40:43]\n"
"v_mfma_f32_16x16x32_bf16 v[84:87], v[192:195], v[60:63], v[84:87]\n"
"v_mfma_f32_16x16x32_bf16 v[20:23], v[60:63], a[112:115], v[20:23]\n"
"v_mfma_f32_16x16x32_bf16 v[28:31], v[60:63], a[116:119], v[28:31]\n"
"v_mfma_f32_16x16x32_bf16 v[36:39], v[60:63], a[120:123], v[36:39]\n"
"v_mfma_f32_16x16x32_bf16 v[44:47], v[60:63], a[124:127], v[44:47]\n"
"s_waitcnt lgkmcnt(2)\n"
"v_mfma_f32_16x16x32_bf16 v[80:83], v[188:191], v[64:67], v[80:83]\n"
"s_waitcnt vmcnt(1)\n"
"s_barrier\n"
"v_mfma_f32_16x16x32_bf16 v[16:19], v[64:67], a[128:131], v[16:19]\n"
"ds_read_b128 v[56:59], v5 offset:20480\n"
"ds_read_b128 v[60:63], v5 offset:20736\n"
"v_mfma_f32_16x16x32_bf16 v[24:27], v[64:67], a[132:135], v[24:27]\n"
"buffer_load_dword v3, v1, s[8:11], 0 offen sc1\n"
"v_mfma_f32_16x16x32_bf16 v[32:35], v[64:67], a[136:139], v[32:35]\n"
"s_add_u32 s42, s28, 1\n"
"s_add_u32 s39, s39, s41\n"
"s_lshl_b32 s25, s42, 10\n"
"v_mfma_f32_16x16x32_bf16 v[40:43], v[64:67], a[140:143], v[40:43]\n"
"s_and_b32 s25, s25, 8192\n"
"s_xor_b32 s25, s25, s16\n"
"s_lshl_b32 s38, s42, 18\n"
"v_mfma_f32_16x16x32_bf16 v[84:87], v[188:191], v[68:71], v[84:87]\n"
"s_add_u32 s38, s38, s25\n"
"s_add_u32 s38, s38, 8388608\n"
"v_mfma_f32_16x16x32_bf16 v[20:23], v[68:71], a[128:131], v[20:23]\n"
"v_mfma_f32_16x16x32_bf16 v[28:31], v[68:71], a[132:135], v[28:31]\n"
"v_mfma_f32_16x16x32_bf16 v[36:39], v[68:71], a[136:139], v[36:39]\n"
"v_mfma_f32_16x16x32_bf16 v[44:47], v[68:71], a[140:143], v[44:47]\n"
"s_waitcnt lgkmcnt(2)\n"
"v_mfma_f32_16x16x32_bf16 v[80:83], v[184:187], v[48:51], v[80:83]\n"
"ds_read_b128 v[64:67], v5 offset:22528\n"
"v_mfma_f32_16x16x32_bf16 v[16:19], v[48:51], a[144:147], v[16:19]\n"
"ds_read_b128 v[68:71], v5 offset:22784\n"
"v_mfma_f32_16x16x32_bf16 v[24:27], v[48:51], a[148:151], v[24:27]\n"
"v_mfma_f32_16x16x32_bf16 v[32:35], v[48:51], a[152:155], v[32:35]\n"
"v_mfma_f32_16x16x32_bf16 v[40:43], v[48:51], a[156:159], v[40:43]\n"
"v_mfma_f32_16x16x32_bf16 v[84:87], v[184:187], v[52:55], v[84:87]\n"
"v_mfma_f32_16x16x32_bf16 v[20:23], v[52:55], a[144:147], v[20:23]\n"
"v_mfma_f32_16x16x32_bf16 v[28:31], v[52:55], a[148:151], v[28:31]\n"
"v_mfma_f32_16x16x32_bf16 v[36:39], v[52:55], a[152:155], v[36:39]\n"
"v_mfma_f32_16x16x32_bf16 v[44:47], v[52:55], a[156:159], v[44:47]\n"
"s_waitcnt lgkmcnt(2)\n"
"v_mfma_f32_16x16x32_bf16 v[80:83], v[180:183], v[56:59], v[80:83]\n"
"ds_read_b128 v[48:51], v5 offset:24576\n"
"v_mfma_f32_16x16x32_bf16 v[16:19], v[56:59], a[160:163], v[16:19]\n"
"ds_read_b128 v[52:55], v5 offset:24832\n"
"v_mfma_f32_16x16x32_bf16 v[24:27], v[56:59], a[164:167], v[24:27]\n"
"v_mfma_f32_16x16x32_bf16 v[32:35], v[56:59], a[168:171], v[32:35]\n"
"v_mfma_f32_16x16x32_bf16 v[40:43], v[56:59], a[172:175], v[40:43]\n"
"v_mfma_f32_16x16x32_bf16 v[84:87], v[180:183], v[60:63], v[84:87]\n"
"v_mfma_f32_16x16x32_bf16 v[20:23], v[60:63], a[160:163], v[20:23]\n"
"v_mfma_f32_16x16x32_bf16 v[28:31], v[60:63], a[164:167], v[28:31]\n"
"v_mfma_f32_16x16x32_bf16 v[36:39], v[60:63], a[168:171], v[36:39]\n"
"v_mfma_f32_16x16x32_bf16 v[44:47], v[60:63], a[172:175], v[44:47]\n"
"s_waitcnt lgkmcnt(2)\n"
"v_mfma_f32_16x16x32_bf16 v[80:83], v[176:179], v[64:67], v[80:83]\n"
"ds_read_b128 v[56:59], v5 offset:26624\n"
"ds_read_b128 v[60:63], v5 offset:26880\n"
"v_mfma_f32_16x16x32_bf16 v[16:19], v[64:67], a[176:179], v[16:19]\n"
"s_waitcnt vmcnt(0)\n"
"v_add_u32 v3, v3, v2\n"
"v_cmp_gt_i32 s[76:77], s42, v3\n"
"v_mfma_f32_16x16x32_bf16 v[24:27], v[64:67], a[180:183], v[24:27]\n"
"v_mfma_f32_16x16x32_bf16 v[32:35], v[64:67], a[184:187], v[32:35]\n"
"s_lshr_b32 s27, s42, 6\n"
"s_or_b32 s76, s76, s27\n"
"v_mfma_f32_16x16x32_bf16 v[40:43], v[64:67], a[188:191], v[40:43]\n"
"s_cmp_eq_u64 s[76:77], 0\n"
"s_cselect_b32 s36, 1, 0\n"
"s_cselect_b32 s34, s10, 0\n"
"v_mfma_f32_16x16x32_bf16 v[84:87], v[176:179], v[68:71], v[84:87]\n"
"s_add_u32 m0, s39, 0\n"
"s_add_u32 s26, s38, 0\n"
"v_mfma_f32_16x16x32_bf16 v[20:23], v[68:71], a[176:179], v[20:23]\n"
"buffer_load_dwordx4 v4, s[32:35], s26 offen sc1 lds\n"
"v_mfma_f32_16x16x32_bf16 v[28:31], v[68:71], a[180:183], v[28:31]\n"
"v_mfma_f32_16x16x32_bf16 v[36:39], v[68:71], a[184:187], v[36:39]\n"
"v_mfma_f32_16x16x32_bf16 v[44:47], v[68:71], a[188:191], v[44:47]\n"
"s_waitcnt lgkmcnt(2)\n"
"v_mfma_f32_16x16x32_bf16 v[80:83], v[172:175], v[48:51], v[80:83]\n"
"ds_read_b128 v[64:67], v5 offset:28672\n"
"s_add_u32 m0, s39, 4096\n"
"s_add_u32 s26, s38, 4096\n"
"v_mfma_f32_16x16x32_bf16 v[16:19], v[48:51], a[192:195], v[16:19]\n"
"ds_read_b128 v[68:71], v5 offset:28928\n"
"v_mfma_f32_16x16x32_bf16 v[24:27], v[48:51], a[196:199], v[24:27]\n"
"buffer_load_dwordx4 v4, s[32:35], s26 offen sc1 lds\n"
"v_mfma_f32_16x16x32_bf16 v[32:35], v[48:51], a[200:203], v[32:35]\n"
"v_mfma_f32_16x16x32_bf16 v[40:43], v[48:51], a[204:207], v[40:43]\n"
"v_mfma_f32_16x16x32_bf16 v[84:87], v[172:175], v[52:55], v[84:87]\n"
"v_mfma_f32_16x16x32_bf16 v[20:23], v[52:55], a[192:195], v[20:23]\n"
"s_add_u32 m0, s39, 8192\n"
"s_add_u32 s26, s38, 16384\n"
"v_mfma_f32_16x16x32_bf16 v[28:31], v[52:55], a[196:199], v[28:31]\n"
"buffer_load_dwordx4 v4, s[32:35], s26 offen sc1 lds\n"
"v_mfma_f32_16x16x32_bf16 v[36:39], v[52:55], a[200:203], v[36:39]\n"
"v_mfma_f32_16x16x32_bf16 v[44:47], v[52:55], a[204:207], v[44:47]\n"
"s_waitcnt lgkmcnt(2)\n"
"v_mfma_f32_16x16x32_bf16 v[80:83], v[168:171], v[56:59], v[80:83]\n"
"ds_read_b128 v[48:51], v5 offset:30720\n"
"v_mfma_f32_16x16x32_bf16 v[16:19], v[56:59], a[208:211], v[16:19]\n"
"ds_read_b128 v[52:55], v5 offset:30976\n"
"v_mfma_f32_16x16x32_bf16 v[24:27], v[56:59], a[212:215], v[24:27]\n"
"v_mfma_f32_16x16x32_bf16 v[32:35], v[56:59], a[216:219], v[32:35]\n"
"v_mfma_f32_16x16x32_bf16 v[40:43], v[56:59], a[220:223], v[40:43]\n"
"v_mfma_f32_16x16x32_bf16 v[84:87], v[168:171], v[60:63], v[84:87]\n"
"v_mfma_f32_16x16x32_bf16 v[20:23], v[60:63], a[208:211], v[20:23]\n"
"v_mfma_f32_16x16x32_bf16 v[28:31], v[60:63], a[212:215], v[28:31]\n"
"v_mfma_f32_16x16x32_bf16 v[36:39], v[60:63], a[216:219], v[36:39]\n"
"v_mfma_f32_16x16x32_bf16 v[44:47], v[60:63], a[220:223], v[44:47]\n"
"s_waitcnt lgkmcnt(2)\n"
"v_mfma_f32_16x16x32_bf16 v[80:83], v[164:167], v[64:67], v[80:83]\n"
"ds_read_b128 v[56:59], v5 offset:32768\n"
"v_mfma_f32_16x16x32_bf16 v[16:19], v[64:67], a[224:227], v[16:19]\n"
"ds_read_b128 v[60:63], v5 offset:33024\n"
"v_mfma_f32_16x16x32_bf16 v[24:27], v[64:67], a[228:231], v[24:27]\n"
"v_mfma_f32_16x16x32_bf16 v[32:35], v[64:67], a[232:235], v[32:35]\n"
"v_mfma_f32_16x16x32_bf16 v[40:43], v[64:67], a[236:239], v[40:43]\n"
"v_mfma_f32_16x16x32_bf16 v[84:87], v[164:167], v[68:71], v[84:87]\n"
"v_mfma_f32_16x16x32_bf16 v[20:23], v[68:71], a[224:227], v[20:23]\n"
"v_mfma_f32_16x16x32_bf16 v[28:31], v[68:71], a[228:231], v[28:31]\n"
"v_mfma_f32_16x16x32_bf16 v[36:39], v[68:71], a[232:235], v[36:39]\n"
"v_mfma_f32_16x16x32_bf16 v[44:47], v[68:71], a[236:239], v[44:47]\n"
"s_waitcnt lgkmcnt(2)\n"
"v_mfma_f32_16x16x32_bf16 v[80:83], v[160:163], v[48:51], v[80:83]\n"
"ds_read_b128 v[64:67], v5 offset:34816\n"
"v_mfma_f32_16x16x32_bf16 v[84:87], v[160:163], v[52:55], v[84:87]\n"
"ds_read_b128 v[68:71], v5 offset:35072\n"
"s_waitcnt lgkmcnt(3)\n"
"v_mfma_f32_16x16x32_bf16 v[80:83], v[156:159], v[56:59], v[80:83]\n"
"s_waitcnt lgkmcnt(2)\n"
"v_mfma_f32_16x16x32_bf16 v[84:87], v[156:159], v[60:63], v[84:87]\n"
"s_waitcnt lgkmcnt(1)\n"
"v_mfma_f32_16x16x32_bf16 v[80:83], v[152:155], v[64:67], v[80:83]\n"
"s_waitcnt lgkmcnt(0)\n"
"v_mfma_f32_16x16x32_bf16 v[84:87], v[152:155], v[68:71], v[84:87]\n"
"v_mfma_f32_16x16x32_bf16 v[16:19], v[48:51], a[240:243], v[16:19]\n"
"v_add_u32 v5, s41, v5\n"
"s_sub_u32 s41, 0, s41\n"
"s_mov_b32 s37, 3\n"
"v_mfma_f32_16x16x32_bf16 v[20:23], v[52:55], a[240:243], v[20:23]\n"
"v_mov_b32 v73, 1\n"
"s_mov_b32 s43, s28\n"
"s_mov_b32 s28, s42\n"
"v_mfma_f32_16x16x32_bf16 v[16:19], v[56:59], v[252:255], v[16:19]\n"
"v_cvt_pk_bf16_f32 v80, v80, v81\n"
"v_cvt_pk_bf16_f32 v81, v82, v83\n"
"v_mfma_f32_16x16x32_bf16 v[20:23], v[60:63], v[252:255], v[20:23]\n"
"v_cvt_pk_bf16_f32 v82, v84, v85\n"
"v_cvt_pk_bf16_f32 v83, v86, v87\n"
"v_mfma_f32_16x16x32_bf16 v[16:19], v[64:67], v[236:239], v[16:19]\n"
"v_permlane16_swap_b32 v80, v82\n"
"v_mfma_f32_16x16x32_bf16 v[20:23], v[68:71], v[236:239], v[20:23]\n"
"v_permlane16_swap_b32 v81, v83\n"
"v_mfma_f32_16x16x32_bf16 v[24:27], v[48:51], a[244:247], v[24:27]\n"
"buffer_store_dwordx4 v[80:83], v6, s[8:11], s57 offen sc1\n"
"v_mfma_f32_16x16x32_bf16 v[28:31], v[52:55], a[244:247], v[28:31]\n"
"s_nop 1\n"
"v_mfma_f32_16x16x32_bf16 v[24:27], v[56:59], v[248:251], v[24:27]\n"
"v_cvt_pk_bf16_f32 v16, v16, v17\n"
"v_cvt_pk_bf16_f32 v17, v18, v19\n"
"v_mfma_f32_16x16x32_bf16 v[28:31], v[60:63], v[248:251], v[28:31]\n"
"v_cvt_pk_bf16_f32 v18, v20, v21\n"
"v_cvt_pk_bf16_f32 v19, v22, v23\n"
"v_mfma_f32_16x16x32_bf16 v[24:27], v[64:67], v[232:235], v[24:27]\n"
"buffer_store_dwordx4 v[16:19], v6, s[8:11], s40 offen offset:0 sc1\n"
"v_mfma_f32_16x16x32_bf16 v[28:31], v[68:71], v[232:235], v[28:31]\n"
"v_mfma_f32_16x16x32_bf16 v[32:35], v[48:51], a[248:251], v[32:35]\n"
"s_nop 1\n"
"v_mfma_f32_16x16x32_bf16 v[36:39], v[52:55], a[248:251], v[36:39]\n"
"s_nop 1\n"
"v_mfma_f32_16x16x32_bf16 v[32:35], v[56:59], v[244:247], v[32:35]\n"
"v_cvt_pk_bf16_f32 v24, v24, v25\n"
"v_cvt_pk_bf16_f32 v25, v26, v27\n"
"v_mfma_f32_16x16x32_bf16 v[36:39], v[60:63], v[244:247], v[36:39]\n"
"v_cvt_pk_bf16_f32 v26, v28, v29\n"
"v_cvt_pk_bf16_f32 v27, v30, v31\n"
"v_mfma_f32_16x16x32_bf16 v[32:35], v[64:67], v[228:231], v[32:35]\n"
"buffer_store_dwordx4 v[24:27], v6, s[8:11], s40 offen offset:1024 sc1\n"
"v_mfma_f32_16x16x32_bf16 v[36:39], v[68:71], v[228:231], v[36:39]\n"
"v_mfma_f32_16x16x32_bf16 v[40:43], v[48:51], a[252:255], v[40:43]\n"
"s_nop 1\n"
"v_mfma_f32_16x16x32_bf16 v[44:47], v[52:55], a[252:255], v[44:47]\n"
"s_nop 1\n"
"v_mfma_f32_16x16x32_bf16 v[40:43], v[56:59], v[240:243], v[40:43]\n"
"v_cvt_pk_bf16_f32 v32, v32, v33\n"
"v_cvt_pk_bf16_f32 v33, v34, v35\n"
"v_mfma_f32_16x16x32_bf16 v[44:47], v[60:63], v[240:243], v[44:47]\n"
"v_cvt_pk_bf16_f32 v34, v36, v37\n"
"v_cvt_pk_bf16_f32 v35, v38, v39\n"
"v_mfma_f32_16x16x32_bf16 v[40:43], v[64:67], v[224:227], v[40:43]\n"
"buffer_store_dwordx4 v[32:35], v6, s[8:11], s40 offen offset:2048 sc1\n"
"v_mfma_f32_16x16x32_bf16 v[44:47], v[68:71], v[224:227], v[44:47]\n"
"s_cmp_eq_u32 s36, 1\n"
"s_cbranch_scc0 MK_VV_np\n"
"s_waitcnt vmcnt(4)\n"
"s_barrier\n"
"ds_read_b128 v[48:51], v5 offset:0\n"
"ds_read_b128 v[52:55], v5 offset:256\n"
"ds_read_b128 v[56:59], v5 offset:2048\n"
"ds_read_b128 v[60:63], v5 offset:2304\n"
"v_cvt_pk_bf16_f32 v40, v40, v41\n"
"v_cvt_pk_bf16_f32 v41, v42, v43\n"
"v_cvt_pk_bf16_f32 v42, v44, v45\n"
"v_cvt_pk_bf16_f32 v43, v46, v47\n"
"buffer_store_dwordx4 v[40:43], v6, s[8:11], s40 offen offset:3072 sc1\n"
"s_branch MK_VV_body\n"
"MK_VV_np:\n"
"s_nop 3\n"
"v_cvt_pk_bf16_f32 v40, v40, v41\n"
"v_cvt_pk_bf16_f32 v41, v42, v43\n"
"v_cvt_pk_bf16_f32 v42, v44, v45\n"
"v_cvt_pk_bf16_f32 v43, v46, v47\n"
"buffer_store_dwordx4 v[40:43], v6, s[8:11], s40 offen offset:3072 sc1\n"
"s_cmp_lt_u32 s28, 64\n"
"s_cbranch_scc1 MK_VV_wait\n"
"s_waitcnt vmcnt(0)\n"
"s_barrier\n"
"s_cmp_eq_u32 s15, 0\n"
"s_cbranch_scc0 MK_pf2\n"
"v_mov_b32 v3, 63\n"
"s_mov_b64 exec, 1\n"
"buffer_store_dword v3, off, s[8:11], s31 sc1\n"
"s_mov_b64 exec, -1\n"
"MK_pf2:\n"
"s_endpgm\n"
"MK_Q:\n"
"s_memrealtime s[18:19]\n"
"s_mul_i32 s24, s12, 500\n"
"s_add_u32 s24, s24, 0\n"
"s_waitcnt lgkmcnt(0)\n"
"s_add_u32 s24, s24, s18\n"
"MK_Q_dly:\n"
"s_memrealtime s[18:19]\n"
"s_waitcnt lgkmcnt(0)\n"
"s_sub_u32 s25, s24, s18\n"
"s_cmp_gt_i32 s25, 0\n"
"s_cbranch_scc0 MK_Q_dlx\n"
"s_sleep 4\n"
"s_branch MK_Q_dly\n"
"MK_Q_dlx:\n"
"s_load_dwordx2 s[44:45], s[6:7], 16\n"
"s_mov_b32 s46, 4718592\n"
"s_mov_b32 s47, 131072\n"
"s_waitcnt lgkmcnt(0)\n"
"s_and_b32 s45, s45, 65535\n"
"v_mbcnt_lo_u32_b32 v0, -1, 0\n"
"v_mbcnt_hi_u32_b32 v0, -1, v0\n"
"s_lshl_b32 s48, s14, 9\n"
"s_lshr_b32 s49, s15, 1\n"
"s_mul_i32 s49, s49, 73984\n"
"v_bfe_u32 v18, v0, 5, 1\n"
"v_mul_u32_u24 v16, 262144, v18\n"
"v_bfe_u32 v18, v0, 4, 1\n"
"v_lshl_add_u32 v16, v18, 17, v16\n"
"v_bfe_u32 v18, v0, 3, 1\n"
"v_lshl_add_u32 v16, v18, 15, v16\n"
"v_bfe_u32 v18, v0, 0, 3\n"
"v_lshl_add_u32 v16, v18, 4, v16\n"
"s_lshr_b32 s24, s15, 1\n"
"s_lshl_b32 s24, s24, 16\n"
"s_and_b32 s25, s15, 1\n"
"s_lshl_b32 s25, s25, 14\n"
"s_add_u32 s24, s24, s25\n"
"s_add_u32 s60, s24, s48\n"
"s_add_u32 s61, s60, 8192\n"
"s_mul_i32 s62, s15, 18496\n"
"s_add_u32 m0, s62, 0\n"
"s_add_u32 s24, s60, 0\n"
"buffer_load_dwordx4 v16, s[44:47], s24 offen lds\n"
"s_add_u32 m0, s62, 1024\n"
"s_add_u32 s24, s60, 524288\n"
"buffer_load_dwordx4 v16, s[44:47], s24 offen lds\n"
"s_add_u32 m0, s62, 2048\n"
"s_add_u32 s24, s60, 1048576\n"
"buffer_load_dwordx4 v16, s[44:47], s24 offen lds\n"
"s_add_u32 m0, s62, 3072\n"
"s_add_u32 s24, s60, 1572864\n"
"buffer_load_dwordx4 v16, s[44:47], s24 offen lds\n"
"s_add_u32 m0, s62, 4096\n"
"s_add_u32 s24, s60, 2097152\n"
"buffer_load_dwordx4 v16, s[44:47], s24 offen lds\n"
"s_add_u32 m0, s62, 5120\n"
"s_add_u32 s24, s60, 2621440\n"
"buffer_load_dwordx4 v16, s[44:47], s24 offen lds\n"
"s_add_u32 m0, s62, 6144\n"
"s_add_u32 s24, s60, 3145728\n"
"buffer_load_dwordx4 v16, s[44:47], s24 offen lds\n"
"s_add_u32 m0, s62, 7168\n"
"s_add_u32 s24, s60, 3670016\n"
"buffer_load_dwordx4 v16, s[44:47], s24 offen lds\n"
"s_add_u32 m0, s62, 8192\n"
"s_add_u32 s24, s60, 4194304\n"
"buffer_load_dwordx4 v16, s[44:47], s24 offen lds\n"
"s_add_u32 m0, s62, 9248\n"
"s_add_u32 s24, s61, 0\n"
"buffer_load_dwordx4 v16, s[44:47], s24 offen lds\n"
"s_add_u32 m0, s62, 10272\n"
"s_add_u32 s24, s61, 524288\n"
"buffer_load_dwordx4 v16, s[44:47], s24 offen lds\n"
"s_add_u32 m0, s62, 11296\n"
"s_add_u32 s24, s61, 1048576\n"
"buffer_load_dwordx4 v16, s[44:47], s24 offen lds\n"
"s_add_u32 m0, s62, 12320\n"
"s_add_u32 s24, s61, 1572864\n"
"buffer_load_dwordx4 v16, s[44:47], s24 offen lds\n"
"s_add_u32 m0, s62, 13344\n"
"s_add_u32 s24, s61, 2097152\n"
"buffer_load_dwordx4 v16, s[44:47], s24 offen lds\n"
"s_add_u32 m0, s62, 14368\n"
"s_add_u32 s24, s61, 2621440\n"
"buffer_load_dwordx4 v16, s[44:47], s24 offen lds\n"
"s_add_u32 m0, s62, 15392\n"
"s_add_u32 s24, s61, 3145728\n"
"buffer_load_dwordx4 v16, s[44:47], s24 offen lds\n"
"s_add_u32 m0, s62, 16416\n"
"s_add_u32 s24, s61, 3670016\n"
"buffer_load_dwordx4 v16, s[44:47], s24 offen lds\n"
"s_add_u32 m0, s62, 17440\n"
"s_add_u32 s24, s61, 4194304\n"
"buffer_load_dwordx4 v16, s[44:47], s24 offen lds\n"
"s_add_u32 m0, s62, 73984\n"
"s_add_u32 s24, s60, 256\n"
"buffer_load_dwordx4 v16, s[44:47], s24 offen lds\n"
"s_add_u32 m0, s62, 75008\n"
"s_add_u32 s24, s60, 524544\n"
"buffer_load_dwordx4 v16, s[44:47], s24 offen lds\n"
"s_add_u32 m0, s62, 76032\n"
"s_add_u32 s24, s60, 1048832\n"
"buffer_load_dwordx4 v16, s[44:47], s24 offen lds\n"
"s_add_u32 m0, s62, 77056\n"
"s_add_u32 s24, s60, 1573120\n"
"buffer_load_dwordx4 v16, s[44:47], s24 offen lds\n"
"s_add_u32 m0, s62, 78080\n"
"s_add_u32 s24, s60, 2097408\n"
"buffer_load_dwordx4 v16, s[44:47], s24 offen lds\n"
"s_add_u32 m0, s62, 79104\n"
"s_add_u32 s24, s60, 2621696\n"
"buffer_load_dwordx4 v16, s[44:47], s24 offen lds\n"
"s_add_u32 m0, s62, 80128\n"
"s_add_u32 s24, s60, 3145984\n"
"buffer_load_dwordx4 v16, s[44:47], s24 offen lds\n"
"s_add_u32 m0, s62, 81152\n"
"s_add_u32 s24, s60, 3670272\n"
"buffer_load_dwordx4 v16, s[44:47], s24 offen lds\n"
"s_add_u32 m0, s62, 82176\n"
"s_add_u32 s24, s60, 4194560\n"
"buffer_load_dwordx4 v16, s[44:47], s24 offen lds\n"
"s_add_u32 m0, s62, 83232\n"
"s_add_u32 s24, s61, 256\n"
"buffer_load_dwordx4 v16, s[44:47], s24 offen lds\n"
"s_add_u32 m0, s62, 84256\n"
"s_add_u32 s24, s61, 524544\n"
"buffer_load_dwordx4 v16, s[44:47], s24 offen lds\n"
"s_add_u32 m0, s62, 85280\n"
"s_add_u32 s24, s61, 1048832\n"
"buffer_load_dwordx4 v16, s[44:47], s24 offen lds\n"
"s_add_u32 m0, s62, 86304\n"
"s_add_u32 s24, s61, 1573120\n"
"buffer_load_dwordx4 v16, s[44:47], s24 offen lds\n"
"s_add_u32 m0, s62, 87328\n"
"s_add_u32 s24, s61, 2097408\n"
"buffer_load_dwordx4 v16, s[44:47], s24 offen lds\n"
"s_add_u32 m0, s62, 88352\n"
"s_add_u32 s24, s61, 2621696\n"
"buffer_load_dwordx4 v16, s[44:47], s24 offen lds\n"
"s_add_u32 m0, s62, 89376\n"
"s_add_u32 s24, s61, 3145984\n"
"buffer_load_dwordx4 v16, s[44:47], s24 offen lds\n"
"s_add_u32 m0, s62, 90400\n"
"s_add_u32 s24, s61, 3670272\n"
"buffer_load_dwordx4 v16, s[44:47], s24 offen lds\n"
"s_add_u32 m0, s62, 91424\n"
"s_add_u32 s24, s61, 4194560\n"
"buffer_load_dwordx4 v16, s[44:47], s24 offen lds\n"
"v_bfe_u32 v18, v0, 4, 1\n"
"v_mul_u32_u24 v17, 36992, v18\n"
"v_bfe_u32 v18, v0, 2, 2\n"
"v_mul_u32_u24 v18, 9248, v18\n"
"v_add_u32 v17, v17, v18\n"
"v_bfe_u32 v18, v0, 5, 1\n"
"v_lshl_add_u32 v17, v18, 8, v17\n"
"v_bfe_u32 v18, v0, 0, 2\n"
"v_lshl_add_u32 v17, v18, 4, v17\n"
"v_add_u32 v17, s49, v17\n"
"s_waitcnt vmcnt(0)\n"
"s_barrier\n"
"ds_read_b64_tr_b16 a[0:1], v17 offset:0\n"
"ds_read_b64_tr_b16 a[2:3], v17 offset:128\n"
"ds_read_b64_tr_b16 a[4:5], v17 offset:8\n"
"ds_read_b64_tr_b16 a[6:7], v17 offset:136\n"
"ds_read_b64_tr_b16 a[8:9], v17 offset:64\n"
"ds_read_b64_tr_b16 a[10:11], v17 offset:192\n"
"ds_read_b64_tr_b16 a[12:13], v17 offset:72\n"
"ds_read_b64_tr_b16 a[14:15], v17 offset:200\n"
"ds_read_b64_tr_b16 a[16:17], v17 offset:512\n"
"ds_read_b64_tr_b16 a[18:19], v17 offset:640\n"
"ds_read_b64_tr_b16 a[20:21], v17 offset:520\n"
"ds_read_b64_tr_b16 a[22:23], v17 offset:648\n"
"ds_read_b64_tr_b16 a[24:25], v17 offset:576\n"
"ds_read_b64_tr_b16 a[26:27], v17 offset:704\n"
"ds_read_b64_tr_b16 a[28:29], v17 offset:584\n"
"ds_read_b64_tr_b16 a[30:31], v17 offset:712\n"
"ds_read_b64_tr_b16 a[32:33], v17 offset:1024\n"
"ds_read_b64_tr_b16 a[34:35], v17 offset:1152\n"
"ds_read_b64_tr_b16 a[36:37], v17 offset:1032\n"
"ds_read_b64_tr_b16 a[38:39], v17 offset:1160\n"
"ds_read_b64_tr_b16 a[40:41], v17 offset:1088\n"
"ds_read_b64_tr_b16 a[42:43], v17 offset:1216\n"
"ds_read_b64_tr_b16 a[44:45], v17 offset:1096\n"
"ds_read_b64_tr_b16 a[46:47], v17 offset:1224\n"
"ds_read_b64_tr_b16 a[48:49], v17 offset:1536\n"
"ds_read_b64_tr_b16 a[50:51], v17 offset:1664\n"
"ds_read_b64_tr_b16 a[52:53], v17 offset:1544\n"
"ds_read_b64_tr_b16 a[54:55], v17 offset:1672\n"
"ds_read_b64_tr_b16 a[56:57], v17 offset:1600\n"
"ds_read_b64_tr_b16 a[58:59], v17 offset:1728\n"
"ds_read_b64_tr_b16 a[60:61], v17 offset:1608\n"
"ds_read_b64_tr_b16 a[62:63], v17 offset:1736\n"
"ds_read_b64_tr_b16 a[64:65], v17 offset:2048\n"
"ds_read_b64_tr_b16 a[66:67], v17 offset:2176\n"
"ds_read_b64_tr_b16 a[68:69], v17 offset:2056\n"
"ds_read_b64_tr_b16 a[70:71], v17 offset:2184\n"
"ds_read_b64_tr_b16 a[72:73], v17 offset:2112\n"
"ds_read_b64_tr_b16 a[74:75], v17 offset:2240\n"
"ds_read_b64_tr_b16 a[76:77], v17 offset:2120\n"
"ds_read_b64_tr_b16 a[78:79], v17 offset:2248\n"
"ds_read_b64_tr_b16 a[80:81], v17 offset:2560\n"
"ds_read_b64_tr_b16 a[82:83], v17 offset:2688\n"
"ds_read_b64_tr_b16 a[84:85], v17 offset:2568\n"
"ds_read_b64_tr_b16 a[86:87], v17 offset:2696\n"
"ds_read_b64_tr_b16 a[88:89], v17 offset:2624\n"
"ds_read_b64_tr_b16 a[90:91], v17 offset:2752\n"
"ds_read_b64_tr_b16 a[92:93], v17 offset:2632\n"
"ds_read_b64_tr_b16 a[94:95], v17 offset:2760\n"
"ds_read_b64_tr_b16 a[96:97], v17 offset:3072\n"
"ds_read_b64_tr_b16 a[98:99], v17 offset:3200\n"
"ds_read_b64_tr_b16 a[100:101], v17 offset:3080\n"
"ds_read_b64_tr_b16 a[102:103], v17 offset:3208\n"
"ds_read_b64_tr_b16 a[104:105], v17 offset:3136\n"
"ds_read_b64_tr_b16 a[106:107], v17 offset:3264\n"
"ds_read_b64_tr_b16 a[108:109], v17 offset:3144\n"
"ds_read_b64_tr_b16 a[110:111], v17 offset:3272\n"
"ds_read_b64_tr_b16 a[112:113], v17 offset:3584\n"
"ds_read_b64_tr_b16 a[114:115], v17 offset:3712\n"
"ds_read_b64_tr_b16 a[116:117], v17 offset:3592\n"
"ds_read_b64_tr_b16 a[118:119], v17 offset:3720\n"
"ds_read_b64_tr_b16 a[120:121], v17 offset:3648\n"
"ds_read_b64_tr_b16 a[122:123], v17 offset:3776\n"
"ds_read_b64_tr_b16 a[124:125], v17 offset:3656\n"
"ds_read_b64_tr_b16 a[126:127], v17 offset:3784\n"
"ds_read_b64_tr_b16 a[128:129], v17 offset:4096\n"
"ds_read_b64_tr_b16 a[130:131], v17 offset:4224\n"
"ds_read_b64_tr_b16 a[132:133], v17 offset:4104\n"
"ds_read_b64_tr_b16 a[134:135], v17 offset:4232\n"
"ds_read_b64_tr_b16 a[136:137], v17 offset:4160\n"
"ds_read_b64_tr_b16 a[138:139], v17 offset:4288\n"
"ds_read_b64_tr_b16 a[140:141], v17 offset:4168\n"
"ds_read_b64_tr_b16 a[142:143], v17 offset:4296\n"
"ds_read_b64_tr_b16 a[144:145], v17 offset:4608\n"
"ds_read_b64_tr_b16 a[146:147], v17 offset:4736\n"
"ds_read_b64_tr_b16 a[148:149], v17 offset:4616\n"
"ds_read_b64_tr_b16 a[150:151], v17 offset:4744\n"
"ds_read_b64_tr_b16 a[152:153], v17 offset:4672\n"
"ds_read_b64_tr_b16 a[154:155], v17 offset:4800\n"
"ds_read_b64_tr_b16 a[156:157], v17 offset:4680\n"
"ds_read_b64_tr_b16 a[158:159], v17 offset:4808\n"
"ds_read_b64_tr_b16 a[160:161], v17 offset:5120\n"
"ds_read_b64_tr_b16 a[162:163], v17 offset:5248\n"
"ds_read_b64_tr_b16 a[164:165], v17 offset:5128\n"
"ds_read_b64_tr_b16 a[166:167], v17 offset:5256\n"
"ds_read_b64_tr_b16 a[168:169], v17 offset:5184\n"
"ds_read_b64_tr_b16 a[170:171], v17 offset:5312\n"
"ds_read_b64_tr_b16 a[172:173], v17 offset:5192\n"
"ds_read_b64_tr_b16 a[174:175], v17 offset:5320\n"
"ds_read_b64_tr_b16 a[176:177], v17 offset:5632\n"
"ds_read_b64_tr_b16 a[178:179], v17 offset:5760\n"
"ds_read_b64_tr_b16 a[180:181], v17 offset:5640\n"
"ds_read_b64_tr_b16 a[182:183], v17 offset:5768\n"
"ds_read_b64_tr_b16 a[184:185], v17 offset:5696\n"
"ds_read_b64_tr_b16 a[186:187], v17 offset:5824\n"
"ds_read_b64_tr_b16 a[188:189], v17 offset:5704\n"
"ds_read_b64_tr_b16 a[190:191], v17 offset:5832\n"
"ds_read_b64_tr_b16 a[192:193], v17 offset:6144\n"
"ds_read_b64_tr_b16 a[194:195], v17 offset:6272\n"
"ds_read_b64_tr_b16 a[196:197], v17 offset:6152\n"
"ds_read_b64_tr_b16 a[198:199], v17 offset:6280\n"
"ds_read_b64_tr_b16 a[200:201], v17 offset:6208\n"
"ds_read_b64_tr_b16 a[202:203], v17 offset:6336\n"
"ds_read_b64_tr_b16 a[204:205], v17 offset:6216\n"
"ds_read_b64_tr_b16 a[206:207], v17 offset:6344\n"
"ds_read_b64_tr_b16 a[208:209], v17 offset:6656\n"
"ds_read_b64_tr_b16 a[210:211], v17 offset:6784\n"
"ds_read_b64_tr_b16 a[212:213], v17 offset:6664\n"
"ds_read_b64_tr_b16 a[214:215], v17 offset:6792\n"
"ds_read_b64_tr_b16 a[216:217], v17 offset:6720\n"
"ds_read_b64_tr_b16 a[218:219], v17 offset:6848\n"
"ds_read_b64_tr_b16 a[220:221], v17 offset:6728\n"
"ds_read_b64_tr_b16 a[222:223], v17 offset:6856\n"
"ds_read_b64_tr_b16 a[224:225], v17 offset:7168\n"
"ds_read_b64_tr_b16 a[226:227], v17 offset:7296\n"
"ds_read_b64_tr_b16 a[228:229], v17 offset:7176\n"
"ds_read_b64_tr_b16 a[230:231], v17 offset:7304\n"
"ds_read_b64_tr_b16 a[232:233], v17 offset:7232\n"
"ds_read_b64_tr_b16 a[234:235], v17 offset:7360\n"
"ds_read_b64_tr_b16 a[236:237], v17 offset:7240\n"
"ds_read_b64_tr_b16 a[238:239], v17 offset:7368\n"
"ds_read_b64_tr_b16 a[240:241], v17 offset:7680\n"
"ds_read_b64_tr_b16 a[242:243], v17 offset:7808\n"
"ds_read_b64_tr_b16 a[244:245], v17 offset:7688\n"
"ds_read_b64_tr_b16 a[246:247], v17 offset:7816\n"
"ds_read_b64_tr_b16 a[248:249], v17 offset:7744\n"
"ds_read_b64_tr_b16 a[250:251], v17 offset:7872\n"
"ds_read_b64_tr_b16 a[252:253], v17 offset:7752\n"
"ds_read_b64_tr_b16 a[254:255], v17 offset:7880\n"
"ds_read_b64_tr_b16 v[252:253], v17 offset:8192\n"
"ds_read_b64_tr_b16 v[254:255], v17 offset:8320\n"
"ds_read_b64_tr_b16 v[248:249], v17 offset:8200\n"
"ds_read_b64_tr_b16 v[250:251], v17 offset:8328\n"
"ds_read_b64_tr_b16 v[244:245], v17 offset:8256\n"
"ds_read_b64_tr_b16 v[246:247], v17 offset:8384\n"
"ds_read_b64_tr_b16 v[240:241], v17 offset:8264\n"
"ds_read_b64_tr_b16 v[242:243], v17 offset:8392\n"
"ds_read_b64_tr_b16 v[236:237], v17 offset:8704\n"
"ds_read_b64_tr_b16 v[238:239], v17 offset:8832\n"
"ds_read_b64_tr_b16 v[232:233], v17 offset:8712\n"
"ds_read_b64_tr_b16 v[234:235], v17 offset:8840\n"
"ds_read_b64_tr_b16 v[228:229], v17 offset:8768\n"
"ds_read_b64_tr_b16 v[230:231], v17 offset:8896\n"
"ds_read_b64_tr_b16 v[224:225], v17 offset:8776\n"
"ds_read_b64_tr_b16 v[226:227], v17 offset:8904\n"
"s_waitcnt lgkmcnt(0)\n"
"s_barrier\n"
"s_lshl_b32 s39, s15, 10\n"
"v_lshlrev_b32 v4, 4, v0\n"
"v_add_u32 v4, s39, v4\n"
"v_bfe_u32 v3, v0, 4, 2\n"
"v_mul_u32_u24 v5, 512, v3\n"
"v_bfe_u32 v3, v0, 0, 4\n"
"v_lshl_add_u32 v5, v3, 4, v5\n"
"s_and_b32 s24, s15, 1\n"
"s_lshl_b32 s24, s24, 8\n"
"v_add_u32 v5, s24, v5\n"
"v_xor_b32 v6, 256, v5\n"
"s_mov_b64 s[32:33], s[8:9]\n"
"s_mov_b32 s35, s11\n"
"s_mov_b32 s36, 0\n"
"s_mov_b32 s37, 7\n"
"s_mov_b32 s28, 0\n"
"s_mov_b32 s41, 36864\n"
"s_mov_b32 s43, -1\n"
"v_mov_b32 v80, 163776\n"
"v_mov_b32 v81, 0\n"
"v_mov_b32 v82, 0\n"
"ds_write_b32 v80, v81\n"
"s_waitcnt lgkmcnt(0)\n"
"s_barrier\n"
"s_add_u32 s31, s14, 64\n"
"s_lshr_b32 s27, s31, 6\n"
"s_lshl_b32 s27, s27, 13\n"
"s_lshl_b32 s31, s31, 7\n"
"s_add_u32 s31, s31, s27\n"
"s_add_u32 s31, s31, s16\n"
"s_add_u32 s31, s31, 1835008\n"
"s_cmp_eq_u32 s12, 0\n"
"s_cselect_b32 s24, 7, 3\n"
"v_lshrrev_b32 v17, 2, v0\n"
"v_and_b32 v17, s24, v17\n"
"v_add_u32 v17, 128, v17\n"
"s_lshl_b32 s24, s14, 3\n"
"v_bfe_u32 v16, v0, 1, 3\n"
"v_add_u32 v16, s24, v16\n"
"v_add_u32 v16, 136, v16\n"
"s_mov_b32 s70, 0\n"
"s_mov_b32 s71, 0xffff0000\n"
"v_cndmask_b32 v17, v17, v16, s[70:71]\n"
"s_lshl_b32 s24, s14, 1\n"
"v_bfe_u32 v16, v0, 3, 1\n"
"v_add_u32 v16, s24, v16\n"
"v_add_u32 v16, 32, v16\n"
"s_mov_b32 s71, 65535\n"
"v_cndmask_b32 v17, v17, v16, s[70:71]\n"
"v_lshrrev_b32 v16, 6, v17\n"
"v_lshlrev_b32 v16, 13, v16\n"
"v_lshl_add_u32 v1, v17, 7, v16\n"
"v_add_u32 v1, 1835008, v1\n"
"v_add_u32 v1, s16, v1\n"
"s_mov_b32 s71, -1\n"
"v_cndmask_b32 v2, -1, 0, s[70:71]\n"
"s_mov_b32 s64, -1\n"
"s_mov_b32 s65, 65535\n"
"s_mov_b32 s66, 0\n"
"s_mov_b32 s67, 0xffff0000\n"
"s_mov_b32 s68, 0x3e0293ee\n"
"s_mov_b32 s69, 0x3e0293ee\n"
"s_lshl_b32 s51, s15, 10\n"
"s_lshl_b32 s52, s14, 15\n"
"s_add_u32 s52, s52, s16\n"
"s_lshr_b32 s24, s14, 1\n"
"s_lshl_b32 s24, s24, 13\n"
"s_lshl_b32 s53, s14, 12\n"
"s_add_u32 s53, s53, s24\n"
"s_add_u32 s53, s53, s16\n"
"s_add_u32 s53, s53, 1835008\n"
"s_lshl_b32 s54, s14, 14\n"
"s_add_u32 s54, s54, s16\n"
"s_add_u32 s54, s54, 1572864\n"
"s_lshr_b32 s25, s15, 1\n"
"s_and_b32 s26, s15, 1\n"
"s_lshl_b32 s24, s25, 14\n"
"s_add_u32 s55, s52, s24\n"
"s_add_u32 s55, s55, 1048576\n"
"v_mov_b32 v7, v5\n"
"v_lshlrev_b32 v17, 4, v0\n"
"s_lshl_b32 s24, s25, 13\n"
"s_add_u32 s24, s24, 73728\n"
"v_add_u32 v8, s24, v17\n"
"s_lshl_b32 s24, s25, 11\n"
"s_add_u32 s24, s24, 106496\n"
"v_bfe_u32 v16, v0, 4, 2\n"
"v_mul_u32_u24 v10, 128, v16\n"
"v_bfe_u32 v16, v0, 0, 3\n"
"v_lshl_add_u32 v10, v16, 4, v10\n"
"v_add_u32 v10, s24, v10\n"
"s_lshl_b32 s24, s25, 12\n"
"s_add_u32 s24, s24, 110592\n"
"v_add_u32 v13, s24, v17\n"
"s_add_u32 s24, s24, 8192\n"
"v_add_u32 v15, s24, v5\n"
"s_lshl_b32 s24, s26, 4\n"
"v_and_b32 v16, 15, v0\n"
"v_add_u32 v16, s24, v16\n"
"v_lshrrev_b32 v17, 4, v0\n"
"v_lshlrev_b32 v17, 2, v17\n"
"v_sub_u32 v16, v16, v17\n"
"v_mov_b32 v17, 0xf149f2ca\n"
"v_cmp_gt_i32 vcc, 0, v16\n"
"s_nop 1\n"
"v_cndmask_b32 v72, 0, v17, vcc\n"
"v_cmp_gt_i32 vcc, 1, v16\n"
"s_nop 1\n"
"v_cndmask_b32 v73, 0, v17, vcc\n"
"v_cmp_gt_i32 vcc, 2, v16\n"
"s_nop 1\n"
"v_cndmask_b32 v74, 0, v17, vcc\n"
"v_cmp_gt_i32 vcc, 3, v16\n"
"s_nop 1\n"
"v_cndmask_b32 v75, 0, v17, vcc\n"
"v_cmp_gt_i32 vcc, 16, v16\n"
"s_nop 1\n"
"v_cndmask_b32 v76, 0, v17, vcc\n"
"v_cmp_gt_i32 vcc, 17, v16\n"
"s_nop 1\n"
"v_cndmask_b32 v77, 0, v17, vcc\n"
"v_cmp_gt_i32 vcc, 18, v16\n"
"s_nop 1\n"
"v_cndmask_b32 v78, 0, v17, vcc\n"
"v_cmp_gt_i32 vcc, 19, v16\n"
"s_nop 1\n"
"v_cndmask_b32 v79, 0, v17, vcc\n"
"s_mov_b32 s29, s30\n"
"MK_Qp0:\n"
"buffer_load_dword v3, v1, s[8:11], 0 offen sc1\n"
"s_add_u32 s29, s29, 1\n"
"s_waitcnt vmcnt(0)\n"
"v_add_u32 v3, v3, v2\n"
"v_cmp_gt_i32 vcc, -1, v3\n"
"s_and_b64 vcc, vcc, s[64:65]\n"
"s_cbranch_vccz MK_Qp0_ok\n"
"s_cmp_lt_u32 s29, 131072\n"
"s_cbranch_scc1 MK_Qp0\n"
"s_mov_b32 s30, 131072\n"
"MK_Qp0_ok:\n"
"s_lshl_b32 s24, s28, 10\n"
"s_and_b32 s24, s24, 8192\n"
"s_xor_b32 s24, s24, s16\n"
"s_lshl_b32 s38, s28, 18\n"
"s_add_u32 s38, s38, s24\n"
"s_add_u32 s38, s38, 8388608\n"
"s_add_u32 m0, s39, 0\n"
"s_add_u32 s26, s38, 0\n"
"buffer_load_dwordx4 v4, s[8:11], s26 offen sc1 lds\n"
"s_add_u32 m0, s39, 4096\n"
"s_add_u32 s26, s38, 4096\n"
"buffer_load_dwordx4 v4, s[8:11], s26 offen sc1 lds\n"
"s_add_u32 m0, s39, 8192\n"
"s_add_u32 s26, s38, 16384\n"
"buffer_load_dwordx4 v4, s[8:11], s26 offen sc1 lds\n"
"s_add_u32 m0, s39, 12288\n"
"s_add_u32 s26, s38, 20480\n"
"buffer_load_dwordx4 v4, s[8:11], s26 offen sc1 lds\n"
"s_add_u32 m0, s39, 16384\n"
"s_add_u32 s26, s38, 32768\n"
"buffer_load_dwordx4 v4, s[8:11], s26 offen sc1 lds\n"
"s_add_u32 m0, s39, 20480\n"
"s_add_u32 s26, s38, 36864\n"
"buffer_load_dwordx4 v4, s[8:11], s26 offen sc1 lds\n"
"s_add_u32 m0, s39, 24576\n"
"s_add_u32 s26, s38, 49152\n"
"buffer_load_dwordx4 v4, s[8:11], s26 offen sc1 lds\n"
"s_add_u32 m0, s39, 28672\n"
"s_add_u32 s26, s38, 53248\n"
"buffer_load_dwordx4 v4, s[8:11], s26 offen sc1 lds\n"
"s_add_u32 m0, s39, 32768\n"
"s_add_u32 s26, s38, 65536\n"
"buffer_load_dwordx4 v4, s[8:11], s26 offen sc1 lds\n"
"s_mov_b32 s57, 0\n"
"MK_Qloop:\n"
"s_cmp_eq_u32 s57, 1\n"
"s_cbranch_scc1 MK_Qgo\n"
"s_mov_b32 s29, s30\n"
"buffer_load_dword v3, v1, s[8:11], 0 offen sc1\n"
"s_waitcnt vmcnt(0)\n"
"s_mov_b64 exec, 1\n"
"ds_add_rtn_u32 v82, v80, v81\n"
"s_mov_b64 exec, -1\n"
"s_waitcnt lgkmcnt(0)\n"
"v_and_b32 v83, 3, v82\n"
"v_cmp_eq_u32 vcc, s37, v83\n"
"v_mov_b32 v84, s43\n"
"s_nop 0\n"
"v_cndmask_b32 v83, -1, 0, vcc\n"
"buffer_store_dword v84, v83, s[8:11], s31 offen sc1\n"
"s_mov_b32 s37, 7\n"
"v_mov_b32 v81, 0\n"
"s_branch MK_Qp1_in\n"
"MK_Qp1:\n"
"buffer_load_dword v3, v1, s[8:11], 0 offen sc1\n"
"s_add_u32 s29, s29, 1\n"
"s_waitcnt vmcnt(0)\n"
"MK_Qp1_in:\n"
"v_add_u32 v3, v3, v2\n"
"v_cmp_gt_i32 vcc, s28, v3\n"
"s_and_b64 vcc, vcc, s[64:65]\n"
"s_cbranch_vccz MK_Qp1_ok\n"
"s_cmp_lt_u32 s29, 131072\n"
"s_cbranch_scc1 MK_Qp1\n"
"s_mov_b32 s30, 131072\n"
"MK_Qp1_ok:\n"
"MK_Qgo:\n"
"s_waitcnt vmcnt(4)\n"
"s_barrier\n"
"ds_read_b128 v[48:51], v5 offset:0\n"
"ds_read_b128 v[52:55], v5 offset:2048\n"
"ds_read_b128 v[56:59], v5 offset:4096\n"
"ds_read_b128 v[60:63], v5 offset:6144\n"
"s_and_b32 s50, s28, 15\n"
"s_lshl_b32 s50, s50, 22\n"
"s_add_u32 s50, s50, 33554432\n"
"s_add_u32 s48, s50, s52\n"
"s_waitcnt lgkmcnt(2)\n"
"v_mfma_f32_16x16x32_bf16 v[16:19], a[0:3], v[48:51], 0\n"
"ds_read_b128 v[64:67], v5 offset:8192\n"
"s_add_u32 s42, s28, 1\n"
"s_add_u32 s39, s39, s41\n"
"v_mfma_f32_16x16x32_bf16 v[24:27], a[4:7], v[48:51], 0\n"
"ds_read_b128 v[68:71], v5 offset:10240\n"
"s_lshl_b32 s25, s42, 10\n"
"s_and_b32 s25, s25, 8192\n"
"v_mfma_f32_16x16x32_bf16 v[32:35], a[8:11], v[48:51], 0\n"
"s_xor_b32 s25, s25, s16\n"
"s_lshl_b32 s38, s42, 18\n"
"s_add_u32 s38, s38, s25\n"
"v_mfma_f32_16x16x32_bf16 v[40:43], a[12:15], v[48:51], 0\n"
"s_add_u32 s38, s38, 8388608\n"
"s_add_u32 m0, s39, 0\n"
"s_add_u32 s26, s38, 0\n"
"v_mfma_f32_16x16x32_bf16 v[16:19], a[16:19], v[52:55], v[16:19]\n"
"buffer_load_dwordx4 v4, s[8:11], s26 offen sc1 lds\n"
"v_mfma_f32_16x16x32_bf16 v[24:27], a[20:23], v[52:55], v[24:27]\n"
"v_mfma_f32_16x16x32_bf16 v[32:35], a[24:27], v[52:55], v[32:35]\n"
"v_mfma_f32_16x16x32_bf16 v[40:43], a[28:31], v[52:55], v[40:43]\n"
"s_waitcnt lgkmcnt(2)\n"
"s_add_u32 m0, s39, 4096\n"
"s_add_u32 s26, s38, 4096\n"
"v_mfma_f32_16x16x32_bf16 v[16:19], a[32:35], v[56:59], v[16:19]\n"
"ds_read_b128 v[48:51], v5 offset:12288\n"
"ds_read_b128 v[52:55], v5 offset:14336\n"
"v_mfma_f32_16x16x32_bf16 v[24:27], a[36:39], v[56:59], v[24:27]\n"
"buffer_load_dwordx4 v4, s[8:11], s26 offen sc1 lds\n"
"v_mfma_f32_16x16x32_bf16 v[32:35], a[40:43], v[56:59], v[32:35]\n"
"v_mfma_f32_16x16x32_bf16 v[40:43], a[44:47], v[56:59], v[40:43]\n"
"v_mfma_f32_16x16x32_bf16 v[16:19], a[48:51], v[60:63], v[16:19]\n"
"s_add_u32 m0, s39, 8192\n"
"s_add_u32 s26, s38, 16384\n"
"v_mfma_f32_16x16x32_bf16 v[24:27], a[52:55], v[60:63], v[24:27]\n"
"buffer_load_dwordx4 v4, s[8:11], s26 offen sc1 lds\n"
"v_mfma_f32_16x16x32_bf16 v[32:35], a[56:59], v[60:63], v[32:35]\n"
"v_mfma_f32_16x16x32_bf16 v[40:43], a[60:63], v[60:63], v[40:43]\n"
"s_waitcnt lgkmcnt(2)\n"
"v_mfma_f32_16x16x32_bf16 v[16:19], a[64:67], v[64:67], v[16:19]\n"
"ds_read_b128 v[56:59], v5 offset:16384\n"
"s_add_u32 m0, s39, 12288\n"
"s_add_u32 s26, s38, 20480\n"
"v_mfma_f32_16x16x32_bf16 v[24:27], a[68:71], v[64:67], v[24:27]\n"
"ds_read_b128 v[60:63], v5 offset:18432\n"
"v_mfma_f32_16x16x32_bf16 v[32:35], a[72:75], v[64:67], v[32:35]\n"
"buffer_load_dwordx4 v4, s[8:11], s26 offen sc1 lds\n"
"v_mfma_f32_16x16x32_bf16 v[40:43], a[76:79], v[64:67], v[40:43]\n"
"v_mfma_f32_16x16x32_bf16 v[16:19], a[80:83], v[68:71], v[16:19]\n"
"v_mfma_f32_16x16x32_bf16 v[24:27], a[84:87], v[68:71], v[24:27]\n"
"s_add_u32 m0, s39, 16384\n"
"s_add_u32 s26, s38, 32768\n"
"v_mfma_f32_16x16x32_bf16 v[32:35], a[88:91], v[68:71], v[32:35]\n"
"buffer_load_dwordx4 v4, s[8:11], s26 offen sc1 lds\n"
"v_mfma_f32_16x16x32_bf16 v[40:43], a[92:95], v[68:71], v[40:43]\n"
"s_waitcnt lgkmcnt(2)\n"
"v_mfma_f32_16x16x32_bf16 v[16:19], a[96:99], v[48:51], v[16:19]\n"
"ds_read_b128 v[64:67], v5 offset:20480\n"
"v_mfma_f32_16x16x32_bf16 v[24:27], a[100:103], v[48:51], v[24:27]\n"
"ds_read_b128 v[68:71], v5 offset:22528\n"
"s_add_u32 m0, s39, 20480\n"
"s_add_u32 s26, s38, 36864\n"
"v_mfma_f32_16x16x32_bf16 v[32:35], a[104:107], v[48:51], v[32:35]\n"
"buffer_load_dwordx4 v4, s[8:11], s26 offen sc1 lds\n"
"v_mfma_f32_16x16x32_bf16 v[40:43], a[108:111], v[48:51], v[40:43]\n"
"v_mfma_f32_16x16x32_bf16 v[16:19], a[112:115], v[52:55], v[16:19]\n"
"v_mfma_f32_16x16x32_bf16 v[24:27], a[116:119], v[52:55], v[24:27]\n"
"s_add_u32 s40, s50, s55\n"
"s_add_u32 s56, s40, 4096\n"
"v_mfma_f32_16x16x32_bf16 v[32:35], a[120:123], v[52:55], v[32:35]\n"
"v_mfma_f32_16x16x32_bf16 v[40:43], a[124:127], v[52:55], v[40:43]\n"
"s_waitcnt lgkmcnt(2)\n"
"s_waitcnt vmcnt(6)\n"
"v_mfma_f32_16x16x32_bf16 v[16:19], a[128:131], v[56:59], v[16:19]\n"
"ds_read_b128 v[48:51], v5 offset:24576\n"
"ds_read_b128 v[52:55], v5 offset:26624\n"
"v_mfma_f32_16x16x32_bf16 v[24:27], a[132:135], v[56:59], v[24:27]\n"
"s_mov_b64 exec, 1\n"
"ds_add_rtn_u32 v82, v80, v81\n"
"s_mov_b64 exec, -1\n"
"v_mfma_f32_16x16x32_bf16 v[32:35], a[136:139], v[56:59], v[32:35]\n"
"v_mfma_f32_16x16x32_bf16 v[40:43], a[140:143], v[56:59], v[40:43]\n"
"v_mfma_f32_16x16x32_bf16 v[16:19], a[144:147], v[60:63], v[16:19]\n"
"v_mfma_f32_16x16x32_bf16 v[24:27], a[148:151], v[60:63], v[24:27]\n"
"v_mfma_f32_16x16x32_bf16 v[32:35], a[152:155], v[60:63], v[32:35]\n"
"v_mfma_f32_16x16x32_bf16 v[40:43], a[156:159], v[60:63], v[40:43]\n"
"s_waitcnt lgkmcnt(3)\n"
"s_waitcnt lgkmcnt(0)\n"
"v_and_b32 v83, 3, v82\n"
"v_cmp_eq_u32 vcc, s37, v83\n"
"v_mfma_f32_16x16x32_bf16 v[16:19], a[160:163], v[64:67], v[16:19]\n"
"ds_read_b128 v[56:59], v5 offset:28672\n"
"v_mov_b32 v84, s43\n"
"s_nop 0\n"
"v_mfma_f32_16x16x32_bf16 v[24:27], a[164:167], v[64:67], v[24:27]\n"
"ds_read_b128 v[60:63], v5 offset:30720\n"
"v_cndmask_b32 v83, -1, 0, vcc\n"
"v_mfma_f32_16x16x32_bf16 v[32:35], a[168:171], v[64:67], v[32:35]\n"
"buffer_store_dword v84, v83, s[8:11], s31 offen sc1\n"
"v_mfma_f32_16x16x32_bf16 v[40:43], a[172:175], v[64:67], v[40:43]\n"
"s_add_u32 m0, s51, 118784\n"
"s_add_u32 s26, s48, 1048576\n"
"v_mfma_f32_16x16x32_bf16 v[16:19], a[176:179], v[68:71], v[16:19]\n"
"buffer_load_dwordx4 v4, s[8:11], s26 offen sc1 lds\n"
"v_mfma_f32_16x16x32_bf16 v[24:27], a[180:183], v[68:71], v[24:27]\n"
"v_mfma_f32_16x16x32_bf16 v[32:35], a[184:187], v[68:71], v[32:35]\n"
"v_mfma_f32_16x16x32_bf16 v[40:43], a[188:191], v[68:71], v[40:43]\n"
"s_add_u32 m0, s51, 122880\n"
"s_add_u32 s26, s48, 1064960\n"
"v_mfma_f32_16x16x32_bf16 v[16:19], a[192:195], v[48:51], v[16:19]\n"
"ds_read_b128 v[64:67], v5 offset:32768\n"
"ds_read_b128 v[68:71], v5 offset:34816\n"
"v_mfma_f32_16x16x32_bf16 v[24:27], a[196:199], v[48:51], v[24:27]\n"
"buffer_load_dwordx4 v4, s[8:11], s26 offen sc1 lds\n"
"v_mfma_f32_16x16x32_bf16 v[32:35], a[200:203], v[48:51], v[32:35]\n"
"v_mfma_f32_16x16x32_bf16 v[40:43], a[204:207], v[48:51], v[40:43]\n"
"v_mfma_f32_16x16x32_bf16 v[16:19], a[208:211], v[52:55], v[16:19]\n"
"s_add_u32 m0, s51, 73728\n"
"s_add_u32 s26, s48, 0\n"
"v_mfma_f32_16x16x32_bf16 v[24:27], a[212:215], v[52:55], v[24:27]\n"
"buffer_load_dwordx4 v4, s[8:11], s26 offen sc1 lds\n"
"v_mfma_f32_16x16x32_bf16 v[32:35], a[216:219], v[52:55], v[32:35]\n"
"v_mfma_f32_16x16x32_bf16 v[40:43], a[220:223], v[52:55], v[40:43]\n"
"s_waitcnt lgkmcnt(2)\n"
"v_mfma_f32_16x16x32_bf16 v[16:19], a[224:227], v[56:59], v[16:19]\n"
"s_add_u32 m0, s51, 77824\n"
"s_add_u32 s26, s48, 4096\n"
"v_mfma_f32_16x16x32_bf16 v[24:27], a[228:231], v[56:59], v[24:27]\n"
"buffer_load_dwordx4 v4, s[8:11], s26 offen sc1 lds\n"
"v_mfma_f32_16x16x32_bf16 v[32:35], a[232:235], v[56:59], v[32:35]\n"
"v_mfma_f32_16x16x32_bf16 v[40:43], a[236:239], v[56:59], v[40:43]\n"
"v_mfma_f32_16x16x32_bf16 v[16:19], a[240:243], v[60:63], v[16:19]\n"
"s_add_u32 m0, s51, 81920\n"
"s_add_u32 s26, s48, 16384\n"
"v_mfma_f32_16x16x32_bf16 v[24:27], a[244:247], v[60:63], v[24:27]\n"
"buffer_load_dwordx4 v4, s[8:11], s26 offen sc1 lds\n"
"v_mfma_f32_16x16x32_bf16 v[32:35], a[248:251], v[60:63], v[32:35]\n"
"v_mfma_f32_16x16x32_bf16 v[40:43], a[252:255], v[60:63], v[40:43]\n"
"s_waitcnt lgkmcnt(0)\n"
"v_mfma_f32_16x16x32_bf16 v[16:19], v[252:255], v[64:67], v[16:19]\n"
"s_add_u32 m0, s51, 86016\n"
"s_add_u32 s26, s48, 20480\n"
"v_mfma_f32_16x16x32_bf16 v[24:27], v[248:251], v[64:67], v[24:27]\n"
"buffer_load_dwordx4 v4, s[8:11], s26 offen sc1 lds\n"
"v_mfma_f32_16x16x32_bf16 v[32:35], v[244:247], v[64:67], v[32:35]\n"
"v_mfma_f32_16x16x32_bf16 v[40:43], v[240:243], v[64:67], v[40:43]\n"
"v_mfma_f32_16x16x32_bf16 v[16:19], v[236:239], v[68:71], v[16:19]\n"
"v_mfma_f32_16x16x32_bf16 v[24:27], v[232:235], v[68:71], v[24:27]\n"
"v_mfma_f32_16x16x32_bf16 v[32:35], v[228:231], v[68:71], v[32:35]\n"
"v_mfma_f32_16x16x32_bf16 v[40:43], v[224:227], v[68:71], v[40:43]\n"
"s_add_u32 m0, s51, 90112\n"
"s_add_u32 s26, s48, 524288\n"
"buffer_load_dwordx4 v4, s[8:11], s26 offen sc1 lds\n"
"s_add_u32 m0, s51, 94208\n"
"s_add_u32 s26, s48, 528384\n"
"buffer_load_dwordx4 v4, s[8:11], s26 offen sc1 lds\n"
"s_add_u32 m0, s51, 98304\n"
"s_add_u32 s26, s48, 540672\n"
"buffer_load_dwordx4 v4, s[8:11], s26 offen sc1 lds\n"
"s_add_u32 m0, s51, 102400\n"
"s_add_u32 s26, s48, 544768\n"
"buffer_load_dwordx4 v4, s[8:11], s26 offen sc1 lds\n"
"buffer_load_dword v3, v1, s[8:11], 0 offen sc1\n"
"v_cvt_pk_bf16_f32 v16, v16, v17\n"
"v_cvt_pk_bf16_f32 v17, v18, v19\n"
"v_cvt_pk_bf16_f32 v18, v24, v25\n"
"v_cvt_pk_bf16_f32 v19, v26, v27\n"
"v_cvt_pk_bf16_f32 v32, v32, v33\n"
"v_cvt_pk_bf16_f32 v33, v34, v35\n"
"v_cvt_pk_bf16_f32 v34, v40, v41\n"
"v_cvt_pk_bf16_f32 v35, v42, v43\n"
"v_add_u32 v5, s41, v5\n"
"v_add_u32 v6, s41, v6\n"
"s_sub_u32 s41, 0, s41\n"
"s_mov_b32 s37, 3\n"
"v_mov_b32 v81, 1\n"
"s_mov_b32 s43, s28\n"
"s_mov_b32 s28, s42\n"
"s_waitcnt vmcnt(5)\n"
"s_barrier\n"
"ds_read_b128 v[96:99], v8 offset:0\n"
"ds_read_b128 v[100:103], v8 offset:1024\n"
"ds_read_b128 v[104:107], v8 offset:2048\n"
"ds_read_b128 v[108:111], v8 offset:3072\n"
"ds_read_b128 v[88:91], v15 offset:0\n"
"ds_read_b128 v[92:95], v15 offset:2048\n"
"s_waitcnt lgkmcnt(5)\n"
"v_mfma_f32_16x16x32_bf16 v[192:195], v[96:99], v[16:19], v[72:75]\n"
"ds_read_b128 v[112:115], v8 offset:4096\n"
"s_waitcnt lgkmcnt(5)\n"
"v_mfma_f32_16x16x32_bf16 v[196:199], v[100:103], v[16:19], v[76:79]\n"
"ds_read_b128 v[116:119], v8 offset:5120\n"
"s_waitcnt lgkmcnt(5)\n"
"v_mfma_f32_16x16x32_bf16 v[192:195], v[104:107], v[32:35], v[192:195]\n"
"ds_read_b128 v[120:123], v8 offset:6144\n"
"s_waitcnt lgkmcnt(5)\n"
"v_mfma_f32_16x16x32_bf16 v[196:199], v[108:111], v[32:35], v[196:199]\n"
"ds_read_b128 v[124:127], v8 offset:7168\n"
"s_waitcnt lgkmcnt(3)\n"
"v_mfma_f32_16x16x32_bf16 v[192:195], v[112:115], v[88:91], v[192:195]\n"
"s_waitcnt lgkmcnt(2)\n"
"v_mfma_f32_16x16x32_bf16 v[196:199], v[116:119], v[88:91], v[196:199]\n"
"s_waitcnt lgkmcnt(1)\n"
"v_mfma_f32_16x16x32_bf16 v[192:195], v[120:123], v[92:95], v[192:195]\n"
"s_waitcnt lgkmcnt(0)\n"
"v_mfma_f32_16x16x32_bf16 v[196:199], v[124:127], v[92:95], v[196:199]\n"
"s_nop 5\n"
"v_max3_f32 v210, v192, v193, v194\n"
"s_nop 0\n"
"v_max3_f32 v210, v210, v195, v196\n"
"v_max3_f32 v210, v210, v197, v198\n"
"v_max_f32 v210, v210, v199\n"
"v_mov_b32 v211, v210\n"
"s_nop 1\n"
"v_permlane16_swap_b32 v210, v211\n"
"v_max_f32 v210, v210, v211\n"
"v_mov_b32 v211, v210\n"
"s_nop 1\n"
"v_permlane32_swap_b32 v210, v211\n"
"v_max_f32 v210, v210, v211\n"
"v_mul_f32 v210, s68, v210\n"
"v_pk_fma_f32 v[192:193], v[192:193], s[68:69], v[210:211] op_sel_hi:[1,1,0] neg_lo:[0,0,1] neg_hi:[0,0,1]\n"
"v_pk_fma_f32 v[194:195], v[194:195], s[68:69], v[210:211] op_sel_hi:[1,1,0] neg_lo:[0,0,1] neg_hi:[0,0,1]\n"
"v_pk_fma_f32 v[196:197], v[196:197], s[68:69], v[210:211] op_sel_hi:[1,1,0] neg_lo:[0,0,1] neg_hi:[0,0,1]\n"
"v_pk_fma_f32 v[198:199], v[198:199], s[68:69], v[210:211] op_sel_hi:[1,1,0] neg_lo:[0,0,1] neg_hi:[0,0,1]\n"
"s_waitcnt vmcnt(0)\n"
"v_add_u32 v3, v3, v2\n"
"v_cmp_gt_i32 s[76:77], s43, v3\n"
"v_cmp_gt_i32 s[74:75], s42, v3\n"
"s_barrier\n"
"ds_read_b128 v[128:131], v8 offset:16384\n"
"ds_read_b128 v[132:135], v8 offset:17408\n"
"ds_read_b128 v[136:139], v8 offset:18432\n"
"ds_read_b128 v[140:143], v8 offset:19456\n"
"ds_read_b128 v[144:147], v8 offset:20480\n"
"ds_read_b128 v[148:151], v8 offset:21504\n"
"ds_read_b128 v[152:155], v8 offset:22528\n"
"ds_read_b128 v[156:159], v8 offset:23552\n"
"s_and_b64 s[70:71], s[74:75], s[64:65]\n"
"s_cselect_b32 s57, 0, 1\n"
"s_and_b64 s[70:71], s[76:77], s[66:67]\n"
"s_cselect_b32 s36, 0, 1\n"
"s_cbranch_scc1 MK_Qnokm\n"
"s_add_u32 m0, s51, 106496\n"
"s_add_u32 s26, s50, s53\n"
"buffer_load_dwordx4 v4, s[8:11], s26 offen sc1 lds\n"
"s_add_u32 m0, s51, 110592\n"
"s_add_u32 s26, s50, s54\n"
"buffer_load_dwordx4 v4, s[8:11], s26 offen sc1 lds\n"
"s_add_u32 m0, s51, 114688\n"
"s_add_u32 s26, s50, s54\n"
"s_add_u32 s26, s26, 4096\n"
"buffer_load_dwordx4 v4, s[8:11], s26 offen sc1 lds\n"
"MK_Qnokm:\n"
"v_exp_f32 v192, v192\n"
"v_exp_f32 v193, v193\n"
"v_exp_f32 v194, v194\n"
"v_exp_f32 v195, v195\n"
"v_exp_f32 v196, v196\n"
"v_exp_f32 v197, v197\n"
"v_exp_f32 v198, v198\n"
"v_exp_f32 v199, v199\n"
"v_cvt_pk_bf16_f32 v204, v192, v193\n"
"v_cvt_pk_bf16_f32 v205, v194, v195\n"
"v_cvt_pk_bf16_f32 v206, v196, v197\n"
"v_cvt_pk_bf16_f32 v207, v198, v199\n"
"v_pk_add_f32 v[192:193], v[192:193], v[194:195]\n"
"v_pk_add_f32 v[196:197], v[196:197], v[198:199]\n"
"s_waitcnt lgkmcnt(7)\n"
"v_mfma_f32_16x16x32_bf16 v[96:99], v[128:131], v[204:207], 0\n"
"v_add_f32 v212, v192, v193\n"
"v_add_f32 v213, v196, v197\n"
"s_waitcnt lgkmcnt(6)\n"
"v_mfma_f32_16x16x32_bf16 v[100:103], v[132:135], v[204:207], 0\n"
"v_add_f32 v212, v212, v213\n"
"v_mov_b32 v213, v212\n"
"s_waitcnt lgkmcnt(5)\n"
"v_mfma_f32_16x16x32_bf16 v[104:107], v[136:139], v[204:207], 0\n"
"s_nop 0\n"
"v_permlane16_swap_b32 v212, v213\n"
"s_waitcnt lgkmcnt(4)\n"
"v_mfma_f32_16x16x32_bf16 v[108:111], v[140:143], v[204:207], 0\n"
"v_add_f32 v212, v212, v213\n"
"v_mov_b32 v213, v212\n"
"s_waitcnt lgkmcnt(3)\n"
"v_mfma_f32_16x16x32_bf16 v[112:115], v[144:147], v[204:207], 0\n"
"s_nop 0\n"
"v_permlane32_swap_b32 v212, v213\n"
"s_waitcnt lgkmcnt(2)\n"
"v_mfma_f32_16x16x32_bf16 v[116:119], v[148:151], v[204:207], 0\n"
"v_add_f32 v212, v212, v213\n"
"s_waitcnt lgkmcnt(1)\n"
"v_mfma_f32_16x16x32_bf16 v[120:123], v[152:155], v[204:207], 0\n"
"s_waitcnt lgkmcnt(0)\n"
"v_mfma_f32_16x16x32_bf16 v[124:127], v[156:159], v[204:207], 0\n"
"s_cmp_eq_u32 s36, 1\n"
"s_cbranch_scc1 MK_Qkmok\n"
"s_mov_b32 s29, s30\n"
"MK_Qp2:\n"
"buffer_load_dword v3, v1, s[8:11], 0 offen sc1\n"
"s_add_u32 s29, s29, 1\n"
"s_waitcnt vmcnt(0)\n"
"v_add_u32 v3, v3, v2\n"
"v_cmp_gt_i32 vcc, s43, v3\n"
"s_and_b64 vcc, vcc, s[66:67]\n"
"s_cbranch_vccz MK_Qp2_ok\n"
"s_cmp_lt_u32 s29, 131072\n"
"s_cbranch_scc1 MK_Qp2\n"
"s_mov_b32 s30, 131072\n"
"MK_Qp2_ok:\n"
"s_add_u32 m0, s51, 106496\n"
"s_add_u32 s26, s50, s53\n"
"buffer_load_dwordx4 v4, s[8:11], s26 offen sc1 lds\n"
"s_add_u32 m0, s51, 110592\n"
"s_add_u32 s26, s50, s54\n"
"buffer_load_dwordx4 v4, s[8:11], s26 offen sc1 lds\n"
"s_add_u32 m0, s51, 114688\n"
"s_add_u32 s26, s50, s54\n"
"s_add_u32 s26, s26, 4096\n"
"buffer_load_dwordx4 v4, s[8:11], s26 offen sc1 lds\n"
"v_cmp_gt_i32 vcc, s42, v3\n"
"s_and_b64 s[70:71], vcc, s[64:65]\n"
"s_cselect_b32 s57, 0, 1\n"
"MK_Qkmok:\n"
"s_add_u32 m0, s39, 24576\n"
"s_add_u32 s26, s38, 49152\n"
"buffer_load_dwordx4 v4, s[8:11], s26 offen sc1 lds\n"
"s_add_u32 m0, s39, 28672\n"
"s_add_u32 s26, s38, 53248\n"
"buffer_load_dwordx4 v4, s[8:11], s26 offen sc1 lds\n"
"s_add_u32 m0, s39, 32768\n"
"s_add_u32 s26, s38, 65536\n"
"buffer_load_dwordx4 v4, s[8:11], s26 offen sc1 lds\n"
"s_waitcnt vmcnt(3)\n"
"s_barrier\n"
"ds_read_b128 v[160:163], v10 offset:0\n"
"ds_read_b128 v[164:167], v10 offset:512\n"
"ds_read_b128 v[168:171], v10 offset:1024\n"
"ds_read_b128 v[172:175], v10 offset:1536\n"
"s_waitcnt lgkmcnt(3)\n"
"v_mfma_f32_16x16x32_bf16 v[200:203], v[160:163], v[16:19], 0\n"
"ds_read_b128 v[176:179], v13 offset:0\n"
"s_waitcnt lgkmcnt(3)\n"
"v_mfma_f32_16x16x32_bf16 v[200:203], v[164:167], v[32:35], v[200:203]\n"
"ds_read_b128 v[180:183], v13 offset:1024\n"
"s_waitcnt lgkmcnt(3)\n"
"v_mfma_f32_16x16x32_bf16 v[200:203], v[168:171], v[88:91], v[200:203]\n"
"ds_read_b128 v[184:187], v13 offset:2048\n"
"s_waitcnt lgkmcnt(3)\n"
"v_mfma_f32_16x16x32_bf16 v[200:203], v[172:175], v[92:95], v[200:203]\n"
"ds_read_b128 v[188:191], v13 offset:3072\n"
"s_nop 6\n"
"v_max3_f32 v214, v200, v201, v202\n"
"v_max_f32 v214, v214, v203\n"
"v_mov_b32 v215, v214\n"
"s_nop 1\n"
"v_permlane16_swap_b32 v214, v215\n"
"v_max_f32 v214, v214, v215\n"
"v_mul_f32 v214, s68, v214\n"
"v_max_f32 v214, v214, v210\n"
"v_pk_fma_f32 v[200:201], v[200:201], s[68:69], v[214:215] op_sel_hi:[1,1,0] neg_lo:[0,0,1] neg_hi:[0,0,1]\n"
"v_pk_fma_f32 v[202:203], v[202:203], s[68:69], v[214:215] op_sel_hi:[1,1,0] neg_lo:[0,0,1] neg_hi:[0,0,1]\n"
"v_sub_f32 v210, v210, v214\n"
"v_exp_f32 v200, v200\n"
"v_exp_f32 v201, v201\n"
"v_exp_f32 v202, v202\n"
"v_exp_f32 v203, v203\n"
"v_exp_f32 v210, v210\n"
"v_pk_add_f32 v[214:215], v[200:201], v[202:203]\n"
"v_add_f32 v214, v214, v215\n"
"v_mov_b32 v215, v214\n"
"s_nop 1\n"
"v_permlane16_swap_b32 v214, v215\n"
"v_add_f32 v214, v214, v215\n"
"v_fmac_f32 v214, v212, v210\n"
"v_rcp_f32 v214, v214\n"
"s_nop 0\n"
"v_mul_f32 v216, v210, v214\n"
"v_pk_mul_f32 v[200:201], v[200:201], v[214:215] op_sel_hi:[1,0]\n"
"v_pk_mul_f32 v[202:203], v[202:203], v[214:215] op_sel_hi:[1,0]\n"
"v_cvt_pk_bf16_f32 v208, v200, v201\n"
"v_cvt_pk_bf16_f32 v209, v202, v203\n"
"v_pk_mul_f32 v[96:97], v[96:97], v[216:217] op_sel_hi:[1,0]\n"
"v_pk_mul_f32 v[98:99], v[98:99], v[216:217] op_sel_hi:[1,0]\n"
"v_pk_mul_f32 v[100:101], v[100:101], v[216:217] op_sel_hi:[1,0]\n"
"v_pk_mul_f32 v[102:103], v[102:103], v[216:217] op_sel_hi:[1,0]\n"
"v_pk_mul_f32 v[104:105], v[104:105], v[216:217] op_sel_hi:[1,0]\n"
"v_pk_mul_f32 v[106:107], v[106:107], v[216:217] op_sel_hi:[1,0]\n"
"v_pk_mul_f32 v[108:109], v[108:109], v[216:217] op_sel_hi:[1,0]\n"
"v_pk_mul_f32 v[110:111], v[110:111], v[216:217] op_sel_hi:[1,0]\n"
"v_pk_mul_f32 v[112:113], v[112:113], v[216:217] op_sel_hi:[1,0]\n"
"v_pk_mul_f32 v[114:115], v[114:115], v[216:217] op_sel_hi:[1,0]\n"
"v_pk_mul_f32 v[116:117], v[116:117], v[216:217] op_sel_hi:[1,0]\n"
"v_pk_mul_f32 v[118:119], v[118:119], v[216:217] op_sel_hi:[1,0]\n"
"v_pk_mul_f32 v[120:121], v[120:121], v[216:217] op_sel_hi:[1,0]\n"
"v_pk_mul_f32 v[122:123], v[122:123], v[216:217] op_sel_hi:[1,0]\n"
"v_pk_mul_f32 v[124:125], v[124:125], v[216:217] op_sel_hi:[1,0]\n"
"v_pk_mul_f32 v[126:127], v[126:127], v[216:217] op_sel_hi:[1,0]\n"
"s_waitcnt lgkmcnt(3)\n"
"v_mfma_f32_16x16x16_bf16 v[96:99], v[176:177], v[208:209], v[96:99]\n"
"s_nop 2\n"
"v_mfma_f32_16x16x16_bf16 v[100:103], v[178:179], v[208:209], v[100:103]\n"
"s_nop 2\n"
"s_waitcnt lgkmcnt(2)\n"
"v_mfma_f32_16x16x16_bf16 v[104:107], v[180:181], v[208:209], v[104:107]\n"
"s_nop 2\n"
"v_mfma_f32_16x16x16_bf16 v[108:111], v[182:183], v[208:209], v[108:111]\n"
"v_cvt_pk_bf16_f32 v96, v96, v97\n"
"v_cvt_pk_bf16_f32 v97, v98, v99\n"
"s_waitcnt lgkmcnt(1)\n"
"v_mfma_f32_16x16x16_bf16 v[112:115], v[184:185], v[208:209], v[112:115]\n"
"v_cvt_pk_bf16_f32 v98, v100, v101\n"
"v_cvt_pk_bf16_f32 v99, v102, v103\n"
"v_mfma_f32_16x16x16_bf16 v[116:119], v[186:187], v[208:209], v[116:119]\n"
"buffer_store_dwordx4 v[96:99], v7, s[8:11], s40 offen offset:0 sc1\n"
"s_waitcnt lgkmcnt(0)\n"
"v_mfma_f32_16x16x16_bf16 v[120:123], v[188:189], v[208:209], v[120:123]\n"
"v_cvt_pk_bf16_f32 v104, v104, v105\n"
"v_cvt_pk_bf16_f32 v105, v106, v107\n"
"v_mfma_f32_16x16x16_bf16 v[124:127], v[190:191], v[208:209], v[124:127]\n"
"v_cvt_pk_bf16_f32 v106, v108, v109\n"
"v_cvt_pk_bf16_f32 v107, v110, v111\n"
"buffer_store_dwordx4 v[104:107], v7, s[8:11], s40 offen offset:2048 sc1\n"
"v_cvt_pk_bf16_f32 v112, v112, v113\n"
"v_cvt_pk_bf16_f32 v113, v114, v115\n"
"v_cvt_pk_bf16_f32 v114, v116, v117\n"
"v_cvt_pk_bf16_f32 v115, v118, v119\n"
"buffer_store_dwordx4 v[112:115], v7, s[8:11], s56 offen offset:0 sc1\n"
"v_cvt_pk_bf16_f32 v120, v120, v121\n"
"v_cvt_pk_bf16_f32 v121, v122, v123\n"
"v_cvt_pk_bf16_f32 v122, v124, v125\n"
"v_cvt_pk_bf16_f32 v123, v126, v127\n"
"buffer_store_dwordx4 v[120:123], v7, s[8:11], s56 offen offset:2048 sc1\n"
"s_cmp_lt_u32 s28, 64\n"
"s_nop 0\n"
"s_cbranch_scc1 MK_Qloop\n"
"s_waitcnt vmcnt(0)\n"
"s_barrier\n"
"s_cmp_eq_u32 s15, 0\n"
"s_cbranch_scc0 MK_pf3\n"
"v_mov_b32 v3, 63\n"
"s_mov_b64 exec, 1\n"
"buffer_store_dword v3, off, s[8:11], s31 sc1\n"
"s_mov_b64 exec, -1\n"
"MK_pf3:\n"
"s_endpgm\n"
"MK_W:\n"
"s_memrealtime s[18:19]\n"
"s_mul_i32 s24, s12, 500\n"
"s_add_u32 s24, s24, 0\n"
"s_waitcnt lgkmcnt(0)\n"
"s_add_u32 s24, s24, s18\n"
"MK_W_dly:\n"
"s_memrealtime s[18:19]\n"
"s_waitcnt lgkmcnt(0)\n"
"s_sub_u32 s25, s24, s18\n"
"s_cmp_gt_i32 s25, 0\n"
"s_cbranch_scc0 MK_W_dlx\n"
"s_sleep 4\n"
"s_branch MK_W_dly\n"
"MK_W_dlx:\n"
"s_load_dwordx2 s[44:45], s[6:7], 40\n"
"s_mov_b32 s46, 4718592\n"
"s_mov_b32 s47, 131072\n"
"s_waitcnt lgkmcnt(0)\n"
"s_and_b32 s45, s45, 65535\n"
"v_mbcnt_lo_u32_b32 v0, -1, 0\n"
"v_mbcnt_hi_u32_b32 v0, -1, v0\n"
"v_add_u32 v17, 0, v0\n"
"v_mul_u32_u24 v18, 2731, v17\n"
"v_lshrrev_b32 v18, 16, v18\n"
"v_bfe_u32 v16, v18, 2, 1\n"
"v_mul_u32_u24 v10, 36864, v16\n"
"v_bfe_u32 v16, v18, 1, 1\n"
"v_mul_u32_u24 v16, 9216, v16\n"
"v_add_u32 v10, v10, v16\n"
"v_bfe_u32 v16, v18, 0, 1\n"
"v_mul_u32_u24 v16, 18432, v16\n"
"v_add_u32 v10, v10, v16\n"
"v_mul_u32_u24 v18, 24, v18\n"
"v_sub_u32 v17, v17, v18\n"
"v_lshl_add_u32 v10, v17, 4, v10\n"
"v_add_u32 v17, 64, v0\n"
"v_mul_u32_u24 v18, 2731, v17\n"
"v_lshrrev_b32 v18, 16, v18\n"
"v_bfe_u32 v16, v18, 2, 1\n"
"v_mul_u32_u24 v11, 36864, v16\n"
"v_bfe_u32 v16, v18, 1, 1\n"
"v_mul_u32_u24 v16, 9216, v16\n"
"v_add_u32 v11, v11, v16\n"
"v_bfe_u32 v16, v18, 0, 1\n"
"v_mul_u32_u24 v16, 18432, v16\n"
"v_add_u32 v11, v11, v16\n"
"v_mul_u32_u24 v18, 24, v18\n"
"v_sub_u32 v17, v17, v18\n"
"v_lshl_add_u32 v11, v17, 4, v11\n"
"v_add_u32 v17, 128, v0\n"
"v_mul_u32_u24 v18, 2731, v17\n"
"v_lshrrev_b32 v18, 16, v18\n"
"v_bfe_u32 v16, v18, 2, 1\n"
"v_mul_u32_u24 v12, 36864, v16\n"
"v_bfe_u32 v16, v18, 1, 1\n"
"v_mul_u32_u24 v16, 9216, v16\n"
"v_add_u32 v12, v12, v16\n"
"v_bfe_u32 v16, v18, 0, 1\n"
"v_mul_u32_u24 v16, 18432, v16\n"
"v_add_u32 v12, v12, v16\n"
"v_mul_u32_u24 v18, 24, v18\n"
"v_sub_u32 v17, v17, v18\n"
"v_lshl_add_u32 v12, v17, 4, v12\n"
"s_lshr_b32 s24, s15, 1\n"
"s_mul_i32 s24, s24, 4\n"
"s_and_b32 s25, s15, 1\n"
"s_lshl_b32 s25, s25, 1\n"
"s_add_u32 s24, s24, s25\n"
"s_mul_i32 s25, s13, 1024\n"
"s_add_u32 s24, s24, s25\n"
"s_mul_i32 s24, s24, 1152\n"
"s_mul_i32 s25, s14, 384\n"
"s_add_u32 s48, s24, s25\n"
"s_add_u32 s49, s48, 1152\n"
"s_mul_i32 s50, s15, 6208\n"
"s_mul_i32 s26, s15, 96\n"
"v_bfe_u32 v16, v0, 4, 1\n"
"v_mul_u32_u24 v13, 12416, v16\n"
"v_bfe_u32 v16, v0, 2, 2\n"
"v_mul_u32_u24 v16, 3104, v16\n"
"v_add_u32 v13, v13, v16\n"
"v_bfe_u32 v16, v0, 5, 1\n"
"v_mul_u32_u24 v16, 768, v16\n"
"v_add_u32 v13, v13, v16\n"
"v_bfe_u32 v16, v0, 0, 2\n"
"v_lshl_add_u32 v13, v16, 3, v13\n"
"v_add_u32 v13, s26, v13\n"
"v_add_u32 v14, 49152, v13\n"
"v_add_u32 v15, 49152, v14\n"
"s_add_u32 s24, s48, 0\n"
"s_add_u32 m0, s50, 0\n"
"s_nop 0\n"
"buffer_load_dwordx4 v10, s[44:47], s24 offen lds\n"
"s_add_u32 m0, s50, 1024\n"
"s_nop 0\n"
"buffer_load_dwordx4 v11, s[44:47], s24 offen lds\n"
"s_add_u32 m0, s50, 2048\n"
"s_nop 0\n"
"buffer_load_dwordx4 v12, s[44:47], s24 offen lds\n"
"s_add_u32 s24, s49, 0\n"
"s_add_u32 m0, s50, 3104\n"
"s_nop 0\n"
"buffer_load_dwordx4 v10, s[44:47], s24 offen lds\n"
"s_add_u32 m0, s50, 4128\n"
"s_nop 0\n"
"buffer_load_dwordx4 v11, s[44:47], s24 offen lds\n"
"s_add_u32 m0, s50, 5152\n"
"s_nop 0\n"
"buffer_load_dwordx4 v12, s[44:47], s24 offen lds\n"
"s_add_u32 s24, s48, 73728\n"
"s_add_u32 m0, s50, 24832\n"
"s_nop 0\n"
"buffer_load_dwordx4 v10, s[44:47], s24 offen lds\n"
"s_add_u32 m0, s50, 25856\n"
"s_nop 0\n"
"buffer_load_dwordx4 v11, s[44:47], s24 offen lds\n"
"s_add_u32 m0, s50, 26880\n"
"s_nop 0\n"
"buffer_load_dwordx4 v12, s[44:47], s24 offen lds\n"
"s_add_u32 s24, s49, 73728\n"
"s_add_u32 m0, s50, 27936\n"
"s_nop 0\n"
"buffer_load_dwordx4 v10, s[44:47], s24 offen lds\n"
"s_add_u32 m0, s50, 28960\n"
"s_nop 0\n"
"buffer_load_dwordx4 v11, s[44:47], s24 offen lds\n"
"s_add_u32 m0, s50, 29984\n"
"s_nop 0\n"
"buffer_load_dwordx4 v12, s[44:47], s24 offen lds\n"
"s_add_u32 s24, s48, 147456\n"
"s_add_u32 m0, s50, 49664\n"
"s_nop 0\n"
"buffer_load_dwordx4 v10, s[44:47], s24 offen lds\n"
"s_add_u32 m0, s50, 50688\n"
"s_nop 0\n"
"buffer_load_dwordx4 v11, s[44:47], s24 offen lds\n"
"s_add_u32 m0, s50, 51712\n"
"s_nop 0\n"
"buffer_load_dwordx4 v12, s[44:47], s24 offen lds\n"
"s_add_u32 s24, s49, 147456\n"
"s_add_u32 m0, s50, 52768\n"
"s_nop 0\n"
"buffer_load_dwordx4 v10, s[44:47], s24 offen lds\n"
"s_add_u32 m0, s50, 53792\n"
"s_nop 0\n"
"buffer_load_dwordx4 v11, s[44:47], s24 offen lds\n"
"s_add_u32 m0, s50, 54816\n"
"s_nop 0\n"
"buffer_load_dwordx4 v12, s[44:47], s24 offen lds\n"
"s_add_u32 s24, s48, 221184\n"
"s_add_u32 m0, s50, 74496\n"
"s_nop 0\n"
"buffer_load_dwordx4 v10, s[44:47], s24 offen lds\n"
"s_add_u32 m0, s50, 75520\n"
"s_nop 0\n"
"buffer_load_dwordx4 v11, s[44:47], s24 offen lds\n"
"s_add_u32 m0, s50, 76544\n"
"s_nop 0\n"
"buffer_load_dwordx4 v12, s[44:47], s24 offen lds\n"
"s_add_u32 s24, s49, 221184\n"
"s_add_u32 m0, s50, 77600\n"
"s_nop 0\n"
"buffer_load_dwordx4 v10, s[44:47], s24 offen lds\n"
"s_add_u32 m0, s50, 78624\n"
"s_nop 0\n"
"buffer_load_dwordx4 v11, s[44:47], s24 offen lds\n"
"s_add_u32 m0, s50, 79648\n"
"s_nop 0\n"
"buffer_load_dwordx4 v12, s[44:47], s24 offen lds\n"
"s_add_u32 s24, s48, 294912\n"
"s_add_u32 m0, s50, 99328\n"
"s_nop 0\n"
"buffer_load_dwordx4 v10, s[44:47], s24 offen lds\n"
"s_add_u32 m0, s50, 100352\n"
"s_nop 0\n"
"buffer_load_dwordx4 v11, s[44:47], s24 offen lds\n"
"s_add_u32 m0, s50, 101376\n"
"s_nop 0\n"
"buffer_load_dwordx4 v12, s[44:47], s24 offen lds\n"
"s_add_u32 s24, s49, 294912\n"
"s_add_u32 m0, s50, 102432\n"
"s_nop 0\n"
"buffer_load_dwordx4 v10, s[44:47], s24 offen lds\n"
"s_add_u32 m0, s50, 103456\n"
"s_nop 0\n"
"buffer_load_dwordx4 v11, s[44:47], s24 offen lds\n"
"s_add_u32 m0, s50, 104480\n"
"s_nop 0\n"
"buffer_load_dwordx4 v12, s[44:47], s24 offen lds\n"
"s_waitcnt vmcnt(24) lgkmcnt(0)\n"
"s_barrier\n"
"s_add_u32 s24, s48, 368640\n"
"s_add_u32 m0, s50, 124160\n"
"s_nop 0\n"
"buffer_load_dwordx4 v10, s[44:47], s24 offen lds\n"
"s_add_u32 m0, s50, 125184\n"
"s_nop 0\n"
"buffer_load_dwordx4 v11, s[44:47], s24 offen lds\n"
"s_add_u32 m0, s50, 126208\n"
"s_nop 0\n"
"buffer_load_dwordx4 v12, s[44:47], s24 offen lds\n"
"s_add_u32 s24, s49, 368640\n"
"s_add_u32 m0, s50, 127264\n"
"s_nop 0\n"
"buffer_load_dwordx4 v10, s[44:47], s24 offen lds\n"
"s_add_u32 m0, s50, 128288\n"
"s_nop 0\n"
"buffer_load_dwordx4 v11, s[44:47], s24 offen lds\n"
"s_add_u32 m0, s50, 129312\n"
"s_nop 0\n"
"buffer_load_dwordx4 v12, s[44:47], s24 offen lds\n"
"ds_read_b64_tr_b16 a[0:1], v13 offset:0\n"
"ds_read_b64_tr_b16 a[2:3], v13 offset:384\n"
"ds_read_b64_tr_b16 a[4:5], v13 offset:32\n"
"ds_read_b64_tr_b16 a[6:7], v13 offset:416\n"
"ds_read_b64_tr_b16 a[8:9], v13 offset:64\n"
"ds_read_b64_tr_b16 a[10:11], v13 offset:448\n"
"ds_read_b64_tr_b16 a[12:13], v13 offset:1536\n"
"ds_read_b64_tr_b16 a[14:15], v13 offset:1920\n"
"ds_read_b64_tr_b16 a[16:17], v13 offset:1568\n"
"ds_read_b64_tr_b16 a[18:19], v13 offset:1952\n"
"ds_read_b64_tr_b16 a[20:21], v13 offset:1600\n"
"ds_read_b64_tr_b16 a[22:23], v13 offset:1984\n"
"s_waitcnt vmcnt(24) lgkmcnt(0)\n"
"s_barrier\n"
"s_add_u32 s24, s48, 442368\n"
"s_add_u32 m0, s50, 0\n"
"s_nop 0\n"
"buffer_load_dwordx4 v10, s[44:47], s24 offen lds\n"
"s_add_u32 m0, s50, 1024\n"
"s_nop 0\n"
"buffer_load_dwordx4 v11, s[44:47], s24 offen lds\n"
"s_add_u32 m0, s50, 2048\n"
"s_nop 0\n"
"buffer_load_dwordx4 v12, s[44:47], s24 offen lds\n"
"s_add_u32 s24, s49, 442368\n"
"s_add_u32 m0, s50, 3104\n"
"s_nop 0\n"
"buffer_load_dwordx4 v10, s[44:47], s24 offen lds\n"
"s_add_u32 m0, s50, 4128\n"
"s_nop 0\n"
"buffer_load_dwordx4 v11, s[44:47], s24 offen lds\n"
"s_add_u32 m0, s50, 5152\n"
"s_nop 0\n"
"buffer_load_dwordx4 v12, s[44:47], s24 offen lds\n"
"ds_read_b64_tr_b16 a[24:25], v13 offset:24832\n"
"ds_read_b64_tr_b16 a[26:27], v13 offset:25216\n"
"ds_read_b64_tr_b16 a[28:29], v13 offset:24864\n"
"ds_read_b64_tr_b16 a[30:31], v13 offset:25248\n"
"ds_read_b64_tr_b16 a[32:33], v13 offset:24896\n"
"ds_read_b64_tr_b16 a[34:35], v13 offset:25280\n"
"ds_read_b64_tr_b16 a[36:37], v13 offset:26368\n"
"ds_read_b64_tr_b16 a[38:39], v13 offset:26752\n"
"ds_read_b64_tr_b16 a[40:41], v13 offset:26400\n"
"ds_read_b64_tr_b16 a[42:43], v13 offset:26784\n"
"ds_read_b64_tr_b16 a[44:45], v13 offset:26432\n"
"ds_read_b64_tr_b16 a[46:47], v13 offset:26816\n"
"s_waitcnt vmcnt(24) lgkmcnt(0)\n"
"s_barrier\n"
"s_add_u32 s24, s48, 516096\n"
"s_add_u32 m0, s50, 24832\n"
"s_nop 0\n"
"buffer_load_dwordx4 v10, s[44:47], s24 offen lds\n"
"s_add_u32 m0, s50, 25856\n"
"s_nop 0\n"
"buffer_load_dwordx4 v11, s[44:47], s24 offen lds\n"
"s_add_u32 m0, s50, 26880\n"
"s_nop 0\n"
"buffer_load_dwordx4 v12, s[44:47], s24 offen lds\n"
"s_add_u32 s24, s49, 516096\n"
"s_add_u32 m0, s50, 27936\n"
"s_nop 0\n"
"buffer_load_dwordx4 v10, s[44:47], s24 offen lds\n"
"s_add_u32 m0, s50, 28960\n"
"s_nop 0\n"
"buffer_load_dwordx4 v11, s[44:47], s24 offen lds\n"
"s_add_u32 m0, s50, 29984\n"
"s_nop 0\n"
"buffer_load_dwordx4 v12, s[44:47], s24 offen lds\n"
"ds_read_b64_tr_b16 a[48:49], v14 offset:512\n"
"ds_read_b64_tr_b16 a[50:51], v14 offset:896\n"
"ds_read_b64_tr_b16 a[52:53], v14 offset:544\n"
"ds_read_b64_tr_b16 a[54:55], v14 offset:928\n"
"ds_read_b64_tr_b16 a[56:57], v14 offset:576\n"
"ds_read_b64_tr_b16 a[58:59], v14 offset:960\n"
"ds_read_b64_tr_b16 a[60:61], v14 offset:2048\n"
"ds_read_b64_tr_b16 a[62:63], v14 offset:2432\n"
"ds_read_b64_tr_b16 a[64:65], v14 offset:2080\n"
"ds_read_b64_tr_b16 a[66:67], v14 offset:2464\n"
"ds_read_b64_tr_b16 a[68:69], v14 offset:2112\n"
"ds_read_b64_tr_b16 a[70:71], v14 offset:2496\n"
"s_waitcnt vmcnt(24) lgkmcnt(0)\n"
"s_barrier\n"
"s_add_u32 s24, s48, 589824\n"
"s_add_u32 m0, s50, 49664\n"
"s_nop 0\n"
"buffer_load_dwordx4 v10, s[44:47], s24 offen lds\n"
"s_add_u32 m0, s50, 50688\n"
"s_nop 0\n"
"buffer_load_dwordx4 v11, s[44:47], s24 offen lds\n"
"s_add_u32 m0, s50, 51712\n"
"s_nop 0\n"
"buffer_load_dwordx4 v12, s[44:47], s24 offen lds\n"
"s_add_u32 s24, s49, 589824\n"
"s_add_u32 m0, s50, 52768\n"
"s_nop 0\n"
"buffer_load_dwordx4 v10, s[44:47], s24 offen lds\n"
"s_add_u32 m0, s50, 53792\n"
"s_nop 0\n"
"buffer_load_dwordx4 v11, s[44:47], s24 offen lds\n"
"s_add_u32 m0, s50, 54816\n"
"s_nop 0\n"
"buffer_load_dwordx4 v12, s[44:47], s24 offen lds\n"
"ds_read_b64_tr_b16 a[72:73], v14 offset:25344\n"
"ds_read_b64_tr_b16 a[74:75], v14 offset:25728\n"
"ds_read_b64_tr_b16 a[76:77], v14 offset:25376\n"
"ds_read_b64_tr_b16 a[78:79], v14 offset:25760\n"
"ds_read_b64_tr_b16 a[80:81], v14 offset:25408\n"
"ds_read_b64_tr_b16 a[82:83], v14 offset:25792\n"
"ds_read_b64_tr_b16 a[84:85], v14 offset:26880\n"
"ds_read_b64_tr_b16 a[86:87], v14 offset:27264\n"
"ds_read_b64_tr_b16 a[88:89], v14 offset:26912\n"
"ds_read_b64_tr_b16 a[90:91], v14 offset:27296\n"
"ds_read_b64_tr_b16 a[92:93], v14 offset:26944\n"
"ds_read_b64_tr_b16 a[94:95], v14 offset:27328\n"
"s_waitcnt vmcnt(24) lgkmcnt(0)\n"
"s_barrier\n"
"s_add_u32 s24, s48, 663552\n"
"s_add_u32 m0, s50, 74496\n"
"s_nop 0\n"
"buffer_load_dwordx4 v10, s[44:47], s24 offen lds\n"
"s_add_u32 m0, s50, 75520\n"
"s_nop 0\n"
"buffer_load_dwordx4 v11, s[44:47], s24 offen lds\n"
"s_add_u32 m0, s50, 76544\n"
"s_nop 0\n"
"buffer_load_dwordx4 v12, s[44:47], s24 offen lds\n"
"s_add_u32 s24, s49, 663552\n"
"s_add_u32 m0, s50, 77600\n"
"s_nop 0\n"
"buffer_load_dwordx4 v10, s[44:47], s24 offen lds\n"
"s_add_u32 m0, s50, 78624\n"
"s_nop 0\n"
"buffer_load_dwordx4 v11, s[44:47], s24 offen lds\n"
"s_add_u32 m0, s50, 79648\n"
"s_nop 0\n"
"buffer_load_dwordx4 v12, s[44:47], s24 offen lds\n"
"ds_read_b64_tr_b16 a[96:97], v15 offset:1024\n"
"ds_read_b64_tr_b16 a[98:99], v15 offset:1408\n"
"ds_read_b64_tr_b16 a[100:101], v15 offset:1056\n"
"ds_read_b64_tr_b16 a[102:103], v15 offset:1440\n"
"ds_read_b64_tr_b16 a[104:105], v15 offset:1088\n"
"ds_read_b64_tr_b16 a[106:107], v15 offset:1472\n"
"ds_read_b64_tr_b16 a[108:109], v15 offset:2560\n"
"ds_read_b64_tr_b16 a[110:111], v15 offset:2944\n"
"ds_read_b64_tr_b16 a[112:113], v15 offset:2592\n"
"ds_read_b64_tr_b16 a[114:115], v15 offset:2976\n"
"ds_read_b64_tr_b16 a[116:117], v15 offset:2624\n"
"ds_read_b64_tr_b16 a[118:119], v15 offset:3008\n"
"s_waitcnt vmcnt(24) lgkmcnt(0)\n"
"s_barrier\n"
"s_add_u32 s24, s48, 737280\n"
"s_add_u32 m0, s50, 99328\n"
"s_nop 0\n"
"buffer_load_dwordx4 v10, s[44:47], s24 offen lds\n"
"s_add_u32 m0, s50, 100352\n"
"s_nop 0\n"
"buffer_load_dwordx4 v11, s[44:47], s24 offen lds\n"
"s_add_u32 m0, s50, 101376\n"
"s_nop 0\n"
"buffer_load_dwordx4 v12, s[44:47], s24 offen lds\n"
"s_add_u32 s24, s49, 737280\n"
"s_add_u32 m0, s50, 102432\n"
"s_nop 0\n"
"buffer_load_dwordx4 v10, s[44:47], s24 offen lds\n"
"s_add_u32 m0, s50, 103456\n"
"s_nop 0\n"
"buffer_load_dwordx4 v11, s[44:47], s24 offen lds\n"
"s_add_u32 m0, s50, 104480\n"
"s_nop 0\n"
"buffer_load_dwordx4 v12, s[44:47], s24 offen lds\n"
"ds_read_b64_tr_b16 a[120:121], v15 offset:25856\n"
"ds_read_b64_tr_b16 a[122:123], v15 offset:26240\n"
"ds_read_b64_tr_b16 a[124:125], v15 offset:25888\n"
"ds_read_b64_tr_b16 a[126:127], v15 offset:26272\n"
"ds_read_b64_tr_b16 a[128:129], v15 offset:25920\n"
"ds_read_b64_tr_b16 a[130:131], v15 offset:26304\n"
"ds_read_b64_tr_b16 a[132:133], v15 offset:27392\n"
"ds_read_b64_tr_b16 a[134:135], v15 offset:27776\n"
"ds_read_b64_tr_b16 a[136:137], v15 offset:27424\n"
"ds_read_b64_tr_b16 a[138:139], v15 offset:27808\n"
"ds_read_b64_tr_b16 a[140:141], v15 offset:27456\n"
"ds_read_b64_tr_b16 a[142:143], v15 offset:27840\n"
"s_waitcnt vmcnt(24) lgkmcnt(0)\n"
"s_barrier\n"
"s_add_u32 s24, s48, 811008\n"
"s_add_u32 m0, s50, 124160\n"
"s_nop 0\n"
"buffer_load_dwordx4 v10, s[44:47], s24 offen lds\n"
"s_add_u32 m0, s50, 125184\n"
"s_nop 0\n"
"buffer_load_dwordx4 v11, s[44:47], s24 offen lds\n"
"s_add_u32 m0, s50, 126208\n"
"s_nop 0\n"
"buffer_load_dwordx4 v12, s[44:47], s24 offen lds\n"
"s_add_u32 s24, s49, 811008\n"
"s_add_u32 m0, s50, 127264\n"
"s_nop 0\n"
"buffer_load_dwordx4 v10, s[44:47], s24 offen lds\n"
"s_add_u32 m0, s50, 128288\n"
"s_nop 0\n"
"buffer_load_dwordx4 v11, s[44:47], s24 offen lds\n"
"s_add_u32 m0, s50, 129312\n"
"s_nop 0\n"
"buffer_load_dwordx4 v12, s[44:47], s24 offen lds\n"
"ds_read_b64_tr_b16 a[144:145], v13 offset:0\n"
"ds_read_b64_tr_b16 a[146:147], v13 offset:384\n"
"ds_read_b64_tr_b16 a[148:149], v13 offset:32\n"
"ds_read_b64_tr_b16 a[150:151], v13 offset:416\n"
"ds_read_b64_tr_b16 a[152:153], v13 offset:64\n"
"ds_read_b64_tr_b16 a[154:155], v13 offset:448\n"
"ds_read_b64_tr_b16 a[156:157], v13 offset:1536\n"
"ds_read_b64_tr_b16 a[158:159], v13 offset:1920\n"
"ds_read_b64_tr_b16 a[160:161], v13 offset:1568\n"
"ds_read_b64_tr_b16 a[162:163], v13 offset:1952\n"
"ds_read_b64_tr_b16 a[164:165], v13 offset:1600\n"
"ds_read_b64_tr_b16 a[166:167], v13 offset:1984\n"
"s_waitcnt vmcnt(24) lgkmcnt(0)\n"
"s_barrier\n"
"s_add_u32 s24, s48, 884736\n"
"s_add_u32 m0, s50, 0\n"
"s_nop 0\n"
"buffer_load_dwordx4 v10, s[44:47], s24 offen lds\n"
"s_add_u32 m0, s50, 1024\n"
"s_nop 0\n"
"buffer_load_dwordx4 v11, s[44:47], s24 offen lds\n"
"s_add_u32 m0, s50, 2048\n"
"s_nop 0\n"
"buffer_load_dwordx4 v12, s[44:47], s24 offen lds\n"
"s_add_u32 s24, s49, 884736\n"
"s_add_u32 m0, s50, 3104\n"
"s_nop 0\n"
"buffer_load_dwordx4 v10, s[44:47], s24 offen lds\n"
"s_add_u32 m0, s50, 4128\n"
"s_nop 0\n"
"buffer_load_dwordx4 v11, s[44:47], s24 offen lds\n"
"s_add_u32 m0, s50, 5152\n"
"s_nop 0\n"
"buffer_load_dwordx4 v12, s[44:47], s24 offen lds\n"
"ds_read_b64_tr_b16 a[168:169], v13 offset:24832\n"
"ds_read_b64_tr_b16 a[170:171], v13 offset:25216\n"
"ds_read_b64_tr_b16 a[172:173], v13 offset:24864\n"
"ds_read_b64_tr_b16 a[174:175], v13 offset:25248\n"
"ds_read_b64_tr_b16 a[176:177], v13 offset:24896\n"
"ds_read_b64_tr_b16 a[178:179], v13 offset:25280\n"
"ds_read_b64_tr_b16 a[180:181], v13 offset:26368\n"
"ds_read_b64_tr_b16 a[182:183], v13 offset:26752\n"
"ds_read_b64_tr_b16 a[184:185], v13 offset:26400\n"
"ds_read_b64_tr_b16 a[186:187], v13 offset:26784\n"
"ds_read_b64_tr_b16 a[188:189], v13 offset:26432\n"
"ds_read_b64_tr_b16 a[190:191], v13 offset:26816\n"
"s_waitcnt vmcnt(24) lgkmcnt(0)\n"
"s_barrier\n"
"s_add_u32 s24, s48, 958464\n"
"s_add_u32 m0, s50, 24832\n"
"s_nop 0\n"
"buffer_load_dwordx4 v10, s[44:47], s24 offen lds\n"
"s_add_u32 m0, s50, 25856\n"
"s_nop 0\n"
"buffer_load_dwordx4 v11, s[44:47], s24 offen lds\n"
"s_add_u32 m0, s50, 26880\n"
"s_nop 0\n"
"buffer_load_dwordx4 v12, s[44:47], s24 offen lds\n"
"s_add_u32 s24, s49, 958464\n"
"s_add_u32 m0, s50, 27936\n"
"s_nop 0\n"
"buffer_load_dwordx4 v10, s[44:47], s24 offen lds\n"
"s_add_u32 m0, s50, 28960\n"
"s_nop 0\n"
"buffer_load_dwordx4 v11, s[44:47], s24 offen lds\n"
"s_add_u32 m0, s50, 29984\n"
"s_nop 0\n"
"buffer_load_dwordx4 v12, s[44:47], s24 offen lds\n"
"ds_read_b64_tr_b16 a[192:193], v14 offset:512\n"
"ds_read_b64_tr_b16 a[194:195], v14 offset:896\n"
"ds_read_b64_tr_b16 a[196:197], v14 offset:544\n"
"ds_read_b64_tr_b16 a[198:199], v14 offset:928\n"
"ds_read_b64_tr_b16 a[200:201], v14 offset:576\n"
"ds_read_b64_tr_b16 a[202:203], v14 offset:960\n"
"ds_read_b64_tr_b16 a[204:205], v14 offset:2048\n"
"ds_read_b64_tr_b16 a[206:207], v14 offset:2432\n"
"ds_read_b64_tr_b16 a[208:209], v14 offset:2080\n"
"ds_read_b64_tr_b16 a[210:211], v14 offset:2464\n"
"ds_read_b64_tr_b16 a[212:213], v14 offset:2112\n"
"ds_read_b64_tr_b16 a[214:215], v14 offset:2496\n"
"s_waitcnt vmcnt(24) lgkmcnt(0)\n"
"s_barrier\n"
"s_add_u32 s24, s48, 1032192\n"
"s_add_u32 m0, s50, 49664\n"
"s_nop 0\n"
"buffer_load_dwordx4 v10, s[44:47], s24 offen lds\n"
"s_add_u32 m0, s50, 50688\n"
"s_nop 0\n"
"buffer_load_dwordx4 v11, s[44:47], s24 offen lds\n"
"s_add_u32 m0, s50, 51712\n"
"s_nop 0\n"
"buffer_load_dwordx4 v12, s[44:47], s24 offen lds\n"
"s_add_u32 s24, s49, 1032192\n"
"s_add_u32 m0, s50, 52768\n"
"s_nop 0\n"
"buffer_load_dwordx4 v10, s[44:47], s24 offen lds\n"
"s_add_u32 m0, s50, 53792\n"
"s_nop 0\n"
"buffer_load_dwordx4 v11, s[44:47], s24 offen lds\n"
"s_add_u32 m0, s50, 54816\n"
"s_nop 0\n"
"buffer_load_dwordx4 v12, s[44:47], s24 offen lds\n"
"ds_read_b64_tr_b16 a[216:217], v14 offset:25344\n"
"ds_read_b64_tr_b16 a[218:219], v14 offset:25728\n"
"ds_read_b64_tr_b16 a[220:221], v14 offset:25376\n"
"ds_read_b64_tr_b16 a[222:223], v14 offset:25760\n"
"ds_read_b64_tr_b16 a[224:225], v14 offset:25408\n"
"ds_read_b64_tr_b16 a[226:227], v14 offset:25792\n"
"ds_read_b64_tr_b16 a[228:229], v14 offset:26880\n"
"ds_read_b64_tr_b16 a[230:231], v14 offset:27264\n"
"ds_read_b64_tr_b16 a[232:233], v14 offset:26912\n"
"ds_read_b64_tr_b16 a[234:235], v14 offset:27296\n"
"ds_read_b64_tr_b16 a[236:237], v14 offset:26944\n"
"ds_read_b64_tr_b16 a[238:239], v14 offset:27328\n"
"s_waitcnt vmcnt(24) lgkmcnt(0)\n"
"s_barrier\n"
"s_add_u32 s24, s48, 1105920\n"
"s_add_u32 m0, s50, 74496\n"
"s_nop 0\n"
"buffer_load_dwordx4 v10, s[44:47], s24 offen lds\n"
"s_add_u32 m0, s50, 75520\n"
"s_nop 0\n"
"buffer_load_dwordx4 v11, s[44:47], s24 offen lds\n"
"s_add_u32 m0, s50, 76544\n"
"s_nop 0\n"
"buffer_load_dwordx4 v12, s[44:47], s24 offen lds\n"
"s_add_u32 s24, s49, 1105920\n"
"s_add_u32 m0, s50, 77600\n"
"s_nop 0\n"
"buffer_load_dwordx4 v10, s[44:47], s24 offen lds\n"
"s_add_u32 m0, s50, 78624\n"
"s_nop 0\n"
"buffer_load_dwordx4 v11, s[44:47], s24 offen lds\n"
"s_add_u32 m0, s50, 79648\n"
"s_nop 0\n"
"buffer_load_dwordx4 v12, s[44:47], s24 offen lds\n"
"ds_read_b64_tr_b16 a[240:241], v15 offset:1024\n"
"ds_read_b64_tr_b16 a[242:243], v15 offset:1408\n"
"ds_read_b64_tr_b16 a[244:245], v15 offset:1056\n"
"ds_read_b64_tr_b16 a[246:247], v15 offset:1440\n"
"ds_read_b64_tr_b16 a[248:249], v15 offset:1088\n"
"ds_read_b64_tr_b16 a[250:251], v15 offset:1472\n"
"ds_read_b64_tr_b16 a[252:253], v15 offset:2560\n"
"ds_read_b64_tr_b16 a[254:255], v15 offset:2944\n"
"ds_read_b64_tr_b16 v[252:253], v15 offset:2592\n"
"ds_read_b64_tr_b16 v[254:255], v15 offset:2976\n"
"ds_read_b64_tr_b16 v[248:249], v15 offset:2624\n"
"ds_read_b64_tr_b16 v[250:251], v15 offset:3008\n"
"s_waitcnt vmcnt(24) lgkmcnt(0)\n"
"s_barrier\n"
"ds_read_b64_tr_b16 v[244:245], v15 offset:25856\n"
"ds_read_b64_tr_b16 v[246:247], v15 offset:26240\n"
"ds_read_b64_tr_b16 v[240:241], v15 offset:25888\n"
"ds_read_b64_tr_b16 v[242:243], v15 offset:26272\n"
"ds_read_b64_tr_b16 v[236:237], v15 offset:25920\n"
"ds_read_b64_tr_b16 v[238:239], v15 offset:26304\n"
"ds_read_b64_tr_b16 v[232:233], v15 offset:27392\n"
"ds_read_b64_tr_b16 v[234:235], v15 offset:27776\n"
"ds_read_b64_tr_b16 v[228:229], v15 offset:27424\n"
"ds_read_b64_tr_b16 v[230:231], v15 offset:27808\n"
"ds_read_b64_tr_b16 v[224:225], v15 offset:27456\n"
"ds_read_b64_tr_b16 v[226:227], v15 offset:27840\n"
"s_waitcnt vmcnt(18) lgkmcnt(0)\n"
"s_barrier\n"
"ds_read_b64_tr_b16 v[220:221], v13 offset:0\n"
"ds_read_b64_tr_b16 v[222:223], v13 offset:384\n"
"ds_read_b64_tr_b16 v[216:217], v13 offset:32\n"
"ds_read_b64_tr_b16 v[218:219], v13 offset:416\n"
"ds_read_b64_tr_b16 v[212:213], v13 offset:64\n"
"ds_read_b64_tr_b16 v[214:215], v13 offset:448\n"
"ds_read_b64_tr_b16 v[208:209], v13 offset:1536\n"
"ds_read_b64_tr_b16 v[210:211], v13 offset:1920\n"
"ds_read_b64_tr_b16 v[204:205], v13 offset:1568\n"
"ds_read_b64_tr_b16 v[206:207], v13 offset:1952\n"
"ds_read_b64_tr_b16 v[200:201], v13 offset:1600\n"
"ds_read_b64_tr_b16 v[202:203], v13 offset:1984\n"
"s_waitcnt vmcnt(12) lgkmcnt(0)\n"
"s_barrier\n"
"ds_read_b64_tr_b16 v[196:197], v13 offset:24832\n"
"ds_read_b64_tr_b16 v[198:199], v13 offset:25216\n"
"ds_read_b64_tr_b16 v[192:193], v13 offset:24864\n"
"ds_read_b64_tr_b16 v[194:195], v13 offset:25248\n"
"ds_read_b64_tr_b16 v[188:189], v13 offset:24896\n"
"ds_read_b64_tr_b16 v[190:191], v13 offset:25280\n"
"ds_read_b64_tr_b16 v[184:185], v13 offset:26368\n"
"ds_read_b64_tr_b16 v[186:187], v13 offset:26752\n"
"ds_read_b64_tr_b16 v[180:181], v13 offset:26400\n"
"ds_read_b64_tr_b16 v[182:183], v13 offset:26784\n"
"ds_read_b64_tr_b16 v[176:177], v13 offset:26432\n"
"ds_read_b64_tr_b16 v[178:179], v13 offset:26816\n"
"s_waitcnt vmcnt(6) lgkmcnt(0)\n"
"s_barrier\n"
"ds_read_b64_tr_b16 v[172:173], v14 offset:512\n"
"ds_read_b64_tr_b16 v[174:175], v14 offset:896\n"
"ds_read_b64_tr_b16 v[168:169], v14 offset:544\n"
"ds_read_b64_tr_b16 v[170:171], v14 offset:928\n"
"ds_read_b64_tr_b16 v[164:165], v14 offset:576\n"
"ds_read_b64_tr_b16 v[166:167], v14 offset:960\n"
"ds_read_b64_tr_b16 v[160:161], v14 offset:2048\n"
"ds_read_b64_tr_b16 v[162:163], v14 offset:2432\n"
"ds_read_b64_tr_b16 v[156:157], v14 offset:2080\n"
"ds_read_b64_tr_b16 v[158:159], v14 offset:2464\n"
"ds_read_b64_tr_b16 v[152:153], v14 offset:2112\n"
"ds_read_b64_tr_b16 v[154:155], v14 offset:2496\n"
"s_waitcnt vmcnt(0) lgkmcnt(0)\n"
"s_barrier\n"
"ds_read_b64_tr_b16 v[148:149], v14 offset:25344\n"
"ds_read_b64_tr_b16 v[150:151], v14 offset:25728\n"
"ds_read_b64_tr_b16 v[144:145], v14 offset:25376\n"
"ds_read_b64_tr_b16 v[146:147], v14 offset:25760\n"
"ds_read_b64_tr_b16 v[140:141], v14 offset:25408\n"
"ds_read_b64_tr_b16 v[142:143], v14 offset:25792\n"
"ds_read_b64_tr_b16 v[136:137], v14 offset:26880\n"
"ds_read_b64_tr_b16 v[138:139], v14 offset:27264\n"
"ds_read_b64_tr_b16 v[132:133], v14 offset:26912\n"
"ds_read_b64_tr_b16 v[134:135], v14 offset:27296\n"
"ds_read_b64_tr_b16 v[128:129], v14 offset:26944\n"
"ds_read_b64_tr_b16 v[130:131], v14 offset:27328\n"
"s_waitcnt lgkmcnt(0)\n"
"s_barrier\n"
"s_lshl_b32 s39, s15, 10\n"
"v_lshlrev_b32 v4, 4, v0\n"
"v_add_u32 v4, s39, v4\n"
"v_bfe_u32 v3, v0, 4, 2\n"
"v_mul_u32_u24 v5, 512, v3\n"
"v_bfe_u32 v3, v0, 0, 4\n"
"v_lshl_add_u32 v5, v3, 4, v5\n"
"v_add_u32 v6, 32768, v5\n"
"s_mov_b64 s[32:33], s[8:9]\n"
"s_mov_b32 s35, s11\n"
"s_mov_b32 s36, 0\n"
"s_mov_b32 s37, 7\n"
"s_mov_b32 s28, 0\n"
"s_mov_b32 s41, 65536\n"
"s_mov_b32 s43, -1\n"
"v_mov_b32 v64, 163776\n"
"v_mov_b32 v65, 0\n"
"v_mov_b32 v66, 0\n"
"ds_write_b32 v64, v65\n"
"s_waitcnt lgkmcnt(0)\n"
"s_barrier\n"
"s_mul_i32 s24, s13, 3\n"
"s_add_u32 s24, s24, s14\n"
"s_add_u32 s31, s24, 80\n"
"s_lshr_b32 s27, s31, 6\n"
"s_lshl_b32 s27, s27, 13\n"
"s_lshl_b32 s31, s31, 7\n"
"s_add_u32 s31, s31, s27\n"
"s_add_u32 s31, s31, s16\n"
"s_add_u32 s31, s31, 1835008\n"
"s_lshl_b32 s24, s13, 2\n"
"s_add_u32 s24, s24, 64\n"
"v_lshrrev_b32 v17, 4, v0\n"
"v_add_u32 v17, s24, v17\n"
"v_lshrrev_b32 v16, 6, v17\n"
"v_lshlrev_b32 v16, 13, v16\n"
"v_lshl_add_u32 v1, v17, 7, v16\n"
"v_add_u32 v1, 1835008, v1\n"
"v_add_u32 v1, s16, v1\n"
"s_mul_i32 s51, s13, 131072\n"
"s_add_u32 s51, s51, s16\n"
"s_add_u32 s51, s51, 1048576\n"
"s_mul_i32 s24, s14, 12\n"
"s_mul_i32 s25, s15, 3\n"
"s_add_u32 s24, s24, s25\n"
"s_mul_i32 s25, s13, 81920\n"
"s_add_u32 s25, s25, s17\n"
"s_add_u32 s25, s25, 2686976\n"
"s_add_u32 s26, s24, 0\n"
"s_lshr_b32 s27, s26, 3\n"
"s_lshl_b32 s27, s27, 13\n"
"s_lshl_b32 s52, s26, 10\n"
"s_add_u32 s52, s52, s27\n"
"s_add_u32 s52, s52, s25\n"
"s_add_u32 s26, s24, 1\n"
"s_lshr_b32 s27, s26, 3\n"
"s_lshl_b32 s27, s27, 13\n"
"s_lshl_b32 s53, s26, 10\n"
"s_add_u32 s53, s53, s27\n"
"s_add_u32 s53, s53, s25\n"
"s_add_u32 s26, s24, 2\n"
"s_lshr_b32 s27, s26, 3\n"
"s_lshl_b32 s27, s27, 13\n"
"s_lshl_b32 s54, s26, 10\n"
"s_add_u32 s54, s54, s27\n"
"s_add_u32 s54, s54, s25\n"
"v_lshlrev_b32 v7, 4, v0\n"
"s_and_b32 s38, s28, 15\n"
"s_lshl_b32 s38, s38, 22\n"
"s_add_u32 s38, s38, 33554432\n"
"s_add_u32 s38, s38, s51\n"
"MK_WS_wait:\n"
"s_mov_b32 s29, s30\n"
"buffer_load_dword v3, v1, s[8:11], 0 offen sc1\n"
"s_waitcnt vmcnt(0)\n"
"s_mov_b64 exec, 1\n"
"ds_add_rtn_u32 v66, v64, v65\n"
"s_mov_b64 exec, -1\n"
"s_waitcnt lgkmcnt(0)\n"
"v_and_b32 v67, 3, v66\n"
"v_cmp_eq_u32 vcc, s37, v67\n"
"v_mov_b32 v68, s43\n"
"s_nop 0\n"
"v_cndmask_b32 v67, -1, 0, vcc\n"
"buffer_store_dword v68, v67, s[8:11], s31 offen sc1\n"
"s_mov_b32 s37, 7\n"
"v_mov_b32 v65, 0\n"
"s_branch MK_WS_poll_in\n"
"MK_WS_poll:\n"
"buffer_load_dword v3, v1, s[8:11], 0 offen sc1\n"
"s_add_u32 s29, s29, 1\n"
"s_waitcnt vmcnt(0)\n"
"MK_WS_poll_in:\n"
"v_cmp_gt_i32 vcc, s28, v3\n"
"s_cbranch_vccz MK_WS_poll_ok\n"
"s_cmp_lt_u32 s29, 131072\n"
"s_cbranch_scc1 MK_WS_poll\n"
"s_mov_b32 s30, 131072\n"
"MK_WS_poll_ok:\n"
"s_add_u32 m0, s39, 0\n"
"s_add_u32 s26, s38, 0\n"
"buffer_load_dwordx4 v4, s[8:11], s26 offen sc1 lds\n"
"s_add_u32 m0, s39, 4096\n"
"s_add_u32 s26, s38, 4096\n"
"buffer_load_dwordx4 v4, s[8:11], s26 offen sc1 lds\n"
"s_add_u32 m0, s39, 8192\n"
"s_add_u32 s26, s38, 16384\n"
"buffer_load_dwordx4 v4, s[8:11], s26 offen sc1 lds\n"
"s_add_u32 m0, s39, 12288\n"
"s_add_u32 s26, s38, 20480\n"
"buffer_load_dwordx4 v4, s[8:11], s26 offen sc1 lds\n"
"s_add_u32 m0, s39, 16384\n"
"s_add_u32 s26, s38, 32768\n"
"buffer_load_dwordx4 v4, s[8:11], s26 offen sc1 lds\n"
"s_waitcnt vmcnt(0)\n"
"s_barrier\n"
"ds_read_b128 v[40:43], v5 offset:0\n"
"ds_read_b128 v[44:47], v5 offset:256\n"
"ds_read_b128 v[48:51], v5 offset:2048\n"
"ds_read_b128 v[52:55], v5 offset:2304\n"
"MK_WS_body:\n"
"s_waitcnt lgkmcnt(2)\n"
"v_mfma_f32_16x16x32_bf16 v[16:19], a[0:3], v[40:43], 0\n"
"ds_read_b128 v[56:59], v5 offset:4096\n"
"s_add_u32 m0, s39, 20480\n"
"s_add_u32 s26, s38, 36864\n"
"v_mfma_f32_16x16x32_bf16 v[20:23], a[4:7], v[40:43], 0\n"
"ds_read_b128 v[60:63], v5 offset:4352\n"
"s_and_b32 s40, s28, 15\n"
"s_lshl_b32 s40, s40, 22\n"
"v_mfma_f32_16x16x32_bf16 v[24:27], a[8:11], v[40:43], 0\n"
"buffer_load_dwordx4 v4, s[8:11], s26 offen sc1 lds\n"
"v_mfma_f32_16x16x32_bf16 v[28:31], a[0:3], v[44:47], 0\n"
"s_add_u32 s40, s40, 33554432\n"
"s_add_u32 s55, s40, s52\n"
"v_mfma_f32_16x16x32_bf16 v[32:35], a[4:7], v[44:47], 0\n"
"s_add_u32 s56, s40, s53\n"
"s_add_u32 s57, s40, s54\n"
"v_mfma_f32_16x16x32_bf16 v[36:39], a[8:11], v[44:47], 0\n"
"s_waitcnt lgkmcnt(2)\n"
"v_mfma_f32_16x16x32_bf16 v[16:19], a[12:15], v[48:51], v[16:19]\n"
"ds_read_b128 v[40:43], v5 offset:6144\n"
"s_add_u32 m0, s39, 24576\n"
"s_add_u32 s26, s38, 49152\n"
"v_mfma_f32_16x16x32_bf16 v[20:23], a[16:19], v[48:51], v[20:23]\n"
"ds_read_b128 v[44:47], v5 offset:6400\n"
"v_mfma_f32_16x16x32_bf16 v[24:27], a[20:23], v[48:51], v[24:27]\n"
"buffer_load_dwordx4 v4, s[8:11], s26 offen sc1 lds\n"
"v_mfma_f32_16x16x32_bf16 v[28:31], a[12:15], v[52:55], v[28:31]\n"
"v_mfma_f32_16x16x32_bf16 v[32:35], a[16:19], v[52:55], v[32:35]\n"
"v_mfma_f32_16x16x32_bf16 v[36:39], a[20:23], v[52:55], v[36:39]\n"
"s_waitcnt lgkmcnt(2)\n"
"v_mfma_f32_16x16x32_bf16 v[16:19], a[24:27], v[56:59], v[16:19]\n"
"ds_read_b128 v[48:51], v5 offset:8192\n"
"s_add_u32 m0, s39, 28672\n"
"s_add_u32 s26, s38, 53248\n"
"v_mfma_f32_16x16x32_bf16 v[20:23], a[28:31], v[56:59], v[20:23]\n"
"ds_read_b128 v[52:55], v5 offset:8448\n"
"v_mfma_f32_16x16x32_bf16 v[24:27], a[32:35], v[56:59], v[24:27]\n"
"buffer_load_dwordx4 v4, s[8:11], s26 offen sc1 lds\n"
"v_mfma_f32_16x16x32_bf16 v[28:31], a[24:27], v[60:63], v[28:31]\n"
"v_mfma_f32_16x16x32_bf16 v[32:35], a[28:31], v[60:63], v[32:35]\n"
"v_mfma_f32_16x16x32_bf16 v[36:39], a[32:35], v[60:63], v[36:39]\n"
"s_waitcnt lgkmcnt(2)\n"
"v_mfma_f32_16x16x32_bf16 v[16:19], a[36:39], v[40:43], v[16:19]\n"
"ds_read_b128 v[56:59], v5 offset:10240\n"
"s_add_u32 m0, s39, 32768\n"
"s_add_u32 s26, s38, 65536\n"
"v_mfma_f32_16x16x32_bf16 v[20:23], a[40:43], v[40:43], v[20:23]\n"
"ds_read_b128 v[60:63], v5 offset:10496\n"
"v_mfma_f32_16x16x32_bf16 v[24:27], a[44:47], v[40:43], v[24:27]\n"
"buffer_load_dwordx4 v4, s[8:11], s26 offen sc1 lds\n"
"v_mfma_f32_16x16x32_bf16 v[28:31], a[36:39], v[44:47], v[28:31]\n"
"v_mfma_f32_16x16x32_bf16 v[32:35], a[40:43], v[44:47], v[32:35]\n"
"v_mfma_f32_16x16x32_bf16 v[36:39], a[44:47], v[44:47], v[36:39]\n"
"s_waitcnt lgkmcnt(2)\n"
"v_mfma_f32_16x16x32_bf16 v[16:19], a[48:51], v[48:51], v[16:19]\n"
"ds_read_b128 v[40:43], v5 offset:12288\n"
"s_add_u32 m0, s39, 36864\n"
"s_add_u32 s26, s38, 69632\n"
"v_mfma_f32_16x16x32_bf16 v[20:23], a[52:55], v[48:51], v[20:23]\n"
"ds_read_b128 v[44:47], v5 offset:12544\n"
"v_mfma_f32_16x16x32_bf16 v[24:27], a[56:59], v[48:51], v[24:27]\n"
"buffer_load_dwordx4 v4, s[8:11], s26 offen sc1 lds\n"
"v_mfma_f32_16x16x32_bf16 v[28:31], a[48:51], v[52:55], v[28:31]\n"
"v_mfma_f32_16x16x32_bf16 v[32:35], a[52:55], v[52:55], v[32:35]\n"
"v_mfma_f32_16x16x32_bf16 v[36:39], a[56:59], v[52:55], v[36:39]\n"
"s_waitcnt lgkmcnt(2)\n"
"v_mfma_f32_16x16x32_bf16 v[16:19], a[60:63], v[56:59], v[16:19]\n"
"ds_read_b128 v[48:51], v5 offset:14336\n"
"s_add_u32 m0, s39, 40960\n"
"s_add_u32 s26, s38, 81920\n"
"v_mfma_f32_16x16x32_bf16 v[20:23], a[64:67], v[56:59], v[20:23]\n"
"ds_read_b128 v[52:55], v5 offset:14592\n"
"v_mfma_f32_16x16x32_bf16 v[24:27], a[68:71], v[56:59], v[24:27]\n"
"buffer_load_dwordx4 v4, s[8:11], s26 offen sc1 lds\n"
"v_mfma_f32_16x16x32_bf16 v[28:31], a[60:63], v[60:63], v[28:31]\n"
"v_mfma_f32_16x16x32_bf16 v[32:35], a[64:67], v[60:63], v[32:35]\n"
"v_mfma_f32_16x16x32_bf16 v[36:39], a[68:71], v[60:63], v[36:39]\n"
"s_waitcnt lgkmcnt(2)\n"
"v_mfma_f32_16x16x32_bf16 v[16:19], a[72:75], v[40:43], v[16:19]\n"
"ds_read_b128 v[56:59], v5 offset:16384\n"
"s_add_u32 m0, s39, 45056\n"
"s_add_u32 s26, s38, 86016\n"
"v_mfma_f32_16x16x32_bf16 v[20:23], a[76:79], v[40:43], v[20:23]\n"
"ds_read_b128 v[60:63], v5 offset:16640\n"
"v_mfma_f32_16x16x32_bf16 v[24:27], a[80:83], v[40:43], v[24:27]\n"
"buffer_load_dwordx4 v4, s[8:11], s26 offen sc1 lds\n"
"v_mfma_f32_16x16x32_bf16 v[28:31], a[72:75], v[44:47], v[28:31]\n"
"v_mfma_f32_16x16x32_bf16 v[32:35], a[76:79], v[44:47], v[32:35]\n"
"v_mfma_f32_16x16x32_bf16 v[36:39], a[80:83], v[44:47], v[36:39]\n"
"s_waitcnt lgkmcnt(2)\n"
"v_mfma_f32_16x16x32_bf16 v[16:19], a[84:87], v[48:51], v[16:19]\n"
"ds_read_b128 v[40:43], v5 offset:18432\n"
"s_add_u32 m0, s39, 49152\n"
"s_add_u32 s26, s38, 98304\n"
"v_mfma_f32_16x16x32_bf16 v[20:23], a[88:91], v[48:51], v[20:23]\n"
"ds_read_b128 v[44:47], v5 offset:18688\n"
"v_mfma_f32_16x16x32_bf16 v[24:27], a[92:95], v[48:51], v[24:27]\n"
"buffer_load_dwordx4 v4, s[8:11], s26 offen sc1 lds\n"
"v_mfma_f32_16x16x32_bf16 v[28:31], a[84:87], v[52:55], v[28:31]\n"
"v_mfma_f32_16x16x32_bf16 v[32:35], a[88:91], v[52:55], v[32:35]\n"
"v_mfma_f32_16x16x32_bf16 v[36:39], a[92:95], v[52:55], v[36:39]\n"
"s_waitcnt lgkmcnt(2)\n"
"v_mfma_f32_16x16x32_bf16 v[16:19], a[96:99], v[56:59], v[16:19]\n"
"s_waitcnt vmcnt(6)\n"
"s_barrier\n"
"v_mfma_f32_16x16x32_bf16 v[20:23], a[100:103], v[56:59], v[20:23]\n"
"ds_read_b128 v[48:51], v5 offset:20480\n"
"ds_read_b128 v[52:55], v5 offset:20736\n"
"v_mfma_f32_16x16x32_bf16 v[24:27], a[104:107], v[56:59], v[24:27]\n"
"s_mov_b64 exec, 1\n"
"ds_add_rtn_u32 v66, v64, v65\n"
"s_mov_b64 exec, -1\n"
"v_mfma_f32_16x16x32_bf16 v[28:31], a[96:99], v[60:63], v[28:31]\n"
"s_add_u32 m0, s39, 53248\n"
"s_add_u32 s26, s38, 102400\n"
"v_mfma_f32_16x16x32_bf16 v[32:35], a[100:103], v[60:63], v[32:35]\n"
"buffer_load_dwordx4 v4, s[8:11], s26 offen sc1 lds\n"
"v_mfma_f32_16x16x32_bf16 v[36:39], a[104:107], v[60:63], v[36:39]\n"
"s_waitcnt lgkmcnt(3)\n"
"v_mfma_f32_16x16x32_bf16 v[16:19], a[108:111], v[40:43], v[16:19]\n"
"ds_read_b128 v[56:59], v5 offset:22528\n"
"v_mfma_f32_16x16x32_bf16 v[20:23], a[112:115], v[40:43], v[20:23]\n"
"ds_read_b128 v[60:63], v5 offset:22784\n"
"v_mfma_f32_16x16x32_bf16 v[24:27], a[116:119], v[40:43], v[24:27]\n"
"s_add_u32 m0, s39, 57344\n"
"s_add_u32 s26, s38, 114688\n"
"s_waitcnt lgkmcnt(2)\n"
"v_and_b32 v67, 3, v66\n"
"v_mfma_f32_16x16x32_bf16 v[28:31], a[108:111], v[44:47], v[28:31]\n"
"buffer_load_dwordx4 v4, s[8:11], s26 offen sc1 lds\n"
"v_mfma_f32_16x16x32_bf16 v[32:35], a[112:115], v[44:47], v[32:35]\n"
"v_cmp_eq_u32 vcc, s37, v67\n"
"v_mov_b32 v68, s43\n"
"v_mfma_f32_16x16x32_bf16 v[36:39], a[116:119], v[44:47], v[36:39]\n"
"s_nop 0\n"
"v_cndmask_b32 v67, -1, 0, vcc\n"
"v_mfma_f32_16x16x32_bf16 v[16:19], a[120:123], v[48:51], v[16:19]\n"
"ds_read_b128 v[40:43], v5 offset:24576\n"
"ds_read_b128 v[44:47], v5 offset:24832\n"
"v_mfma_f32_16x16x32_bf16 v[20:23], a[124:127], v[48:51], v[20:23]\n"
"buffer_store_dword v68, v67, s[8:11], s31 offen sc1\n"
"v_mfma_f32_16x16x32_bf16 v[24:27], a[128:131], v[48:51], v[24:27]\n"
"s_add_u32 m0, s39, 61440\n"
"s_add_u32 s26, s38, 118784\n"
"v_mfma_f32_16x16x32_bf16 v[28:31], a[120:123], v[52:55], v[28:31]\n"
"buffer_load_dwordx4 v4, s[8:11], s26 offen sc1 lds\n"
"v_mfma_f32_16x16x32_bf16 v[32:35], a[124:127], v[52:55], v[32:35]\n"
"v_mfma_f32_16x16x32_bf16 v[36:39], a[128:131], v[52:55], v[36:39]\n"
"s_waitcnt lgkmcnt(2)\n"
"v_mfma_f32_16x16x32_bf16 v[16:19], a[132:135], v[56:59], v[16:19]\n"
"ds_read_b128 v[48:51], v5 offset:26624\n"
"v_mfma_f32_16x16x32_bf16 v[20:23], a[136:139], v[56:59], v[20:23]\n"
"ds_read_b128 v[52:55], v5 offset:26880\n"
"v_mfma_f32_16x16x32_bf16 v[24:27], a[140:143], v[56:59], v[24:27]\n"
"v_mfma_f32_16x16x32_bf16 v[28:31], a[132:135], v[60:63], v[28:31]\n"
"v_mfma_f32_16x16x32_bf16 v[32:35], a[136:139], v[60:63], v[32:35]\n"
"v_mfma_f32_16x16x32_bf16 v[36:39], a[140:143], v[60:63], v[36:39]\n"
"s_waitcnt lgkmcnt(2)\n"
"v_mfma_f32_16x16x32_bf16 v[16:19], a[144:147], v[40:43], v[16:19]\n"
"s_waitcnt vmcnt(7)\n"
"s_barrier\n"
"v_mfma_f32_16x16x32_bf16 v[20:23], a[148:151], v[40:43], v[20:23]\n"
"ds_read_b128 v[56:59], v5 offset:28672\n"
"v_mfma_f32_16x16x32_bf16 v[24:27], a[152:155], v[40:43], v[24:27]\n"
"ds_read_b128 v[60:63], v5 offset:28928\n"
"v_mfma_f32_16x16x32_bf16 v[28:31], a[144:147], v[44:47], v[28:31]\n"
"v_mfma_f32_16x16x32_bf16 v[32:35], a[148:151], v[44:47], v[32:35]\n"
"v_mfma_f32_16x16x32_bf16 v[36:39], a[152:155], v[44:47], v[36:39]\n"
"s_waitcnt lgkmcnt(2)\n"
"v_mfma_f32_16x16x32_bf16 v[16:19], a[156:159], v[48:51], v[16:19]\n"
"ds_read_b128 v[40:43], v5 offset:30720\n"
"v_mfma_f32_16x16x32_bf16 v[20:23], a[160:163], v[48:51], v[20:23]\n"
"ds_read_b128 v[44:47], v5 offset:30976\n"
"v_mfma_f32_16x16x32_bf16 v[24:27], a[164:167], v[48:51], v[24:27]\n"
"v_mfma_f32_16x16x32_bf16 v[28:31], a[156:159], v[52:55], v[28:31]\n"
"v_mfma_f32_16x16x32_bf16 v[32:35], a[160:163], v[52:55], v[32:35]\n"
"v_mfma_f32_16x16x32_bf16 v[36:39], a[164:167], v[52:55], v[36:39]\n"
"s_waitcnt lgkmcnt(2)\n"
"v_mfma_f32_16x16x32_bf16 v[16:19], a[168:171], v[56:59], v[16:19]\n"
"ds_read_b128 v[48:51], v6 offset:0\n"
"v_mfma_f32_16x16x32_bf16 v[20:23], a[172:175], v[56:59], v[20:23]\n"
"ds_read_b128 v[52:55], v6 offset:256\n"
"v_mfma_f32_16x16x32_bf16 v[24:27], a[176:179], v[56:59], v[24:27]\n"
"v_mfma_f32_16x16x32_bf16 v[28:31], a[168:171], v[60:63], v[28:31]\n"
"v_mfma_f32_16x16x32_bf16 v[32:35], a[172:175], v[60:63], v[32:35]\n"
"v_mfma_f32_16x16x32_bf16 v[36:39], a[176:179], v[60:63], v[36:39]\n"
"s_waitcnt lgkmcnt(2)\n"
"v_mfma_f32_16x16x32_bf16 v[16:19], a[180:183], v[40:43], v[16:19]\n"
"ds_read_b128 v[56:59], v6 offset:2048\n"
"ds_read_b128 v[60:63], v6 offset:2304\n"
"v_mfma_f32_16x16x32_bf16 v[20:23], a[184:187], v[40:43], v[20:23]\n"
"buffer_load_dword v3, v1, s[8:11], 0 offen sc1\n"
"v_mfma_f32_16x16x32_bf16 v[24:27], a[188:191], v[40:43], v[24:27]\n"
"s_add_u32 s42, s28, 1\n"
"s_add_u32 s39, s39, s41\n"
"s_and_b32 s38, s42, 15\n"
"v_mfma_f32_16x16x32_bf16 v[28:31], a[180:183], v[44:47], v[28:31]\n"
"s_lshl_b32 s38, s38, 22\n"
"s_add_u32 s38, s38, 33554432\n"
"s_add_u32 s38, s38, s51\n"
"v_mfma_f32_16x16x32_bf16 v[32:35], a[184:187], v[44:47], v[32:35]\n"
"v_mfma_f32_16x16x32_bf16 v[36:39], a[188:191], v[44:47], v[36:39]\n"
"s_waitcnt lgkmcnt(2)\n"
"v_mfma_f32_16x16x32_bf16 v[16:19], a[192:195], v[48:51], v[16:19]\n"
"ds_read_b128 v[40:43], v6 offset:4096\n"
"v_mfma_f32_16x16x32_bf16 v[20:23], a[196:199], v[48:51], v[20:23]\n"
"ds_read_b128 v[44:47], v6 offset:4352\n"
"v_mfma_f32_16x16x32_bf16 v[24:27], a[200:203], v[48:51], v[24:27]\n"
"v_mfma_f32_16x16x32_bf16 v[28:31], a[192:195], v[52:55], v[28:31]\n"
"v_mfma_f32_16x16x32_bf16 v[32:35], a[196:199], v[52:55], v[32:35]\n"
"v_mfma_f32_16x16x32_bf16 v[36:39], a[200:203], v[52:55], v[36:39]\n"
"s_waitcnt lgkmcnt(2)\n"
"v_mfma_f32_16x16x32_bf16 v[16:19], a[204:207], v[56:59], v[16:19]\n"
"ds_read_b128 v[48:51], v6 offset:6144\n"
"v_mfma_f32_16x16x32_bf16 v[20:23], a[208:211], v[56:59], v[20:23]\n"
"ds_read_b128 v[52:55], v6 offset:6400\n"
"v_mfma_f32_16x16x32_bf16 v[24:27], a[212:215], v[56:59], v[24:27]\n"
"v_mfma_f32_16x16x32_bf16 v[28:31], a[204:207], v[60:63], v[28:31]\n"
"v_mfma_f32_16x16x32_bf16 v[32:35], a[208:211], v[60:63], v[32:35]\n"
"v_mfma_f32_16x16x32_bf16 v[36:39], a[212:215], v[60:63], v[36:39]\n"
"s_waitcnt lgkmcnt(2)\n"
"v_mfma_f32_16x16x32_bf16 v[16:19], a[216:219], v[40:43], v[16:19]\n"
"s_waitcnt vmcnt(1)\n"
"s_barrier\n"
"v_mfma_f32_16x16x32_bf16 v[20:23], a[220:223], v[40:43], v[20:23]\n"
"ds_read_b128 v[56:59], v6 offset:8192\n"
"v_mfma_f32_16x16x32_bf16 v[24:27], a[224:227], v[40:43], v[24:27]\n"
"ds_read_b128 v[60:63], v6 offset:8448\n"
"v_mfma_f32_16x16x32_bf16 v[28:31], a[216:219], v[44:47], v[28:31]\n"
"v_mfma_f32_16x16x32_bf16 v[32:35], a[220:223], v[44:47], v[32:35]\n"
"v_mfma_f32_16x16x32_bf16 v[36:39], a[224:227], v[44:47], v[36:39]\n"
"s_waitcnt lgkmcnt(2)\n"
"v_mfma_f32_16x16x32_bf16 v[16:19], a[228:231], v[48:51], v[16:19]\n"
"ds_read_b128 v[40:43], v6 offset:10240\n"
"v_mfma_f32_16x16x32_bf16 v[20:23], a[232:235], v[48:51], v[20:23]\n"
"ds_read_b128 v[44:47], v6 offset:10496\n"
"v_mfma_f32_16x16x32_bf16 v[24:27], a[236:239], v[48:51], v[24:27]\n"
"v_mfma_f32_16x16x32_bf16 v[28:31], a[228:231], v[52:55], v[28:31]\n"
"v_mfma_f32_16x16x32_bf16 v[32:35], a[232:235], v[52:55], v[32:35]\n"
"v_mfma_f32_16x16x32_bf16 v[36:39], a[236:239], v[52:55], v[36:39]\n"
"s_waitcnt lgkmcnt(2)\n"
"v_mfma_f32_16x16x32_bf16 v[16:19], a[240:243], v[56:59], v[16:19]\n"
"ds_read_b128 v[48:51], v6 offset:12288\n"
"ds_read_b128 v[52:55], v6 offset:12544\n"
"v_mfma_f32_16x16x32_bf16 v[20:23], a[244:247], v[56:59], v[20:23]\n"
"s_waitcnt vmcnt(0)\n"
"v_cmp_gt_i32 s[76:77], s42, v3\n"
"v_mfma_f32_16x16x32_bf16 v[24:27], a[248:251], v[56:59], v[24:27]\n"
"v_mfma_f32_16x16x32_bf16 v[28:31], a[240:243], v[60:63], v[28:31]\n"
"s_lshr_b32 s27, s42, 6\n"
"s_or_b32 s76, s76, s27\n"
"v_mfma_f32_16x16x32_bf16 v[32:35], a[244:247], v[60:63], v[32:35]\n"
"s_cmp_eq_u64 s[76:77], 0\n"
"s_cselect_b32 s36, 1, 0\n"
"s_cselect_b32 s34, s10, 0\n"
"v_mfma_f32_16x16x32_bf16 v[36:39], a[248:251], v[60:63], v[36:39]\n"
"s_waitcnt lgkmcnt(2)\n"
"s_add_u32 m0, s39, 0\n"
"s_add_u32 s26, s38, 0\n"
"v_mfma_f32_16x16x32_bf16 v[16:19], a[252:255], v[40:43], v[16:19]\n"
"ds_read_b128 v[56:59], v6 offset:14336\n"
"ds_read_b128 v[60:63], v6 offset:14592\n"
"v_mfma_f32_16x16x32_bf16 v[20:23], v[252:255], v[40:43], v[20:23]\n"
"buffer_load_dwordx4 v4, s[32:35], s26 offen sc1 lds\n"
"v_mfma_f32_16x16x32_bf16 v[24:27], v[248:251], v[40:43], v[24:27]\n"
"v_mfma_f32_16x16x32_bf16 v[28:31], a[252:255], v[44:47], v[28:31]\n"
"v_mfma_f32_16x16x32_bf16 v[32:35], v[252:255], v[44:47], v[32:35]\n"
"v_mfma_f32_16x16x32_bf16 v[36:39], v[248:251], v[44:47], v[36:39]\n"
"s_waitcnt lgkmcnt(2)\n"
"s_add_u32 m0, s39, 4096\n"
"s_add_u32 s26, s38, 4096\n"
"v_mfma_f32_16x16x32_bf16 v[16:19], v[244:247], v[48:51], v[16:19]\n"
"ds_read_b128 v[40:43], v6 offset:16384\n"
"ds_read_b128 v[44:47], v6 offset:16640\n"
"v_mfma_f32_16x16x32_bf16 v[20:23], v[240:243], v[48:51], v[20:23]\n"
"buffer_load_dwordx4 v4, s[32:35], s26 offen sc1 lds\n"
"v_mfma_f32_16x16x32_bf16 v[24:27], v[236:239], v[48:51], v[24:27]\n"
"v_mfma_f32_16x16x32_bf16 v[28:31], v[244:247], v[52:55], v[28:31]\n"
"v_mfma_f32_16x16x32_bf16 v[32:35], v[240:243], v[52:55], v[32:35]\n"
"v_mfma_f32_16x16x32_bf16 v[36:39], v[236:239], v[52:55], v[36:39]\n"
"s_waitcnt lgkmcnt(2)\n"
"s_add_u32 m0, s39, 8192\n"
"s_add_u32 s26, s38, 16384\n"
"v_mfma_f32_16x16x32_bf16 v[16:19], v[232:235], v[56:59], v[16:19]\n"
"ds_read_b128 v[48:51], v6 offset:18432\n"
"ds_read_b128 v[52:55], v6 offset:18688\n"
"v_mfma_f32_16x16x32_bf16 v[20:23], v[228:231], v[56:59], v[20:23]\n"
"buffer_load_dwordx4 v4, s[32:35], s26 offen sc1 lds\n"
"v_mfma_f32_16x16x32_bf16 v[24:27], v[224:227], v[56:59], v[24:27]\n"
"v_mfma_f32_16x16x32_bf16 v[28:31], v[232:235], v[60:63], v[28:31]\n"
"v_mfma_f32_16x16x32_bf16 v[32:35], v[228:231], v[60:63], v[32:35]\n"
"v_mfma_f32_16x16x32_bf16 v[36:39], v[224:227], v[60:63], v[36:39]\n"
"s_waitcnt lgkmcnt(2)\n"
"s_add_u32 m0, s39, 12288\n"
"s_add_u32 s26, s38, 20480\n"
"v_mfma_f32_16x16x32_bf16 v[16:19], v[220:223], v[40:43], v[16:19]\n"
"ds_read_b128 v[56:59], v6 offset:20480\n"
"ds_read_b128 v[60:63], v6 offset:20736\n"
"v_mfma_f32_16x16x32_bf16 v[20:23], v[216:219], v[40:43], v[20:23]\n"
"buffer_load_dwordx4 v4, s[32:35], s26 offen sc1 lds\n"
"v_mfma_f32_16x16x32_bf16 v[24:27], v[212:215], v[40:43], v[24:27]\n"
"v_mfma_f32_16x16x32_bf16 v[28:31], v[220:223], v[44:47], v[28:31]\n"
"v_mfma_f32_16x16x32_bf16 v[32:35], v[216:219], v[44:47], v[32:35]\n"
"v_mfma_f32_16x16x32_bf16 v[36:39], v[212:215], v[44:47], v[36:39]\n"
"s_waitcnt lgkmcnt(2)\n"
"s_add_u32 m0, s39, 16384\n"
"s_add_u32 s26, s38, 32768\n"
"v_mfma_f32_16x16x32_bf16 v[16:19], v[208:211], v[48:51], v[16:19]\n"
"ds_read_b128 v[40:43], v6 offset:22528\n"
"ds_read_b128 v[44:47], v6 offset:22784\n"
"v_mfma_f32_16x16x32_bf16 v[20:23], v[204:207], v[48:51], v[20:23]\n"
"buffer_load_dwordx4 v4, s[32:35], s26 offen sc1 lds\n"
"v_mfma_f32_16x16x32_bf16 v[24:27], v[200:203], v[48:51], v[24:27]\n"
"v_mfma_f32_16x16x32_bf16 v[28:31], v[208:211], v[52:55], v[28:31]\n"
"v_mfma_f32_16x16x32_bf16 v[32:35], v[204:207], v[52:55], v[32:35]\n"
"v_mfma_f32_16x16x32_bf16 v[36:39], v[200:203], v[52:55], v[36:39]\n"
"s_waitcnt lgkmcnt(2)\n"
"v_mfma_f32_16x16x32_bf16 v[16:19], v[196:199], v[56:59], v[16:19]\n"
"ds_read_b128 v[48:51], v6 offset:24576\n"
"v_mfma_f32_16x16x32_bf16 v[20:23], v[192:195], v[56:59], v[20:23]\n"
"ds_read_b128 v[52:55], v6 offset:24832\n"
"v_mfma_f32_16x16x32_bf16 v[24:27], v[188:191], v[56:59], v[24:27]\n"
"v_mfma_f32_16x16x32_bf16 v[28:31], v[196:199], v[60:63], v[28:31]\n"
"v_mfma_f32_16x16x32_bf16 v[32:35], v[192:195], v[60:63], v[32:35]\n"
"v_mfma_f32_16x16x32_bf16 v[36:39], v[188:191], v[60:63], v[36:39]\n"
"s_waitcnt lgkmcnt(2)\n"
"v_mfma_f32_16x16x32_bf16 v[16:19], v[184:187], v[40:43], v[16:19]\n"
"ds_read_b128 v[56:59], v6 offset:26624\n"
"v_mfma_f32_16x16x32_bf16 v[20:23], v[180:183], v[40:43], v[20:23]\n"
"ds_read_b128 v[60:63], v6 offset:26880\n"
"v_mfma_f32_16x16x32_bf16 v[24:27], v[176:179], v[40:43], v[24:27]\n"
"v_mfma_f32_16x16x32_bf16 v[28:31], v[184:187], v[44:47], v[28:31]\n"
"v_mfma_f32_16x16x32_bf16 v[32:35], v[180:183], v[44:47], v[32:35]\n"
"v_mfma_f32_16x16x32_bf16 v[36:39], v[176:179], v[44:47], v[36:39]\n"
"s_waitcnt lgkmcnt(2)\n"
"v_mfma_f32_16x16x32_bf16 v[16:19], v[172:175], v[48:51], v[16:19]\n"
"ds_read_b128 v[40:43], v6 offset:28672\n"
"v_mfma_f32_16x16x32_bf16 v[20:23], v[168:171], v[48:51], v[20:23]\n"
"ds_read_b128 v[44:47], v6 offset:28928\n"
"v_mfma_f32_16x16x32_bf16 v[24:27], v[164:167], v[48:51], v[24:27]\n"
"v_mfma_f32_16x16x32_bf16 v[28:31], v[172:175], v[52:55], v[28:31]\n"
"v_mfma_f32_16x16x32_bf16 v[32:35], v[168:171], v[52:55], v[32:35]\n"
"v_mfma_f32_16x16x32_bf16 v[36:39], v[164:167], v[52:55], v[36:39]\n"
"s_waitcnt lgkmcnt(2)\n"
"v_mfma_f32_16x16x32_bf16 v[16:19], v[160:163], v[56:59], v[16:19]\n"
"ds_read_b128 v[48:51], v6 offset:30720\n"
"v_mfma_f32_16x16x32_bf16 v[28:31], v[160:163], v[60:63], v[28:31]\n"
"ds_read_b128 v[52:55], v6 offset:30976\n"
"s_waitcnt lgkmcnt(3)\n"
"v_mfma_f32_16x16x32_bf16 v[16:19], v[148:151], v[40:43], v[16:19]\n"
"s_waitcnt lgkmcnt(2)\n"
"v_mfma_f32_16x16x32_bf16 v[28:31], v[148:151], v[44:47], v[28:31]\n"
"s_waitcnt lgkmcnt(1)\n"
"v_mfma_f32_16x16x32_bf16 v[16:19], v[136:139], v[48:51], v[16:19]\n"
"s_waitcnt lgkmcnt(0)\n"
"v_mfma_f32_16x16x32_bf16 v[28:31], v[136:139], v[52:55], v[28:31]\n"
"v_mfma_f32_16x16x32_bf16 v[20:23], v[156:159], v[56:59], v[20:23]\n"
"v_add_u32 v5, s41, v5\n"
"v_add_u32 v6, s41, v6\n"
"s_sub_u32 s41, 0, s41\n"
"v_mfma_f32_16x16x32_bf16 v[32:35], v[156:159], v[60:63], v[32:35]\n"
"s_mov_b32 s37, 3\n"
"v_mov_b32 v65, 1\n"
"s_mov_b32 s43, s28\n"
"v_mfma_f32_16x16x32_bf16 v[20:23], v[144:147], v[40:43], v[20:23]\n"
"s_mov_b32 s28, s42\n"
"v_cvt_pk_bf16_f32 v16, v16, v17\n"
"v_cvt_pk_bf16_f32 v17, v18, v19\n"
"v_mfma_f32_16x16x32_bf16 v[32:35], v[144:147], v[44:47], v[32:35]\n"
"v_cvt_pk_bf16_f32 v18, v28, v29\n"
"v_cvt_pk_bf16_f32 v19, v30, v31\n"
"v_mfma_f32_16x16x32_bf16 v[20:23], v[132:135], v[48:51], v[20:23]\n"
"v_permlane16_swap_b32 v16, v18\n"
"v_mfma_f32_16x16x32_bf16 v[32:35], v[132:135], v[52:55], v[32:35]\n"
"v_permlane16_swap_b32 v17, v19\n"
"v_mfma_f32_16x16x32_bf16 v[24:27], v[152:155], v[56:59], v[24:27]\n"
"buffer_store_dwordx4 v[16:19], v7, s[8:11], s55 offen sc1\n"
"v_mfma_f32_16x16x32_bf16 v[36:39], v[152:155], v[60:63], v[36:39]\n"
"s_nop 1\n"
"v_mfma_f32_16x16x32_bf16 v[24:27], v[140:143], v[40:43], v[24:27]\n"
"v_cvt_pk_bf16_f32 v20, v20, v21\n"
"v_cvt_pk_bf16_f32 v21, v22, v23\n"
"v_mfma_f32_16x16x32_bf16 v[36:39], v[140:143], v[44:47], v[36:39]\n"
"v_cvt_pk_bf16_f32 v22, v32, v33\n"
"v_cvt_pk_bf16_f32 v23, v34, v35\n"
"v_mfma_f32_16x16x32_bf16 v[24:27], v[128:131], v[48:51], v[24:27]\n"
"v_permlane16_swap_b32 v20, v22\n"
"v_mfma_f32_16x16x32_bf16 v[36:39], v[128:131], v[52:55], v[36:39]\n"
"v_permlane16_swap_b32 v21, v23\n"
"buffer_store_dwordx4 v[20:23], v7, s[8:11], s56 offen sc1\n"
"s_cmp_eq_u32 s36, 1\n"
"s_cbranch_scc0 MK_WS_np\n"
"s_waitcnt vmcnt(2)\n"
"s_barrier\n"
"ds_read_b128 v[40:43], v5 offset:0\n"
"ds_read_b128 v[44:47], v5 offset:256\n"
"ds_read_b128 v[48:51], v5 offset:2048\n"
"ds_read_b128 v[52:55], v5 offset:2304\n"
"v_cvt_pk_bf16_f32 v24, v24, v25\n"
"v_cvt_pk_bf16_f32 v25, v26, v27\n"
"v_cvt_pk_bf16_f32 v26, v36, v37\n"
"v_cvt_pk_bf16_f32 v27, v38, v39\n"
"s_nop 0\n"
"v_permlane16_swap_b32 v24, v26\n"
"v_permlane16_swap_b32 v25, v27\n"
"buffer_store_dwordx4 v[24:27], v7, s[8:11], s57 offen sc1\n"
"s_branch MK_WS_body\n"
"MK_WS_np:\n"
"s_nop 1\n"
"v_cvt_pk_bf16_f32 v24, v24, v25\n"
"v_cvt_pk_bf16_f32 v25, v26, v27\n"
"v_cvt_pk_bf16_f32 v26, v36, v37\n"
"v_cvt_pk_bf16_f32 v27, v38, v39\n"
"s_nop 0\n"
"v_permlane16_swap_b32 v24, v26\n"
"v_permlane16_swap_b32 v25, v27\n"
"buffer_store_dwordx4 v[24:27], v7, s[8:11], s57 offen sc1\n"
"s_cmp_lt_u32 s28, 64\n"
"s_cbranch_scc1 MK_WS_wait\n"
"s_waitcnt vmcnt(0)\n"
"s_barrier\n"
"s_cmp_eq_u32 s15, 0\n"
"s_cbranch_scc0 MK_pf4\n"
"v_mov_b32 v3, 63\n"
"s_mov_b64 exec, 1\n"
"buffer_store_dword v3, off, s[8:11], s31 sc1\n"
"s_mov_b64 exec, -1\n"
"MK_pf4:\n"
"s_endpgm\n"
"MK_F1:\n"
"s_memrealtime s[18:19]\n"
"s_mul_i32 s24, s12, 500\n"
"s_add_u32 s24, s24, 500\n"
"s_waitcnt lgkmcnt(0)\n"
"s_add_u32 s24, s24, s18\n"
"MK_F1_dly:\n"
"s_memrealtime s[18:19]\n"
"s_waitcnt lgkmcnt(0)\n"
"s_sub_u32 s25, s24, s18\n"
"s_cmp_gt_i32 s25, 0\n"
"s_cbranch_scc0 MK_F1_dlx\n"
"s_sleep 4\n"
"s_branch MK_F1_dly\n"
"MK_F1_dlx:\n"
"s_cmp_eq_u32 s12, 0\n"
"s_cselect_b32 s24, 0, 0\n"
"s_add_u32 s16, s16, s24\n"
"s_add_u32 s17, s17, s24\n"
"s_load_dwordx2 s[44:45], s[6:7], 48\n"
"s_mov_b32 s46, 5308416\n"
"s_mov_b32 s47, 131072\n"
"s_waitcnt lgkmcnt(0)\n"
"s_and_b32 s45, s45, 65535\n"
"s_load_dwordx2 s[64:65], s[6:7], 56\n"
"s_mov_b32 s66, 9216\n"
"s_mov_b32 s67, 131072\n"
"s_waitcnt lgkmcnt(0)\n"
"s_and_b32 s65, s65, 65535\n"
"v_mbcnt_lo_u32_b32 v0, -1, 0\n"
"v_mbcnt_hi_u32_b32 v0, -1, v0\n"
"s_mul_i32 s48, s14, 576\n"
"s_lshl_b32 s49, s15, 7\n"
"v_bfe_u32 v15, v0, 5, 1\n"
"v_mul_u32_u24 v10, 36864, v15\n"
"v_bfe_u32 v15, v0, 0, 5\n"
"v_lshl_add_u32 v10, v15, 4, v10\n"
"v_bfe_u32 v15, v0, 5, 1\n"
"v_mul_u32_u24 v11, 36864, v15\n"
"v_bfe_u32 v15, v0, 0, 5\n"
"v_lshl_add_u32 v11, v15, 4, v11\n"
"v_add_u32 v11, 147456, v11\n"
"s_lshr_b32 s24, s15, 1\n"
"s_mul_i32 s24, s24, 73728\n"
"s_and_b32 s25, s15, 1\n"
"s_mul_i32 s25, s25, 18432\n"
"s_add_u32 s24, s24, s25\n"
"s_add_u32 s60, s24, s48\n"
"s_add_u32 s61, s60, 9216\n"
"s_mul_i32 s62, s15, 4160\n"
"v_bfe_u32 v15, v0, 4, 1\n"
"v_mul_u32_u24 v12, 8320, v15\n"
"v_bfe_u32 v15, v0, 2, 2\n"
"v_mul_u32_u24 v15, 2080, v15\n"
"v_add_u32 v12, v12, v15\n"
"v_bfe_u32 v15, v0, 5, 1\n"
"v_lshl_add_u32 v12, v15, 10, v12\n"
"v_bfe_u32 v15, v0, 0, 2\n"
"v_lshl_add_u32 v12, v15, 4, v12\n"
"v_add_u32 v12, s49, v12\n"
"v_add_u32 v13, 49152, v12\n"
"v_add_u32 v14, 49152, v13\n"
"v_bfe_u32 v16, v0, 4, 2\n"
"v_mul_u32_u24 v17, 294912, v16\n"
"v_bfe_u32 v16, v0, 3, 1\n"
"v_mul_u32_u24 v16, 147456, v16\n"
"v_add_u32 v17, v17, v16\n"
"v_bfe_u32 v16, v0, 2, 1\n"
"v_mul_u32_u24 v16, 36864, v16\n"
"v_add_u32 v17, v17, v16\n"
"v_bfe_u32 v16, v0, 0, 2\n"
"v_lshl_add_u32 v17, v16, 4, v17\n"
"v_add_u32 v17, 512, v17\n"
"s_mul_i32 s25, s15, 10304\n"
"s_add_u32 s24, s60, 0\n"
"s_add_u32 m0, s25, 116480\n"
"s_nop 0\n"
"buffer_load_dwordx4 v17, s[44:47], s24 offen lds\n"
"s_add_u32 s24, s60, 1179648\n"
"s_add_u32 m0, s25, 117504\n"
"s_nop 0\n"
"buffer_load_dwordx4 v17, s[44:47], s24 offen lds\n"
"s_add_u32 s24, s60, 2359296\n"
"s_add_u32 m0, s25, 118528\n"
"s_nop 0\n"
"buffer_load_dwordx4 v17, s[44:47], s24 offen lds\n"
"s_add_u32 s24, s60, 3538944\n"
"s_add_u32 m0, s25, 119552\n"
"s_nop 0\n"
"buffer_load_dwordx4 v17, s[44:47], s24 offen lds\n"
"s_add_u32 s24, s60, 4718592\n"
"s_add_u32 m0, s25, 120576\n"
"s_nop 0\n"
"buffer_load_dwordx4 v17, s[44:47], s24 offen lds\n"
"s_add_u32 s24, s61, 0\n"
"s_add_u32 m0, s25, 121632\n"
"s_nop 0\n"
"buffer_load_dwordx4 v17, s[44:47], s24 offen lds\n"
"s_add_u32 s24, s61, 1179648\n"
"s_add_u32 m0, s25, 122656\n"
"s_nop 0\n"
"buffer_load_dwordx4 v17, s[44:47], s24 offen lds\n"
"s_add_u32 s24, s61, 2359296\n"
"s_add_u32 m0, s25, 123680\n"
"s_nop 0\n"
"buffer_load_dwordx4 v17, s[44:47], s24 offen lds\n"
"s_add_u32 s24, s61, 3538944\n"
"s_add_u32 m0, s25, 124704\n"
"s_nop 0\n"
"buffer_load_dwordx4 v17, s[44:47], s24 offen lds\n"
"s_add_u32 s24, s61, 4718592\n"
"s_add_u32 m0, s25, 125728\n"
"s_nop 0\n"
"buffer_load_dwordx4 v17, s[44:47], s24 offen lds\n"
"s_add_u32 s24, s60, 0\n"
"s_add_u32 m0, s62, 0\n"
"s_nop 0\n"
"buffer_load_dwordx4 v10, s[44:47], s24 offen lds\n"
"s_add_u32 m0, s62, 1024\n"
"s_nop 0\n"
"buffer_load_dwordx4 v11, s[44:47], s24 offen lds\n"
"s_add_u32 s24, s61, 0\n"
"s_add_u32 m0, s62, 2080\n"
"s_nop 0\n"
"buffer_load_dwordx4 v10, s[44:47], s24 offen lds\n"
"s_add_u32 m0, s62, 3104\n"
"s_nop 0\n"
"buffer_load_dwordx4 v11, s[44:47], s24 offen lds\n"
"s_add_u32 s24, s60, 294912\n"
"s_add_u32 m0, s62, 16640\n"
"s_nop 0\n"
"buffer_load_dwordx4 v10, s[44:47], s24 offen lds\n"
"s_add_u32 m0, s62, 17664\n"
"s_nop 0\n"
"buffer_load_dwordx4 v11, s[44:47], s24 offen lds\n"
"s_add_u32 s24, s61, 294912\n"
"s_add_u32 m0, s62, 18720\n"
"s_nop 0\n"
"buffer_load_dwordx4 v10, s[44:47], s24 offen lds\n"
"s_add_u32 m0, s62, 19744\n"
"s_nop 0\n"
"buffer_load_dwordx4 v11, s[44:47], s24 offen lds\n"
"s_add_u32 s24, s60, 589824\n"
"s_add_u32 m0, s62, 33280\n"
"s_nop 0\n"
"buffer_load_dwordx4 v10, s[44:47], s24 offen lds\n"
"s_add_u32 m0, s62, 34304\n"
"s_nop 0\n"
"buffer_load_dwordx4 v11, s[44:47], s24 offen lds\n"
"s_add_u32 s24, s61, 589824\n"
"s_add_u32 m0, s62, 35360\n"
"s_nop 0\n"
"buffer_load_dwordx4 v10, s[44:47], s24 offen lds\n"
"s_add_u32 m0, s62, 36384\n"
"s_nop 0\n"
"buffer_load_dwordx4 v11, s[44:47], s24 offen lds\n"
"s_add_u32 s24, s60, 884736\n"
"s_add_u32 m0, s62, 49920\n"
"s_nop 0\n"
"buffer_load_dwordx4 v10, s[44:47], s24 offen lds\n"
"s_add_u32 m0, s62, 50944\n"
"s_nop 0\n"
"buffer_load_dwordx4 v11, s[44:47], s24 offen lds\n"
"s_add_u32 s24, s61, 884736\n"
"s_add_u32 m0, s62, 52000\n"
"s_nop 0\n"
"buffer_load_dwordx4 v10, s[44:47], s24 offen lds\n"
"s_add_u32 m0, s62, 53024\n"
"s_nop 0\n"
"buffer_load_dwordx4 v11, s[44:47], s24 offen lds\n"
"s_add_u32 s24, s60, 1179648\n"
"s_add_u32 m0, s62, 66560\n"
"s_nop 0\n"
"buffer_load_dwordx4 v10, s[44:47], s24 offen lds\n"
"s_add_u32 m0, s62, 67584\n"
"s_nop 0\n"
"buffer_load_dwordx4 v11, s[44:47], s24 offen lds\n"
"s_add_u32 s24, s61, 1179648\n"
"s_add_u32 m0, s62, 68640\n"
"s_nop 0\n"
"buffer_load_dwordx4 v10, s[44:47], s24 offen lds\n"
"s_add_u32 m0, s62, 69664\n"
"s_nop 0\n"
"buffer_load_dwordx4 v11, s[44:47], s24 offen lds\n"
"s_add_u32 s24, s60, 1474560\n"
"s_add_u32 m0, s62, 83200\n"
"s_nop 0\n"
"buffer_load_dwordx4 v10, s[44:47], s24 offen lds\n"
"s_add_u32 m0, s62, 84224\n"
"s_nop 0\n"
"buffer_load_dwordx4 v11, s[44:47], s24 offen lds\n"
"s_add_u32 s24, s61, 1474560\n"
"s_add_u32 m0, s62, 85280\n"
"s_nop 0\n"
"buffer_load_dwordx4 v10, s[44:47], s24 offen lds\n"
"s_add_u32 m0, s62, 86304\n"
"s_nop 0\n"
"buffer_load_dwordx4 v11, s[44:47], s24 offen lds\n"
"s_waitcnt vmcnt(20) lgkmcnt(0)\n"
"s_barrier\n"
"s_add_u32 s24, s60, 1769472\n"
"s_add_u32 m0, s62, 99840\n"
"s_nop 0\n"
"buffer_load_dwordx4 v10, s[44:47], s24 offen lds\n"
"s_add_u32 m0, s62, 100864\n"
"s_nop 0\n"
"buffer_load_dwordx4 v11, s[44:47], s24 offen lds\n"
"s_add_u32 s24, s61, 1769472\n"
"s_add_u32 m0, s62, 101920\n"
"s_nop 0\n"
"buffer_load_dwordx4 v10, s[44:47], s24 offen lds\n"
"s_add_u32 m0, s62, 102944\n"
"s_nop 0\n"
"buffer_load_dwordx4 v11, s[44:47], s24 offen lds\n"
"ds_read_b64_tr_b16 a[0:1], v12 offset:0\n"
"ds_read_b64_tr_b16 a[2:3], v12 offset:512\n"
"ds_read_b64_tr_b16 a[4:5], v12 offset:8\n"
"ds_read_b64_tr_b16 a[6:7], v12 offset:520\n"
"ds_read_b64_tr_b16 a[8:9], v12 offset:64\n"
"ds_read_b64_tr_b16 a[10:11], v12 offset:576\n"
"ds_read_b64_tr_b16 a[12:13], v12 offset:72\n"
"ds_read_b64_tr_b16 a[14:15], v12 offset:584\n"
"s_waitcnt vmcnt(20) lgkmcnt(0)\n"
"s_barrier\n"
"s_add_u32 s24, s60, 2064384\n"
"s_add_u32 m0, s62, 0\n"
"s_nop 0\n"
"buffer_load_dwordx4 v10, s[44:47], s24 offen lds\n"
"s_add_u32 m0, s62, 1024\n"
"s_nop 0\n"
"buffer_load_dwordx4 v11, s[44:47], s24 offen lds\n"
"s_add_u32 s24, s61, 2064384\n"
"s_add_u32 m0, s62, 2080\n"
"s_nop 0\n"
"buffer_load_dwordx4 v10, s[44:47], s24 offen lds\n"
"s_add_u32 m0, s62, 3104\n"
"s_nop 0\n"
"buffer_load_dwordx4 v11, s[44:47], s24 offen lds\n"
"ds_read_b64_tr_b16 a[16:17], v12 offset:16640\n"
"ds_read_b64_tr_b16 a[18:19], v12 offset:17152\n"
"ds_read_b64_tr_b16 a[20:21], v12 offset:16648\n"
"ds_read_b64_tr_b16 a[22:23], v12 offset:17160\n"
"ds_read_b64_tr_b16 a[24:25], v12 offset:16704\n"
"ds_read_b64_tr_b16 a[26:27], v12 offset:17216\n"
"ds_read_b64_tr_b16 a[28:29], v12 offset:16712\n"
"ds_read_b64_tr_b16 a[30:31], v12 offset:17224\n"
"s_waitcnt vmcnt(20) lgkmcnt(0)\n"
"s_barrier\n"
"s_add_u32 s24, s60, 2359296\n"
"s_add_u32 m0, s62, 16640\n"
"s_nop 0\n"
"buffer_load_dwordx4 v10, s[44:47], s24 offen lds\n"
"s_add_u32 m0, s62, 17664\n"
"s_nop 0\n"
"buffer_load_dwordx4 v11, s[44:47], s24 offen lds\n"
"s_add_u32 s24, s61, 2359296\n"
"s_add_u32 m0, s62, 18720\n"
"s_nop 0\n"
"buffer_load_dwordx4 v10, s[44:47], s24 offen lds\n"
"s_add_u32 m0, s62, 19744\n"
"s_nop 0\n"
"buffer_load_dwordx4 v11, s[44:47], s24 offen lds\n"
"ds_read_b64_tr_b16 a[32:33], v12 offset:33280\n"
"ds_read_b64_tr_b16 a[34:35], v12 offset:33792\n"
"ds_read_b64_tr_b16 a[36:37], v12 offset:33288\n"
"ds_read_b64_tr_b16 a[38:39], v12 offset:33800\n"
"ds_read_b64_tr_b16 a[40:41], v12 offset:33344\n"
"ds_read_b64_tr_b16 a[42:43], v12 offset:33856\n"
"ds_read_b64_tr_b16 a[44:45], v12 offset:33352\n"
"ds_read_b64_tr_b16 a[46:47], v12 offset:33864\n"
"s_waitcnt vmcnt(20) lgkmcnt(0)\n"
"s_barrier\n"
"s_add_u32 s24, s60, 2654208\n"
"s_add_u32 m0, s62, 33280\n"
"s_nop 0\n"
"buffer_load_dwordx4 v10, s[44:47], s24 offen lds\n"
"s_add_u32 m0, s62, 34304\n"
"s_nop 0\n"
"buffer_load_dwordx4 v11, s[44:47], s24 offen lds\n"
"s_add_u32 s24, s61, 2654208\n"
"s_add_u32 m0, s62, 35360\n"
"s_nop 0\n"
"buffer_load_dwordx4 v10, s[44:47], s24 offen lds\n"
"s_add_u32 m0, s62, 36384\n"
"s_nop 0\n"
"buffer_load_dwordx4 v11, s[44:47], s24 offen lds\n"
"ds_read_b64_tr_b16 a[48:49], v13 offset:768\n"
"ds_read_b64_tr_b16 a[50:51], v13 offset:1280\n"
"ds_read_b64_tr_b16 a[52:53], v13 offset:776\n"
"ds_read_b64_tr_b16 a[54:55], v13 offset:1288\n"
"ds_read_b64_tr_b16 a[56:57], v13 offset:832\n"
"ds_read_b64_tr_b16 a[58:59], v13 offset:1344\n"
"ds_read_b64_tr_b16 a[60:61], v13 offset:840\n"
"ds_read_b64_tr_b16 a[62:63], v13 offset:1352\n"
"s_waitcnt vmcnt(20) lgkmcnt(0)\n"
"s_barrier\n"
"s_add_u32 s24, s60, 2949120\n"
"s_add_u32 m0, s62, 49920\n"
"s_nop 0\n"
"buffer_load_dwordx4 v10, s[44:47], s24 offen lds\n"
"s_add_u32 m0, s62, 50944\n"
"s_nop 0\n"
"buffer_load_dwordx4 v11, s[44:47], s24 offen lds\n"
"s_add_u32 s24, s61, 2949120\n"
"s_add_u32 m0, s62, 52000\n"
"s_nop 0\n"
"buffer_load_dwordx4 v10, s[44:47], s24 offen lds\n"
"s_add_u32 m0, s62, 53024\n"
"s_nop 0\n"
"buffer_load_dwordx4 v11, s[44:47], s24 offen lds\n"
"ds_read_b64_tr_b16 a[64:65], v13 offset:17408\n"
"ds_read_b64_tr_b16 a[66:67], v13 offset:17920\n"
"ds_read_b64_tr_b16 a[68:69], v13 offset:17416\n"
"ds_read_b64_tr_b16 a[70:71], v13 offset:17928\n"
"ds_read_b64_tr_b16 a[72:73], v13 offset:17472\n"
"ds_read_b64_tr_b16 a[74:75], v13 offset:17984\n"
"ds_read_b64_tr_b16 a[76:77], v13 offset:17480\n"
"ds_read_b64_tr_b16 a[78:79], v13 offset:17992\n"
"s_waitcnt vmcnt(20) lgkmcnt(0)\n"
"s_barrier\n"
"s_add_u32 s24, s60, 3244032\n"
"s_add_u32 m0, s62, 66560\n"
"s_nop 0\n"
"buffer_load_dwordx4 v10, s[44:47], s24 offen lds\n"
"s_add_u32 m0, s62, 67584\n"
"s_nop 0\n"
"buffer_load_dwordx4 v11, s[44:47], s24 offen lds\n"
"s_add_u32 s24, s61, 3244032\n"
"s_add_u32 m0, s62, 68640\n"
"s_nop 0\n"
"buffer_load_dwordx4 v10, s[44:47], s24 offen lds\n"
"s_add_u32 m0, s62, 69664\n"
"s_nop 0\n"
"buffer_load_dwordx4 v11, s[44:47], s24 offen lds\n"
"ds_read_b64_tr_b16 a[80:81], v13 offset:34048\n"
"ds_read_b64_tr_b16 a[82:83], v13 offset:34560\n"
"ds_read_b64_tr_b16 a[84:85], v13 offset:34056\n"
"ds_read_b64_tr_b16 a[86:87], v13 offset:34568\n"
"ds_read_b64_tr_b16 a[88:89], v13 offset:34112\n"
"ds_read_b64_tr_b16 a[90:91], v13 offset:34624\n"
"ds_read_b64_tr_b16 a[92:93], v13 offset:34120\n"
"ds_read_b64_tr_b16 a[94:95], v13 offset:34632\n"
"s_waitcnt vmcnt(20) lgkmcnt(0)\n"
"s_barrier\n"
"s_add_u32 s24, s60, 3538944\n"
"s_add_u32 m0, s62, 83200\n"
"s_nop 0\n"
"buffer_load_dwordx4 v10, s[44:47], s24 offen lds\n"
"s_add_u32 m0, s62, 84224\n"
"s_nop 0\n"
"buffer_load_dwordx4 v11, s[44:47], s24 offen lds\n"
"s_add_u32 s24, s61, 3538944\n"
"s_add_u32 m0, s62, 85280\n"
"s_nop 0\n"
"buffer_load_dwordx4 v10, s[44:47], s24 offen lds\n"
"s_add_u32 m0, s62, 86304\n"
"s_nop 0\n"
"buffer_load_dwordx4 v11, s[44:47], s24 offen lds\n"
"ds_read_b64_tr_b16 a[96:97], v14 offset:1536\n"
"ds_read_b64_tr_b16 a[98:99], v14 offset:2048\n"
"ds_read_b64_tr_b16 a[100:101], v14 offset:1544\n"
"ds_read_b64_tr_b16 a[102:103], v14 offset:2056\n"
"ds_read_b64_tr_b16 a[104:105], v14 offset:1600\n"
"ds_read_b64_tr_b16 a[106:107], v14 offset:2112\n"
"ds_read_b64_tr_b16 a[108:109], v14 offset:1608\n"
"ds_read_b64_tr_b16 a[110:111], v14 offset:2120\n"
"s_waitcnt vmcnt(20) lgkmcnt(0)\n"
"s_barrier\n"
"s_add_u32 s24, s60, 3833856\n"
"s_add_u32 m0, s62, 99840\n"
"s_nop 0\n"
"buffer_load_dwordx4 v10, s[44:47], s24 offen lds\n"
"s_add_u32 m0, s62, 100864\n"
"s_nop 0\n"
"buffer_load_dwordx4 v11, s[44:47], s24 offen lds\n"
"s_add_u32 s24, s61, 3833856\n"
"s_add_u32 m0, s62, 101920\n"
"s_nop 0\n"
"buffer_load_dwordx4 v10, s[44:47], s24 offen lds\n"
"s_add_u32 m0, s62, 102944\n"
"s_nop 0\n"
"buffer_load_dwordx4 v11, s[44:47], s24 offen lds\n"
"ds_read_b64_tr_b16 a[112:113], v12 offset:0\n"
"ds_read_b64_tr_b16 a[114:115], v12 offset:512\n"
"ds_read_b64_tr_b16 a[116:117], v12 offset:8\n"
"ds_read_b64_tr_b16 a[118:119], v12 offset:520\n"
"ds_read_b64_tr_b16 a[120:121], v12 offset:64\n"
"ds_read_b64_tr_b16 a[122:123], v12 offset:576\n"
"ds_read_b64_tr_b16 a[124:125], v12 offset:72\n"
"ds_read_b64_tr_b16 a[126:127], v12 offset:584\n"
"s_waitcnt vmcnt(20) lgkmcnt(0)\n"
"s_barrier\n"
"s_add_u32 s24, s60, 4128768\n"
"s_add_u32 m0, s62, 0\n"
"s_nop 0\n"
"buffer_load_dwordx4 v10, s[44:47], s24 offen lds\n"
"s_add_u32 m0, s62, 1024\n"
"s_nop 0\n"
"buffer_load_dwordx4 v11, s[44:47], s24 offen lds\n"
"s_add_u32 s24, s61, 4128768\n"
"s_add_u32 m0, s62, 2080\n"
"s_nop 0\n"
"buffer_load_dwordx4 v10, s[44:47], s24 offen lds\n"
"s_add_u32 m0, s62, 3104\n"
"s_nop 0\n"
"buffer_load_dwordx4 v11, s[44:47], s24 offen lds\n"
"ds_read_b64_tr_b16 a[128:129], v12 offset:16640\n"
"ds_read_b64_tr_b16 a[130:131], v12 offset:17152\n"
"ds_read_b64_tr_b16 a[132:133], v12 offset:16648\n"
"ds_read_b64_tr_b16 a[134:135], v12 offset:17160\n"
"ds_read_b64_tr_b16 a[136:137], v12 offset:16704\n"
"ds_read_b64_tr_b16 a[138:139], v12 offset:17216\n"
"ds_read_b64_tr_b16 a[140:141], v12 offset:16712\n"
"ds_read_b64_tr_b16 a[142:143], v12 offset:17224\n"
"s_waitcnt vmcnt(20) lgkmcnt(0)\n"
"s_barrier\n"
"s_add_u32 s24, s60, 4423680\n"
"s_add_u32 m0, s62, 16640\n"
"s_nop 0\n"
"buffer_load_dwordx4 v10, s[44:47], s24 offen lds\n"
"s_add_u32 m0, s62, 17664\n"
"s_nop 0\n"
"buffer_load_dwordx4 v11, s[44:47], s24 offen lds\n"
"s_add_u32 s24, s61, 4423680\n"
"s_add_u32 m0, s62, 18720\n"
"s_nop 0\n"
"buffer_load_dwordx4 v10, s[44:47], s24 offen lds\n"
"s_add_u32 m0, s62, 19744\n"
"s_nop 0\n"
"buffer_load_dwordx4 v11, s[44:47], s24 offen lds\n"
"ds_read_b64_tr_b16 a[144:145], v12 offset:33280\n"
"ds_read_b64_tr_b16 a[146:147], v12 offset:33792\n"
"ds_read_b64_tr_b16 a[148:149], v12 offset:33288\n"
"ds_read_b64_tr_b16 a[150:151], v12 offset:33800\n"
"ds_read_b64_tr_b16 a[152:153], v12 offset:33344\n"
"ds_read_b64_tr_b16 a[154:155], v12 offset:33856\n"
"ds_read_b64_tr_b16 a[156:157], v12 offset:33352\n"
"ds_read_b64_tr_b16 a[158:159], v12 offset:33864\n"
"s_waitcnt vmcnt(20) lgkmcnt(0)\n"
"s_barrier\n"
"s_add_u32 s24, s60, 4718592\n"
"s_add_u32 m0, s62, 33280\n"
"s_nop 0\n"
"buffer_load_dwordx4 v10, s[44:47], s24 offen lds\n"
"s_add_u32 m0, s62, 34304\n"
"s_nop 0\n"
"buffer_load_dwordx4 v11, s[44:47], s24 offen lds\n"
"s_add_u32 s24, s61, 4718592\n"
"s_add_u32 m0, s62, 35360\n"
"s_nop 0\n"
"buffer_load_dwordx4 v10, s[44:47], s24 offen lds\n"
"s_add_u32 m0, s62, 36384\n"
"s_nop 0\n"
"buffer_load_dwordx4 v11, s[44:47], s24 offen lds\n"
"ds_read_b64_tr_b16 a[160:161], v13 offset:768\n"
"ds_read_b64_tr_b16 a[162:163], v13 offset:1280\n"
"ds_read_b64_tr_b16 a[164:165], v13 offset:776\n"
"ds_read_b64_tr_b16 a[166:167], v13 offset:1288\n"
"ds_read_b64_tr_b16 a[168:169], v13 offset:832\n"
"ds_read_b64_tr_b16 a[170:171], v13 offset:1344\n"
"ds_read_b64_tr_b16 a[172:173], v13 offset:840\n"
"ds_read_b64_tr_b16 a[174:175], v13 offset:1352\n"
"s_waitcnt vmcnt(20) lgkmcnt(0)\n"
"s_barrier\n"
"s_add_u32 s24, s60, 5013504\n"
"s_add_u32 m0, s62, 49920\n"
"s_nop 0\n"
"buffer_load_dwordx4 v10, s[44:47], s24 offen lds\n"
"s_add_u32 m0, s62, 50944\n"
"s_nop 0\n"
"buffer_load_dwordx4 v11, s[44:47], s24 offen lds\n"
"s_add_u32 s24, s61, 5013504\n"
"s_add_u32 m0, s62, 52000\n"
"s_nop 0\n"
"buffer_load_dwordx4 v10, s[44:47], s24 offen lds\n"
"s_add_u32 m0, s62, 53024\n"
"s_nop 0\n"
"buffer_load_dwordx4 v11, s[44:47], s24 offen lds\n"
"ds_read_b64_tr_b16 a[176:177], v13 offset:17408\n"
"ds_read_b64_tr_b16 a[178:179], v13 offset:17920\n"
"ds_read_b64_tr_b16 a[180:181], v13 offset:17416\n"
"ds_read_b64_tr_b16 a[182:183], v13 offset:17928\n"
"ds_read_b64_tr_b16 a[184:185], v13 offset:17472\n"
"ds_read_b64_tr_b16 a[186:187], v13 offset:17984\n"
"ds_read_b64_tr_b16 a[188:189], v13 offset:17480\n"
"ds_read_b64_tr_b16 a[190:191], v13 offset:17992\n"
"s_waitcnt vmcnt(20) lgkmcnt(0)\n"
"s_barrier\n"
"ds_read_b64_tr_b16 a[192:193], v13 offset:34048\n"
"ds_read_b64_tr_b16 a[194:195], v13 offset:34560\n"
"ds_read_b64_tr_b16 a[196:197], v13 offset:34056\n"
"ds_read_b64_tr_b16 a[198:199], v13 offset:34568\n"
"ds_read_b64_tr_b16 a[200:201], v13 offset:34112\n"
"ds_read_b64_tr_b16 a[202:203], v13 offset:34624\n"
"ds_read_b64_tr_b16 a[204:205], v13 offset:34120\n"
"ds_read_b64_tr_b16 a[206:207], v13 offset:34632\n"
"s_waitcnt vmcnt(16) lgkmcnt(0)\n"
"s_barrier\n"
"ds_read_b64_tr_b16 a[208:209], v14 offset:1536\n"
"ds_read_b64_tr_b16 a[210:211], v14 offset:2048\n"
"ds_read_b64_tr_b16 a[212:213], v14 offset:1544\n"
"ds_read_b64_tr_b16 a[214:215], v14 offset:2056\n"
"ds_read_b64_tr_b16 a[216:217], v14 offset:1600\n"
"ds_read_b64_tr_b16 a[218:219], v14 offset:2112\n"
"ds_read_b64_tr_b16 a[220:221], v14 offset:1608\n"
"ds_read_b64_tr_b16 a[222:223], v14 offset:2120\n"
"s_waitcnt vmcnt(12) lgkmcnt(0)\n"
"s_barrier\n"
"ds_read_b64_tr_b16 a[224:225], v12 offset:0\n"
"ds_read_b64_tr_b16 a[226:227], v12 offset:512\n"
"ds_read_b64_tr_b16 a[228:229], v12 offset:8\n"
"ds_read_b64_tr_b16 a[230:231], v12 offset:520\n"
"ds_read_b64_tr_b16 a[232:233], v12 offset:64\n"
"ds_read_b64_tr_b16 a[234:235], v12 offset:576\n"
"ds_read_b64_tr_b16 a[236:237], v12 offset:72\n"
"ds_read_b64_tr_b16 a[238:239], v12 offset:584\n"
"s_waitcnt vmcnt(8) lgkmcnt(0)\n"
"s_barrier\n"
"ds_read_b64_tr_b16 a[240:241], v12 offset:16640\n"
"ds_read_b64_tr_b16 a[242:243], v12 offset:17152\n"
"ds_read_b64_tr_b16 a[244:245], v12 offset:16648\n"
"ds_read_b64_tr_b16 a[246:247], v12 offset:17160\n"
"ds_read_b64_tr_b16 a[248:249], v12 offset:16704\n"
"ds_read_b64_tr_b16 a[250:251], v12 offset:17216\n"
"ds_read_b64_tr_b16 a[252:253], v12 offset:16712\n"
"ds_read_b64_tr_b16 a[254:255], v12 offset:17224\n"
"s_waitcnt vmcnt(4) lgkmcnt(0)\n"
"s_barrier\n"
"ds_read_b64_tr_b16 v[252:253], v12 offset:33280\n"
"ds_read_b64_tr_b16 v[254:255], v12 offset:33792\n"
"ds_read_b64_tr_b16 v[248:249], v12 offset:33288\n"
"ds_read_b64_tr_b16 v[250:251], v12 offset:33800\n"
"ds_read_b64_tr_b16 v[244:245], v12 offset:33344\n"
"ds_read_b64_tr_b16 v[246:247], v12 offset:33856\n"
"ds_read_b64_tr_b16 v[240:241], v12 offset:33352\n"
"ds_read_b64_tr_b16 v[242:243], v12 offset:33864\n"
"s_waitcnt vmcnt(0) lgkmcnt(0)\n"
"s_barrier\n"
"ds_read_b64_tr_b16 v[236:237], v13 offset:768\n"
"ds_read_b64_tr_b16 v[238:239], v13 offset:1280\n"
"ds_read_b64_tr_b16 v[232:233], v13 offset:776\n"
"ds_read_b64_tr_b16 v[234:235], v13 offset:1288\n"
"ds_read_b64_tr_b16 v[228:229], v13 offset:832\n"
"ds_read_b64_tr_b16 v[230:231], v13 offset:1344\n"
"ds_read_b64_tr_b16 v[224:225], v13 offset:840\n"
"ds_read_b64_tr_b16 v[226:227], v13 offset:1352\n"
"s_waitcnt lgkmcnt(0)\n"
"s_lshr_b32 s24, s15, 1\n"
"s_lshl_b32 s24, s24, 5\n"
"v_bfe_u32 v16, v0, 4, 1\n"
"v_mul_u32_u24 v17, 20608, v16\n"
"v_bfe_u32 v16, v0, 2, 2\n"
"v_mul_u32_u24 v16, 5152, v16\n"
"v_add_u32 v17, v17, v16\n"
"v_bfe_u32 v16, v0, 5, 1\n"
"v_lshl_add_u32 v17, v16, 7, v17\n"
"v_bfe_u32 v16, v0, 0, 2\n"
"v_lshl_add_u32 v17, v16, 3, v17\n"
"v_add_u32 v17, 116480, v17\n"
"v_add_u32 v17, s24, v17\n"
"ds_read_b64_tr_b16 v[220:221], v17 offset:0\n"
"ds_read_b64_tr_b16 v[222:223], v17 offset:64\n"
"ds_read_b64_tr_b16 v[216:217], v17 offset:256\n"
"ds_read_b64_tr_b16 v[218:219], v17 offset:320\n"
"ds_read_b64_tr_b16 v[212:213], v17 offset:512\n"
"ds_read_b64_tr_b16 v[214:215], v17 offset:576\n"
"ds_read_b64_tr_b16 v[208:209], v17 offset:768\n"
"ds_read_b64_tr_b16 v[210:211], v17 offset:832\n"
"ds_read_b64_tr_b16 v[204:205], v17 offset:1024\n"
"ds_read_b64_tr_b16 v[206:207], v17 offset:1088\n"
"ds_read_b64_tr_b16 v[200:201], v17 offset:1280\n"
"ds_read_b64_tr_b16 v[202:203], v17 offset:1344\n"
"ds_read_b64_tr_b16 v[196:197], v17 offset:1536\n"
"ds_read_b64_tr_b16 v[198:199], v17 offset:1600\n"
"ds_read_b64_tr_b16 v[192:193], v17 offset:1792\n"
"ds_read_b64_tr_b16 v[194:195], v17 offset:1856\n"
"ds_read_b64_tr_b16 v[188:189], v17 offset:2048\n"
"ds_read_b64_tr_b16 v[190:191], v17 offset:2112\n"
"ds_read_b64_tr_b16 v[184:185], v17 offset:2304\n"
"ds_read_b64_tr_b16 v[186:187], v17 offset:2368\n"
"ds_read_b64_tr_b16 v[180:181], v17 offset:2560\n"
"ds_read_b64_tr_b16 v[182:183], v17 offset:2624\n"
"ds_read_b64_tr_b16 v[176:177], v17 offset:2816\n"
"ds_read_b64_tr_b16 v[178:179], v17 offset:2880\n"
"ds_read_b64_tr_b16 v[172:173], v17 offset:3072\n"
"ds_read_b64_tr_b16 v[174:175], v17 offset:3136\n"
"ds_read_b64_tr_b16 v[168:169], v17 offset:3328\n"
"ds_read_b64_tr_b16 v[170:171], v17 offset:3392\n"
"ds_read_b64_tr_b16 v[164:165], v17 offset:3584\n"
"ds_read_b64_tr_b16 v[166:167], v17 offset:3648\n"
"ds_read_b64_tr_b16 v[160:161], v17 offset:3840\n"
"ds_read_b64_tr_b16 v[162:163], v17 offset:3904\n"
"ds_read_b64_tr_b16 v[156:157], v17 offset:4096\n"
"ds_read_b64_tr_b16 v[158:159], v17 offset:4160\n"
"ds_read_b64_tr_b16 v[152:153], v17 offset:4352\n"
"ds_read_b64_tr_b16 v[154:155], v17 offset:4416\n"
"s_waitcnt lgkmcnt(0)\n"
"s_barrier\n"
"s_lshl_b32 s39, s15, 10\n"
"v_lshlrev_b32 v4, 4, v0\n"
"v_add_u32 v4, s39, v4\n"
"v_bfe_u32 v3, v0, 4, 2\n"
"v_mul_u32_u24 v5, 512, v3\n"
"v_bfe_u32 v3, v0, 0, 4\n"
"v_lshl_add_u32 v5, v3, 4, v5\n"
"s_and_b32 s24, s15, 1\n"
"s_lshl_b32 s24, s24, 8\n"
"v_add_u32 v5, s24, v5\n"
"v_xor_b32 v6, 256, v5\n"
"s_mov_b64 s[32:33], s[8:9]\n"
"s_mov_b32 s35, s11\n"
"s_mov_b32 s36, 0\n"
"s_mov_b32 s37, 7\n"
"s_mov_b32 s28, 0\n"
"s_mov_b32 s41, 36864\n"
"s_mov_b32 s43, -1\n"
"v_mov_b32 v110, 163776\n"
"v_mov_b32 v111, 0\n"
"v_mov_b32 v112, 0\n"
"ds_write_b32 v110, v111\n"
"s_waitcnt lgkmcnt(0)\n"
"s_barrier\n"
"s_add_u32 s31, s14, 92\n"
"s_lshr_b32 s27, s31, 6\n"
"s_lshl_b32 s27, s27, 13\n"
"s_lshl_b32 s31, s31, 7\n"
"s_add_u32 s31, s31, s27\n"
"s_add_u32 s31, s31, s16\n"
"s_add_u32 s31, s31, 1835008\n"
"v_lshrrev_b32 v17, 4, v0\n"
"v_add_u32 v17, 120, v17\n"
"v_lshrrev_b32 v16, 6, v17\n"
"v_lshlrev_b32 v16, 13, v16\n"
"v_lshl_add_u32 v1, v17, 7, v16\n"
"v_add_u32 v1, 1835008, v1\n"
"v_add_u32 v1, s16, v1\n"
"v_mov_b32 v7, v5\n"
"v_mov_b32 v8, v6\n"
"s_lshr_b32 s24, s15, 1\n"
"s_lshl_b32 s24, s24, 10\n"
"s_and_b32 s25, s15, 1\n"
"s_lshl_b32 s25, s25, 8\n"
"s_add_u32 s24, s24, s25\n"
"v_bfe_u32 v16, v0, 5, 1\n"
"v_mul_u32_u24 v9, 512, v16\n"
"v_bfe_u32 v16, v0, 0, 4\n"
"v_lshl_add_u32 v9, v16, 4, v9\n"
"v_bfe_u32 v16, v0, 4, 1\n"
"v_lshl_add_u32 v9, v16, 3, v9\n"
"v_add_u32 v9, s24, v9\n"
"s_mul_i32 s24, s14, 9\n"
"s_lshl_b32 s25, s15, 1\n"
"s_add_u32 s25, s25, s24\n"
"s_add_u32 s26, s25, 0\n"
"s_lshr_b32 s27, s26, 2\n"
"s_lshl_b32 s27, s27, 13\n"
"s_lshl_b32 s50, s26, 11\n"
"s_add_u32 s50, s50, s27\n"
"s_add_u32 s50, s50, s17\n"
"s_add_u32 s50, s50, 2097152\n"
"s_add_u32 s26, s25, 1\n"
"s_lshr_b32 s27, s26, 2\n"
"s_lshl_b32 s27, s27, 13\n"
"s_lshl_b32 s51, s26, 11\n"
"s_add_u32 s51, s51, s27\n"
"s_add_u32 s51, s51, s17\n"
"s_add_u32 s51, s51, 2097152\n"
"s_add_u32 s26, s24, 8\n"
"s_lshr_b32 s27, s26, 2\n"
"s_lshl_b32 s27, s27, 13\n"
"s_lshl_b32 s52, s26, 11\n"
"s_add_u32 s52, s52, s27\n"
"s_add_u32 s52, s52, s17\n"
"s_add_u32 s52, s52, 2097152\n"
"v_bfe_u32 v16, v0, 4, 2\n"
"v_mul_u32_u24 v17, 16, v16\n"
"v_add_u32 v17, s48, v17\n"
"v_add_u32 v17, s49, v17\n"
"buffer_load_dwordx4 v[24:27], v17, s[64:67], 0 offen offset:0\n"
"buffer_load_dwordx4 v[28:31], v17, s[64:67], 0 offen offset:64\n"
"s_lshr_b32 s24, s15, 1\n"
"s_lshl_b32 s24, s24, 5\n"
"v_bfe_u32 v16, v0, 4, 2\n"
"v_mul_u32_u24 v18, 8, v16\n"
"v_add_u32 v18, s48, v18\n"
"v_add_u32 v18, s24, v18\n"
"buffer_load_dwordx2 v[32:33], v18, s[64:67], 0 offen offset:512\n"
"s_waitcnt vmcnt(2)\n"
"v_lshlrev_b32 v76, 16, v24\n"
"v_and_b32 v77, 4294901760, v24\n"
"v_lshlrev_b32 v78, 16, v25\n"
"v_and_b32 v79, 4294901760, v25\n"
"v_lshlrev_b32 v80, 16, v26\n"
"v_and_b32 v81, 4294901760, v26\n"
"v_lshlrev_b32 v82, 16, v27\n"
"v_and_b32 v83, 4294901760, v27\n"
"s_waitcnt vmcnt(1)\n"
"v_lshlrev_b32 v84, 16, v28\n"
"v_and_b32 v85, 4294901760, v28\n"
"v_lshlrev_b32 v86, 16, v29\n"
"v_and_b32 v87, 4294901760, v29\n"
"v_lshlrev_b32 v88, 16, v30\n"
"v_and_b32 v89, 4294901760, v30\n"
"v_lshlrev_b32 v90, 16, v31\n"
"v_and_b32 v91, 4294901760, v31\n"
"s_waitcnt vmcnt(0)\n"
"v_lshlrev_b32 v92, 16, v32\n"
"v_and_b32 v93, 4294901760, v32\n"
"v_lshlrev_b32 v94, 16, v33\n"
"v_and_b32 v95, 4294901760, v33\n"
"v_mov_b32 v96, 0xc0135761\n"
"s_mov_b32 s58, 0xbdd2d3e8\n"
"v_mov_b32 v97, 0xc0135761\n"
"s_mov_b32 s59, 0xbdd2d3e8\n"
"s_and_b32 s38, s28, 15\n"
"s_lshl_b32 s38, s38, 22\n"
"s_add_u32 s38, s38, 33554432\n"
"s_add_u32 s38, s38, s17\n"
"s_add_u32 s38, s38, 3342336\n"
"MK_F1S_wait:\n"
"s_mov_b32 s29, s30\n"
"buffer_load_dword v3, v1, s[8:11], 0 offen sc1\n"
"s_waitcnt vmcnt(0)\n"
"s_mov_b64 exec, 1\n"
"ds_add_rtn_u32 v112, v110, v111\n"
"s_mov_b64 exec, -1\n"
"s_waitcnt lgkmcnt(0)\n"
"v_and_b32 v113, 3, v112\n"
"v_cmp_eq_u32 vcc, s37, v113\n"
"v_mov_b32 v114, s43\n"
"s_nop 0\n"
"v_cndmask_b32 v113, -1, 0, vcc\n"
"buffer_store_dword v114, v113, s[8:11], s31 offen sc1\n"
"s_mov_b32 s37, 7\n"
"v_mov_b32 v111, 0\n"
"s_branch MK_F1S_poll_in\n"
"MK_F1S_poll:\n"
"buffer_load_dword v3, v1, s[8:11], 0 offen sc1\n"
"s_add_u32 s29, s29, 1\n"
"s_waitcnt vmcnt(0)\n"
"MK_F1S_poll_in:\n"
"v_cmp_gt_i32 vcc, s28, v3\n"
"s_cbranch_vccz MK_F1S_poll_ok\n"
"s_cmp_lt_u32 s29, 131072\n"
"s_cbranch_scc1 MK_F1S_poll\n"
"s_mov_b32 s30, 131072\n"
"MK_F1S_poll_ok:\n"
"s_add_u32 m0, s39, 0\n"
"s_add_u32 s26, s38, 0\n"
"buffer_load_dwordx4 v4, s[8:11], s26 offen sc1 lds\n"
"s_add_u32 m0, s39, 4096\n"
"s_add_u32 s26, s38, 4096\n"
"buffer_load_dwordx4 v4, s[8:11], s26 offen sc1 lds\n"
"s_add_u32 m0, s39, 8192\n"
"s_add_u32 s26, s38, 16384\n"
"buffer_load_dwordx4 v4, s[8:11], s26 offen sc1 lds\n"
"s_add_u32 m0, s39, 12288\n"
"s_add_u32 s26, s38, 20480\n"
"buffer_load_dwordx4 v4, s[8:11], s26 offen sc1 lds\n"
"s_add_u32 m0, s39, 16384\n"
"s_add_u32 s26, s38, 32768\n"
"buffer_load_dwordx4 v4, s[8:11], s26 offen sc1 lds\n"
"s_add_u32 m0, s39, 20480\n"
"s_add_u32 s26, s38, 36864\n"
"buffer_load_dwordx4 v4, s[8:11], s26 offen sc1 lds\n"
"s_waitcnt vmcnt(0)\n"
"s_barrier\n"
"ds_read_b128 v[48:51], v5 offset:0\n"
"ds_read_b128 v[52:55], v5 offset:2048\n"
"ds_read_b128 v[56:59], v5 offset:4096\n"
"ds_read_b128 v[60:63], v5 offset:6144\n"
"MK_F1S_body:\n"
"s_waitcnt lgkmcnt(2)\n"
"v_mfma_f32_16x16x32_bf16 v[16:19], a[0:3], v[48:51], v[76:79]\n"
"ds_read_b128 v[64:67], v5 offset:8192\n"
"s_add_u32 m0, s39, 24576\n"
"s_add_u32 s26, s38, 49152\n"
"v_mfma_f32_16x16x32_bf16 v[24:27], a[4:7], v[48:51], v[80:83]\n"
"ds_read_b128 v[68:71], v5 offset:10240\n"
"s_and_b32 s40, s28, 15\n"
"s_lshl_b32 s40, s40, 22\n"
"v_mfma_f32_16x16x32_bf16 v[32:35], a[8:11], v[48:51], v[84:87]\n"
"buffer_load_dwordx4 v4, s[8:11], s26 offen sc1 lds\n"
"v_mfma_f32_16x16x32_bf16 v[40:43], a[12:15], v[48:51], v[88:91]\n"
"s_add_u32 s40, s40, 33554432\n"
"s_add_u32 s53, s40, s50\n"
"v_mfma_f32_16x16x32_bf16 v[72:75], v[220:223], v[48:51], v[92:95]\n"
"s_add_u32 m0, s39, 28672\n"
"s_add_u32 s26, s38, 53248\n"
"s_add_u32 s54, s40, s51\n"
"v_mfma_f32_16x16x32_bf16 v[16:19], a[16:19], v[52:55], v[16:19]\n"
"buffer_load_dwordx4 v4, s[8:11], s26 offen sc1 lds\n"
"v_mfma_f32_16x16x32_bf16 v[24:27], a[20:23], v[52:55], v[24:27]\n"
"s_add_u32 s55, s40, s52\n"
"v_mfma_f32_16x16x32_bf16 v[32:35], a[24:27], v[52:55], v[32:35]\n"
"s_add_u32 m0, s39, 32768\n"
"s_add_u32 s26, s38, 65536\n"
"v_mfma_f32_16x16x32_bf16 v[40:43], a[28:31], v[52:55], v[40:43]\n"
"buffer_load_dwordx4 v4, s[8:11], s26 offen sc1 lds\n"
"v_mfma_f32_16x16x32_bf16 v[72:75], v[216:219], v[52:55], v[72:75]\n"
"s_waitcnt lgkmcnt(2)\n"
"v_mfma_f32_16x16x32_bf16 v[16:19], a[32:35], v[56:59], v[16:19]\n"
"ds_read_b128 v[48:51], v5 offset:12288\n"
"v_mfma_f32_16x16x32_bf16 v[24:27], a[36:39], v[56:59], v[24:27]\n"
"ds_read_b128 v[52:55], v5 offset:14336\n"
"v_mfma_f32_16x16x32_bf16 v[32:35], a[40:43], v[56:59], v[32:35]\n"
"v_mfma_f32_16x16x32_bf16 v[40:43], a[44:47], v[56:59], v[40:43]\n"
"v_mfma_f32_16x16x32_bf16 v[72:75], v[212:215], v[56:59], v[72:75]\n"
"v_mfma_f32_16x16x32_bf16 v[16:19], a[48:51], v[60:63], v[16:19]\n"
"v_mfma_f32_16x16x32_bf16 v[24:27], a[52:55], v[60:63], v[24:27]\n"
"v_mfma_f32_16x16x32_bf16 v[32:35], a[56:59], v[60:63], v[32:35]\n"
"v_mfma_f32_16x16x32_bf16 v[40:43], a[60:63], v[60:63], v[40:43]\n"
"v_mfma_f32_16x16x32_bf16 v[72:75], v[208:211], v[60:63], v[72:75]\n"
"s_waitcnt lgkmcnt(2)\n"
"v_mfma_f32_16x16x32_bf16 v[16:19], a[64:67], v[64:67], v[16:19]\n"
"ds_read_b128 v[56:59], v5 offset:16384\n"
"v_mfma_f32_16x16x32_bf16 v[24:27], a[68:71], v[64:67], v[24:27]\n"
"ds_read_b128 v[60:63], v5 offset:18432\n"
"v_mfma_f32_16x16x32_bf16 v[32:35], a[72:75], v[64:67], v[32:35]\n"
"v_mfma_f32_16x16x32_bf16 v[40:43], a[76:79], v[64:67], v[40:43]\n"
"v_mfma_f32_16x16x32_bf16 v[72:75], v[204:207], v[64:67], v[72:75]\n"
"v_mfma_f32_16x16x32_bf16 v[16:19], a[80:83], v[68:71], v[16:19]\n"
"v_mfma_f32_16x16x32_bf16 v[24:27], a[84:87], v[68:71], v[24:27]\n"
"v_mfma_f32_16x16x32_bf16 v[32:35], a[88:91], v[68:71], v[32:35]\n"
"v_mfma_f32_16x16x32_bf16 v[40:43], a[92:95], v[68:71], v[40:43]\n"
"v_mfma_f32_16x16x32_bf16 v[72:75], v[200:203], v[68:71], v[72:75]\n"
"s_waitcnt lgkmcnt(2)\n"
"v_mfma_f32_16x16x32_bf16 v[16:19], a[96:99], v[48:51], v[16:19]\n"
"ds_read_b128 v[64:67], v5 offset:20480\n"
"v_mfma_f32_16x16x32_bf16 v[24:27], a[100:103], v[48:51], v[24:27]\n"
"ds_read_b128 v[68:71], v5 offset:22528\n"
"v_mfma_f32_16x16x32_bf16 v[32:35], a[104:107], v[48:51], v[32:35]\n"
"v_mfma_f32_16x16x32_bf16 v[40:43], a[108:111], v[48:51], v[40:43]\n"
"v_mfma_f32_16x16x32_bf16 v[72:75], v[196:199], v[48:51], v[72:75]\n"
"v_mfma_f32_16x16x32_bf16 v[16:19], a[112:115], v[52:55], v[16:19]\n"
"v_mfma_f32_16x16x32_bf16 v[24:27], a[116:119], v[52:55], v[24:27]\n"
"v_mfma_f32_16x16x32_bf16 v[32:35], a[120:123], v[52:55], v[32:35]\n"
"v_mfma_f32_16x16x32_bf16 v[40:43], a[124:127], v[52:55], v[40:43]\n"
"v_mfma_f32_16x16x32_bf16 v[72:75], v[192:195], v[52:55], v[72:75]\n"
"s_waitcnt lgkmcnt(2)\n"
"v_mfma_f32_16x16x32_bf16 v[16:19], a[128:131], v[56:59], v[16:19]\n"
"s_waitcnt vmcnt(1)\n"
"s_barrier\n"
"v_mfma_f32_16x16x32_bf16 v[24:27], a[132:135], v[56:59], v[24:27]\n"
"ds_read_b128 v[48:51], v5 offset:24576\n"
"ds_read_b128 v[52:55], v5 offset:26624\n"
"v_mfma_f32_16x16x32_bf16 v[32:35], a[136:139], v[56:59], v[32:35]\n"
"s_mov_b64 exec, 1\n"
"ds_add_rtn_u32 v112, v110, v111\n"
"s_mov_b64 exec, -1\n"
"v_mfma_f32_16x16x32_bf16 v[40:43], a[140:143], v[56:59], v[40:43]\n"
"v_mfma_f32_16x16x32_bf16 v[72:75], v[188:191], v[56:59], v[72:75]\n"
"v_mfma_f32_16x16x32_bf16 v[16:19], a[144:147], v[60:63], v[16:19]\n"
"v_mfma_f32_16x16x32_bf16 v[24:27], a[148:151], v[60:63], v[24:27]\n"
"v_mfma_f32_16x16x32_bf16 v[32:35], a[152:155], v[60:63], v[32:35]\n"
"v_mfma_f32_16x16x32_bf16 v[40:43], a[156:159], v[60:63], v[40:43]\n"
"s_waitcnt lgkmcnt(0)\n"
"v_and_b32 v113, 3, v112\n"
"v_cmp_eq_u32 vcc, s37, v113\n"
"v_mfma_f32_16x16x32_bf16 v[72:75], v[184:187], v[60:63], v[72:75]\n"
"v_mov_b32 v114, s43\n"
"s_nop 0\n"
"v_mfma_f32_16x16x32_bf16 v[16:19], a[160:163], v[64:67], v[16:19]\n"
"ds_read_b128 v[56:59], v5 offset:28672\n"
"v_cndmask_b32 v113, -1, 0, vcc\n"
"v_mfma_f32_16x16x32_bf16 v[24:27], a[164:167], v[64:67], v[24:27]\n"
"ds_read_b128 v[60:63], v5 offset:30720\n"
"v_mfma_f32_16x16x32_bf16 v[32:35], a[168:171], v[64:67], v[32:35]\n"
"buffer_store_dword v114, v113, s[8:11], s31 offen sc1\n"
"v_mfma_f32_16x16x32_bf16 v[40:43], a[172:175], v[64:67], v[40:43]\n"
"v_mfma_f32_16x16x32_bf16 v[72:75], v[180:183], v[64:67], v[72:75]\n"
"v_mfma_f32_16x16x32_bf16 v[16:19], a[176:179], v[68:71], v[16:19]\n"
"v_mfma_f32_16x16x32_bf16 v[24:27], a[180:183], v[68:71], v[24:27]\n"
"v_mfma_f32_16x16x32_bf16 v[32:35], a[184:187], v[68:71], v[32:35]\n"
"v_mfma_f32_16x16x32_bf16 v[40:43], a[188:191], v[68:71], v[40:43]\n"
"v_mfma_f32_16x16x32_bf16 v[72:75], v[176:179], v[68:71], v[72:75]\n"
"v_mfma_f32_16x16x32_bf16 v[16:19], a[192:195], v[48:51], v[16:19]\n"
"s_waitcnt vmcnt(1)\n"
"s_barrier\n"
"v_mfma_f32_16x16x32_bf16 v[24:27], a[196:199], v[48:51], v[24:27]\n"
"ds_read_b128 v[64:67], v5 offset:32768\n"
"v_mfma_f32_16x16x32_bf16 v[32:35], a[200:203], v[48:51], v[32:35]\n"
"ds_read_b128 v[68:71], v5 offset:34816\n"
"v_mfma_f32_16x16x32_bf16 v[40:43], a[204:207], v[48:51], v[40:43]\n"
"v_mfma_f32_16x16x32_bf16 v[72:75], v[172:175], v[48:51], v[72:75]\n"
"v_mfma_f32_16x16x32_bf16 v[16:19], a[208:211], v[52:55], v[16:19]\n"
"v_mfma_f32_16x16x32_bf16 v[24:27], a[212:215], v[52:55], v[24:27]\n"
"v_mfma_f32_16x16x32_bf16 v[32:35], a[216:219], v[52:55], v[32:35]\n"
"v_mfma_f32_16x16x32_bf16 v[40:43], a[220:223], v[52:55], v[40:43]\n"
"v_mfma_f32_16x16x32_bf16 v[72:75], v[168:171], v[52:55], v[72:75]\n"
"s_waitcnt lgkmcnt(2)\n"
"v_mfma_f32_16x16x32_bf16 v[16:19], a[224:227], v[56:59], v[16:19]\n"
"ds_read_b128 v[48:51], v6 offset:0\n"
"v_mfma_f32_16x16x32_bf16 v[24:27], a[228:231], v[56:59], v[24:27]\n"
"ds_read_b128 v[52:55], v6 offset:2048\n"
"v_mfma_f32_16x16x32_bf16 v[32:35], a[232:235], v[56:59], v[32:35]\n"
"v_mfma_f32_16x16x32_bf16 v[40:43], a[236:239], v[56:59], v[40:43]\n"
"v_mfma_f32_16x16x32_bf16 v[72:75], v[164:167], v[56:59], v[72:75]\n"
"v_mfma_f32_16x16x32_bf16 v[16:19], a[240:243], v[60:63], v[16:19]\n"
"v_mfma_f32_16x16x32_bf16 v[24:27], a[244:247], v[60:63], v[24:27]\n"
"v_mfma_f32_16x16x32_bf16 v[32:35], a[248:251], v[60:63], v[32:35]\n"
"v_mfma_f32_16x16x32_bf16 v[40:43], a[252:255], v[60:63], v[40:43]\n"
"v_mfma_f32_16x16x32_bf16 v[72:75], v[160:163], v[60:63], v[72:75]\n"
"s_waitcnt lgkmcnt(2)\n"
"v_mfma_f32_16x16x32_bf16 v[16:19], v[252:255], v[64:67], v[16:19]\n"
"ds_read_b128 v[56:59], v6 offset:4096\n"
"v_mfma_f32_16x16x32_bf16 v[24:27], v[248:251], v[64:67], v[24:27]\n"
"ds_read_b128 v[60:63], v6 offset:6144\n"
"v_mfma_f32_16x16x32_bf16 v[32:35], v[244:247], v[64:67], v[32:35]\n"
"v_mfma_f32_16x16x32_bf16 v[40:43], v[240:243], v[64:67], v[40:43]\n"
"v_mfma_f32_16x16x32_bf16 v[72:75], v[156:159], v[64:67], v[72:75]\n"
"v_mfma_f32_16x16x32_bf16 v[16:19], v[236:239], v[68:71], v[16:19]\n"
"v_mfma_f32_16x16x32_bf16 v[24:27], v[232:235], v[68:71], v[24:27]\n"
"v_mfma_f32_16x16x32_bf16 v[32:35], v[228:231], v[68:71], v[32:35]\n"
"v_mfma_f32_16x16x32_bf16 v[40:43], v[224:227], v[68:71], v[40:43]\n"
"v_mfma_f32_16x16x32_bf16 v[72:75], v[152:155], v[68:71], v[72:75]\n"
"s_waitcnt lgkmcnt(2)\n"
"v_mfma_f32_16x16x32_bf16 v[20:23], a[0:3], v[48:51], v[76:79]\n"
"ds_read_b128 v[64:67], v6 offset:8192\n"
"v_mfma_f32_16x16x32_bf16 v[28:31], a[4:7], v[48:51], v[80:83]\n"
"ds_read_b128 v[68:71], v6 offset:10240\n"
"v_mfma_f32_16x16x32_bf16 v[20:23], a[16:19], v[52:55], v[20:23]\n"
"v_mul_f32 v100, v16, v16\n"
"v_mul_f32 v101, v17, v17\n"
"v_mfma_f32_16x16x32_bf16 v[28:31], a[20:23], v[52:55], v[28:31]\n"
"s_waitcnt lgkmcnt(2)\n"
"v_mul_f32 v102, v18, v18\n"
"v_mul_f32 v103, v19, v19\n"
"v_mfma_f32_16x16x32_bf16 v[20:23], a[32:35], v[56:59], v[20:23]\n"
"ds_read_b128 v[48:51], v6 offset:12288\n"
"v_fma_f32 v100, v100, s58, v96\n"
"v_fma_f32 v101, v101, s58, v96\n"
"v_mfma_f32_16x16x32_bf16 v[28:31], a[36:39], v[56:59], v[28:31]\n"
"ds_read_b128 v[52:55], v6 offset:14336\n"
"v_fma_f32 v102, v102, s58, v96\n"
"v_fma_f32 v103, v103, s58, v96\n"
"v_mfma_f32_16x16x32_bf16 v[20:23], a[48:51], v[60:63], v[20:23]\n"
"v_mul_f32 v100, v100, v16\n"
"v_mul_f32 v101, v101, v17\n"
"v_mfma_f32_16x16x32_bf16 v[28:31], a[52:55], v[60:63], v[28:31]\n"
"s_waitcnt lgkmcnt(2)\n"
"v_mul_f32 v102, v102, v18\n"
"v_mul_f32 v103, v103, v19\n"
"v_mfma_f32_16x16x32_bf16 v[20:23], a[64:67], v[64:67], v[20:23]\n"
"ds_read_b128 v[56:59], v6 offset:16384\n"
"v_exp_f32 v100, v100\n"
"v_mfma_f32_16x16x32_bf16 v[28:31], a[68:71], v[64:67], v[28:31]\n"
"ds_read_b128 v[60:63], v6 offset:18432\n"
"v_exp_f32 v101, v101\n"
"v_mfma_f32_16x16x32_bf16 v[20:23], a[80:83], v[68:71], v[20:23]\n"
"v_exp_f32 v102, v102\n"
"v_mfma_f32_16x16x32_bf16 v[28:31], a[84:87], v[68:71], v[28:31]\n"
"s_waitcnt lgkmcnt(2)\n"
"v_exp_f32 v103, v103\n"
"v_mfma_f32_16x16x32_bf16 v[20:23], a[96:99], v[48:51], v[20:23]\n"
"ds_read_b128 v[64:67], v6 offset:20480\n"
"v_add_f32 v100, 1.0, v100\n"
"v_add_f32 v101, 1.0, v101\n"
"v_mfma_f32_16x16x32_bf16 v[28:31], a[100:103], v[48:51], v[28:31]\n"
"ds_read_b128 v[68:71], v6 offset:22528\n"
"v_add_f32 v102, 1.0, v102\n"
"v_add_f32 v103, 1.0, v103\n"
"v_mfma_f32_16x16x32_bf16 v[20:23], a[112:115], v[52:55], v[20:23]\n"
"v_rcp_f32 v100, v100\n"
"v_mfma_f32_16x16x32_bf16 v[28:31], a[116:119], v[52:55], v[28:31]\n"
"s_waitcnt lgkmcnt(2)\n"
"v_rcp_f32 v101, v101\n"
"v_mfma_f32_16x16x32_bf16 v[20:23], a[128:131], v[56:59], v[20:23]\n"
"ds_read_b128 v[48:51], v6 offset:24576\n"
"ds_read_b128 v[52:55], v6 offset:26624\n"
"v_mfma_f32_16x16x32_bf16 v[28:31], a[132:135], v[56:59], v[28:31]\n"
"buffer_load_dword v3, v1, s[8:11], 0 offen sc1\n"
"v_mfma_f32_16x16x32_bf16 v[20:23], a[144:147], v[60:63], v[20:23]\n"
"s_add_u32 s42, s28, 1\n"
"s_add_u32 s39, s39, s41\n"
"s_and_b32 s38, s42, 15\n"
"v_mfma_f32_16x16x32_bf16 v[28:31], a[148:151], v[60:63], v[28:31]\n"
"s_waitcnt lgkmcnt(2)\n"
"s_lshl_b32 s38, s38, 22\n"
"s_add_u32 s38, s38, 33554432\n"
"v_mfma_f32_16x16x32_bf16 v[20:23], a[160:163], v[64:67], v[20:23]\n"
"ds_read_b128 v[56:59], v6 offset:28672\n"
"s_add_u32 s38, s38, s17\n"
"s_add_u32 s38, s38, 3342336\n"
"v_mfma_f32_16x16x32_bf16 v[28:31], a[164:167], v[64:67], v[28:31]\n"
"ds_read_b128 v[60:63], v6 offset:30720\n"
"v_rcp_f32 v102, v102\n"
"v_mfma_f32_16x16x32_bf16 v[20:23], a[176:179], v[68:71], v[20:23]\n"
"v_rcp_f32 v103, v103\n"
"v_mfma_f32_16x16x32_bf16 v[28:31], a[180:183], v[68:71], v[28:31]\n"
"s_waitcnt lgkmcnt(2)\n"
"v_mul_f32 v100, v100, v16\n"
"v_mul_f32 v101, v101, v17\n"
"v_mfma_f32_16x16x32_bf16 v[20:23], a[192:195], v[48:51], v[20:23]\n"
"ds_read_b128 v[64:67], v6 offset:32768\n"
"v_mul_f32 v102, v102, v18\n"
"v_mul_f32 v103, v103, v19\n"
"v_mfma_f32_16x16x32_bf16 v[28:31], a[196:199], v[48:51], v[28:31]\n"
"ds_read_b128 v[68:71], v6 offset:34816\n"
"v_cvt_pk_bf16_f32 v16, v100, v101\n"
"v_cvt_pk_bf16_f32 v17, v102, v103\n"
"v_mfma_f32_16x16x32_bf16 v[20:23], a[208:211], v[52:55], v[20:23]\n"
"v_mul_f32 v104, v24, v24\n"
"v_mul_f32 v105, v25, v25\n"
"v_mfma_f32_16x16x32_bf16 v[28:31], a[212:215], v[52:55], v[28:31]\n"
"s_waitcnt lgkmcnt(2)\n"
"v_mul_f32 v106, v26, v26\n"
"v_mul_f32 v107, v27, v27\n"
"v_mfma_f32_16x16x32_bf16 v[20:23], a[224:227], v[56:59], v[20:23]\n"
"ds_read_b128 v[48:51], v6 offset:0\n"
"v_fma_f32 v104, v104, s58, v96\n"
"v_fma_f32 v105, v105, s58, v96\n"
"v_mfma_f32_16x16x32_bf16 v[28:31], a[228:231], v[56:59], v[28:31]\n"
"ds_read_b128 v[52:55], v6 offset:2048\n"
"v_fma_f32 v106, v106, s58, v96\n"
"v_fma_f32 v107, v107, s58, v96\n"
"v_mfma_f32_16x16x32_bf16 v[20:23], a[240:243], v[60:63], v[20:23]\n"
"v_mul_f32 v104, v104, v24\n"
"v_mul_f32 v105, v105, v25\n"
"v_mfma_f32_16x16x32_bf16 v[28:31], a[244:247], v[60:63], v[28:31]\n"
"s_waitcnt lgkmcnt(2)\n"
"v_mul_f32 v106, v106, v26\n"
"v_mul_f32 v107, v107, v27\n"
"v_mfma_f32_16x16x32_bf16 v[20:23], v[252:255], v[64:67], v[20:23]\n"
"ds_read_b128 v[56:59], v6 offset:4096\n"
"v_exp_f32 v104, v104\n"
"v_mfma_f32_16x16x32_bf16 v[28:31], v[248:251], v[64:67], v[28:31]\n"
"ds_read_b128 v[60:63], v6 offset:6144\n"
"v_exp_f32 v105, v105\n"
"v_mfma_f32_16x16x32_bf16 v[20:23], v[236:239], v[68:71], v[20:23]\n"
"v_exp_f32 v106, v106\n"
"v_mfma_f32_16x16x32_bf16 v[28:31], v[232:235], v[68:71], v[28:31]\n"
"s_waitcnt lgkmcnt(2)\n"
"v_exp_f32 v107, v107\n"
"v_mfma_f32_16x16x32_bf16 v[36:39], a[8:11], v[48:51], v[84:87]\n"
"ds_read_b128 v[64:67], v6 offset:8192\n"
"v_add_f32 v104, 1.0, v104\n"
"v_add_f32 v105, 1.0, v105\n"
"v_mfma_f32_16x16x32_bf16 v[44:47], a[12:15], v[48:51], v[88:91]\n"
"ds_read_b128 v[68:71], v6 offset:10240\n"
"v_add_f32 v106, 1.0, v106\n"
"v_add_f32 v107, 1.0, v107\n"
"v_mfma_f32_16x16x32_bf16 v[36:39], a[24:27], v[52:55], v[36:39]\n"
"v_rcp_f32 v104, v104\n"
"v_mfma_f32_16x16x32_bf16 v[44:47], a[28:31], v[52:55], v[44:47]\n"
"s_waitcnt lgkmcnt(2)\n"
"v_rcp_f32 v105, v105\n"
"v_mfma_f32_16x16x32_bf16 v[36:39], a[40:43], v[56:59], v[36:39]\n"
"ds_read_b128 v[48:51], v6 offset:12288\n"
"v_rcp_f32 v106, v106\n"
"v_mfma_f32_16x16x32_bf16 v[44:47], a[44:47], v[56:59], v[44:47]\n"
"ds_read_b128 v[52:55], v6 offset:14336\n"
"v_rcp_f32 v107, v107\n"
"v_mfma_f32_16x16x32_bf16 v[36:39], a[56:59], v[60:63], v[36:39]\n"
"v_mul_f32 v104, v104, v24\n"
"v_mul_f32 v105, v105, v25\n"
"v_mfma_f32_16x16x32_bf16 v[44:47], a[60:63], v[60:63], v[44:47]\n"
"s_waitcnt lgkmcnt(2)\n"
"v_mul_f32 v106, v106, v26\n"
"v_mul_f32 v107, v107, v27\n"
"v_mfma_f32_16x16x32_bf16 v[36:39], a[72:75], v[64:67], v[36:39]\n"
"ds_read_b128 v[56:59], v6 offset:16384\n"
"ds_read_b128 v[60:63], v6 offset:18432\n"
"v_cvt_pk_bf16_f32 v18, v104, v105\n"
"v_mfma_f32_16x16x32_bf16 v[44:47], a[76:79], v[64:67], v[44:47]\n"
"s_waitcnt vmcnt(0)\n"
"v_cmp_gt_i32 s[76:77], s42, v3\n"
"v_mfma_f32_16x16x32_bf16 v[36:39], a[88:91], v[68:71], v[36:39]\n"
"v_cvt_pk_bf16_f32 v19, v106, v107\n"
"v_mfma_f32_16x16x32_bf16 v[44:47], a[92:95], v[68:71], v[44:47]\n"
"s_waitcnt lgkmcnt(2)\n"
"s_lshr_b32 s27, s42, 6\n"
"s_or_b32 s76, s76, s27\n"
"v_mfma_f32_16x16x32_bf16 v[36:39], a[104:107], v[48:51], v[36:39]\n"
"ds_read_b128 v[64:67], v6 offset:20480\n"
"ds_read_b128 v[68:71], v6 offset:22528\n"
"v_mfma_f32_16x16x32_bf16 v[44:47], a[108:111], v[48:51], v[44:47]\n"
"s_cmp_eq_u64 s[76:77], 0\n"
"s_cselect_b32 s36, 1, 0\n"
"s_cselect_b32 s34, s10, 0\n"
"v_mfma_f32_16x16x32_bf16 v[36:39], a[120:123], v[52:55], v[36:39]\n"
"buffer_store_dwordx4 v[16:19], v7, s[8:11], s53 offen sc1\n"
"v_mfma_f32_16x16x32_bf16 v[44:47], a[124:127], v[52:55], v[44:47]\n"
"s_waitcnt lgkmcnt(2)\n"
"s_add_u32 m0, s39, 0\n"
"s_add_u32 s26, s38, 0\n"
"v_mfma_f32_16x16x32_bf16 v[36:39], a[136:139], v[56:59], v[36:39]\n"
"ds_read_b128 v[48:51], v6 offset:24576\n"
"ds_read_b128 v[52:55], v6 offset:26624\n"
"v_mul_f32 v100, v72, v72\n"
"v_mfma_f32_16x16x32_bf16 v[44:47], a[140:143], v[56:59], v[44:47]\n"
"buffer_load_dwordx4 v4, s[32:35], s26 offen sc1 lds\n"
"v_mfma_f32_16x16x32_bf16 v[36:39], a[152:155], v[60:63], v[36:39]\n"
"v_mul_f32 v101, v73, v73\n"
"v_mul_f32 v102, v74, v74\n"
"v_mfma_f32_16x16x32_bf16 v[44:47], a[156:159], v[60:63], v[44:47]\n"
"s_waitcnt lgkmcnt(2)\n"
"s_add_u32 m0, s39, 4096\n"
"s_add_u32 s26, s38, 4096\n"
"v_mfma_f32_16x16x32_bf16 v[36:39], a[168:171], v[64:67], v[36:39]\n"
"ds_read_b128 v[56:59], v6 offset:28672\n"
"ds_read_b128 v[60:63], v6 offset:30720\n"
"v_mul_f32 v103, v75, v75\n"
"v_mfma_f32_16x16x32_bf16 v[44:47], a[172:175], v[64:67], v[44:47]\n"
"buffer_load_dwordx4 v4, s[32:35], s26 offen sc1 lds\n"
"v_mfma_f32_16x16x32_bf16 v[36:39], a[184:187], v[68:71], v[36:39]\n"
"v_fma_f32 v100, v100, s58, v96\n"
"v_fma_f32 v101, v101, s58, v96\n"
"v_mfma_f32_16x16x32_bf16 v[44:47], a[188:191], v[68:71], v[44:47]\n"
"s_waitcnt lgkmcnt(2)\n"
"s_add_u32 m0, s39, 8192\n"
"s_add_u32 s26, s38, 16384\n"
"v_mfma_f32_16x16x32_bf16 v[36:39], a[200:203], v[48:51], v[36:39]\n"
"ds_read_b128 v[64:67], v6 offset:32768\n"
"ds_read_b128 v[68:71], v6 offset:34816\n"
"v_fma_f32 v102, v102, s58, v96\n"
"v_mfma_f32_16x16x32_bf16 v[44:47], a[204:207], v[48:51], v[44:47]\n"
"buffer_load_dwordx4 v4, s[32:35], s26 offen sc1 lds\n"
"v_mfma_f32_16x16x32_bf16 v[36:39], a[216:219], v[52:55], v[36:39]\n"
"v_fma_f32 v103, v103, s58, v96\n"
"v_mul_f32 v100, v100, v72\n"
"v_mfma_f32_16x16x32_bf16 v[44:47], a[220:223], v[52:55], v[44:47]\n"
"s_waitcnt lgkmcnt(2)\n"
"s_add_u32 m0, s39, 12288\n"
"s_add_u32 s26, s38, 20480\n"
"v_mfma_f32_16x16x32_bf16 v[36:39], a[232:235], v[56:59], v[36:39]\n"
"buffer_load_dwordx4 v4, s[32:35], s26 offen sc1 lds\n"
"v_mfma_f32_16x16x32_bf16 v[44:47], a[236:239], v[56:59], v[44:47]\n"
"v_mul_f32 v101, v101, v73\n"
"v_mul_f32 v102, v102, v74\n"
"v_mfma_f32_16x16x32_bf16 v[36:39], a[248:251], v[60:63], v[36:39]\n"
"s_add_u32 m0, s39, 16384\n"
"s_add_u32 s26, s38, 32768\n"
"v_mul_f32 v103, v103, v75\n"
"v_mfma_f32_16x16x32_bf16 v[44:47], a[252:255], v[60:63], v[44:47]\n"
"s_waitcnt lgkmcnt(0)\n"
"v_exp_f32 v100, v100\n"
"v_mfma_f32_16x16x32_bf16 v[36:39], v[244:247], v[64:67], v[36:39]\n"
"buffer_load_dwordx4 v4, s[32:35], s26 offen sc1 lds\n"
"v_mfma_f32_16x16x32_bf16 v[44:47], v[240:243], v[64:67], v[44:47]\n"
"v_exp_f32 v101, v101\n"
"v_mfma_f32_16x16x32_bf16 v[36:39], v[228:231], v[68:71], v[36:39]\n"
"s_add_u32 m0, s39, 20480\n"
"s_add_u32 s26, s38, 36864\n"
"v_mfma_f32_16x16x32_bf16 v[44:47], v[224:227], v[68:71], v[44:47]\n"
"buffer_load_dwordx4 v4, s[32:35], s26 offen sc1 lds\n"
"v_add_u32 v5, s41, v5\n"
"v_add_u32 v6, s41, v6\n"
"s_sub_u32 s41, 0, s41\n"
"s_mov_b32 s37, 3\n"
"v_mov_b32 v111, 1\n"
"s_mov_b32 s43, s28\n"
"s_mov_b32 s28, s42\n"
"v_exp_f32 v102, v102\n"
"v_exp_f32 v103, v103\n"
"v_add_f32 v100, 1.0, v100\n"
"v_add_f32 v101, 1.0, v101\n"
"v_add_f32 v102, 1.0, v102\n"
"v_add_f32 v103, 1.0, v103\n"
"v_rcp_f32 v100, v100\n"
"v_rcp_f32 v101, v101\n"
"v_rcp_f32 v102, v102\n"
"v_rcp_f32 v103, v103\n"
"v_mul_f32 v100, v100, v72\n"
"v_mul_f32 v101, v101, v73\n"
"v_mul_f32 v102, v102, v74\n"
"v_mul_f32 v103, v103, v75\n"
"v_cvt_pk_bf16_f32 v72, v100, v101\n"
"v_cvt_pk_bf16_f32 v73, v102, v103\n"
"buffer_store_dwordx2 v[72:73], v9, s[8:11], s55 offen sc1\n"
"v_pk_mul_f32 v[104:105], v[32:33], v[32:33]\n"
"v_pk_mul_f32 v[106:107], v[34:35], v[34:35]\n"
"v_pk_fma_f32 v[104:105], v[104:105], s[58:59], v[96:97]\n"
"v_pk_fma_f32 v[106:107], v[106:107], s[58:59], v[96:97]\n"
"v_pk_mul_f32 v[104:105], v[104:105], v[32:33]\n"
"v_pk_mul_f32 v[106:107], v[106:107], v[34:35]\n"
"v_exp_f32 v104, v104\n"
"v_exp_f32 v105, v105\n"
"v_exp_f32 v106, v106\n"
"v_exp_f32 v107, v107\n"
"v_pk_add_f32 v[104:105], v[104:105], 1.0 op_sel_hi:[1,0]\n"
"v_pk_add_f32 v[106:107], v[106:107], 1.0 op_sel_hi:[1,0]\n"
"v_rcp_f32 v104, v104\n"
"v_rcp_f32 v105, v105\n"
"v_rcp_f32 v106, v106\n"
"v_rcp_f32 v107, v107\n"
"v_pk_mul_f32 v[104:105], v[104:105], v[32:33]\n"
"v_pk_mul_f32 v[106:107], v[106:107], v[34:35]\n"
"v_cvt_pk_bf16_f32 v32, v104, v105\n"
"v_cvt_pk_bf16_f32 v33, v106, v107\n"
"v_pk_mul_f32 v[100:101], v[40:41], v[40:41]\n"
"v_pk_mul_f32 v[102:103], v[42:43], v[42:43]\n"
"v_pk_fma_f32 v[100:101], v[100:101], s[58:59], v[96:97]\n"
"v_pk_fma_f32 v[102:103], v[102:103], s[58:59], v[96:97]\n"
"v_pk_mul_f32 v[100:101], v[100:101], v[40:41]\n"
"v_pk_mul_f32 v[102:103], v[102:103], v[42:43]\n"
"v_exp_f32 v100, v100\n"
"v_exp_f32 v101, v101\n"
"v_exp_f32 v102, v102\n"
"v_exp_f32 v103, v103\n"
"v_pk_add_f32 v[100:101], v[100:101], 1.0 op_sel_hi:[1,0]\n"
"v_pk_add_f32 v[102:103], v[102:103], 1.0 op_sel_hi:[1,0]\n"
"v_rcp_f32 v100, v100\n"
"v_rcp_f32 v101, v101\n"
"v_rcp_f32 v102, v102\n"
"v_rcp_f32 v103, v103\n"
"v_pk_mul_f32 v[100:101], v[100:101], v[40:41]\n"
"v_pk_mul_f32 v[102:103], v[102:103], v[42:43]\n"
"v_cvt_pk_bf16_f32 v34, v100, v101\n"
"v_cvt_pk_bf16_f32 v35, v102, v103\n"
"buffer_store_dwordx4 v[32:35], v7, s[8:11], s54 offen sc1\n"
"v_pk_mul_f32 v[104:105], v[20:21], v[20:21]\n"
"v_pk_mul_f32 v[106:107], v[22:23], v[22:23]\n"
"v_pk_fma_f32 v[104:105], v[104:105], s[58:59], v[96:97]\n"
"v_pk_fma_f32 v[106:107], v[106:107], s[58:59], v[96:97]\n"
"v_pk_mul_f32 v[104:105], v[104:105], v[20:21]\n"
"v_pk_mul_f32 v[106:107], v[106:107], v[22:23]\n"
"v_exp_f32 v104, v104\n"
"v_exp_f32 v105, v105\n"
"v_exp_f32 v106, v106\n"
"v_exp_f32 v107, v107\n"
"v_pk_add_f32 v[104:105], v[104:105], 1.0 op_sel_hi:[1,0]\n"
"v_pk_add_f32 v[106:107], v[106:107], 1.0 op_sel_hi:[1,0]\n"
"v_rcp_f32 v104, v104\n"
"v_rcp_f32 v105, v105\n"
"v_rcp_f32 v106, v106\n"
"v_rcp_f32 v107, v107\n"
"v_pk_mul_f32 v[104:105], v[104:105], v[20:21]\n"
"v_pk_mul_f32 v[106:107], v[106:107], v[22:23]\n"
"v_cvt_pk_bf16_f32 v20, v104, v105\n"
"v_cvt_pk_bf16_f32 v21, v106, v107\n"
"v_pk_mul_f32 v[100:101], v[28:29], v[28:29]\n"
"v_pk_mul_f32 v[102:103], v[30:31], v[30:31]\n"
"v_pk_fma_f32 v[100:101], v[100:101], s[58:59], v[96:97]\n"
"v_pk_fma_f32 v[102:103], v[102:103], s[58:59], v[96:97]\n"
"v_pk_mul_f32 v[100:101], v[100:101], v[28:29]\n"
"v_pk_mul_f32 v[102:103], v[102:103], v[30:31]\n"
"v_exp_f32 v100, v100\n"
"v_exp_f32 v101, v101\n"
"v_exp_f32 v102, v102\n"
"v_exp_f32 v103, v103\n"
"v_pk_add_f32 v[100:101], v[100:101], 1.0 op_sel_hi:[1,0]\n"
"v_pk_add_f32 v[102:103], v[102:103], 1.0 op_sel_hi:[1,0]\n"
"v_rcp_f32 v100, v100\n"
"v_rcp_f32 v101, v101\n"
"v_rcp_f32 v102, v102\n"
"v_rcp_f32 v103, v103\n"
"v_pk_mul_f32 v[100:101], v[100:101], v[28:29]\n"
"v_pk_mul_f32 v[102:103], v[102:103], v[30:31]\n"
"v_cvt_pk_bf16_f32 v22, v100, v101\n"
"v_cvt_pk_bf16_f32 v23, v102, v103\n"
"buffer_store_dwordx4 v[20:23], v8, s[8:11], s53 offen sc1\n"
"v_pk_mul_f32 v[104:105], v[36:37], v[36:37]\n"
"v_pk_mul_f32 v[106:107], v[38:39], v[38:39]\n"
"v_pk_fma_f32 v[104:105], v[104:105], s[58:59], v[96:97]\n"
"v_pk_fma_f32 v[106:107], v[106:107], s[58:59], v[96:97]\n"
"v_pk_mul_f32 v[104:105], v[104:105], v[36:37]\n"
"v_pk_mul_f32 v[106:107], v[106:107], v[38:39]\n"
"v_exp_f32 v104, v104\n"
"v_exp_f32 v105, v105\n"
"v_exp_f32 v106, v106\n"
"v_exp_f32 v107, v107\n"
"v_pk_add_f32 v[104:105], v[104:105], 1.0 op_sel_hi:[1,0]\n"
"v_pk_add_f32 v[106:107], v[106:107], 1.0 op_sel_hi:[1,0]\n"
"v_rcp_f32 v104, v104\n"
"v_rcp_f32 v105, v105\n"
"v_rcp_f32 v106, v106\n"
"v_rcp_f32 v107, v107\n"
"v_pk_mul_f32 v[104:105], v[104:105], v[36:37]\n"
"v_pk_mul_f32 v[106:107], v[106:107], v[38:39]\n"
"v_cvt_pk_bf16_f32 v36, v104, v105\n"
"v_cvt_pk_bf16_f32 v37, v106, v107\n"
"s_cmp_eq_u32 s36, 1\n"
"s_cbranch_scc0 MK_F1S_np\n"
"s_waitcnt vmcnt(3)\n"
"s_barrier\n"
"ds_read_b128 v[48:51], v5 offset:0\n"
"ds_read_b128 v[52:55], v5 offset:2048\n"
"ds_read_b128 v[56:59], v5 offset:4096\n"
"ds_read_b128 v[60:63], v5 offset:6144\n"
"v_pk_mul_f32 v[100:101], v[44:45], v[44:45]\n"
"v_pk_mul_f32 v[102:103], v[46:47], v[46:47]\n"
"v_pk_fma_f32 v[100:101], v[100:101], s[58:59], v[96:97]\n"
"v_pk_fma_f32 v[102:103], v[102:103], s[58:59], v[96:97]\n"
"v_pk_mul_f32 v[100:101], v[100:101], v[44:45]\n"
"v_pk_mul_f32 v[102:103], v[102:103], v[46:47]\n"
"v_exp_f32 v100, v100\n"
"v_exp_f32 v101, v101\n"
"v_exp_f32 v102, v102\n"
"v_exp_f32 v103, v103\n"
"v_pk_add_f32 v[100:101], v[100:101], 1.0 op_sel_hi:[1,0]\n"
"v_pk_add_f32 v[102:103], v[102:103], 1.0 op_sel_hi:[1,0]\n"
"v_rcp_f32 v100, v100\n"
"v_rcp_f32 v101, v101\n"
"v_rcp_f32 v102, v102\n"
"v_rcp_f32 v103, v103\n"
"v_pk_mul_f32 v[100:101], v[100:101], v[44:45]\n"
"v_pk_mul_f32 v[102:103], v[102:103], v[46:47]\n"
"v_cvt_pk_bf16_f32 v38, v100, v101\n"
"v_cvt_pk_bf16_f32 v39, v102, v103\n"
"buffer_store_dwordx4 v[36:39], v8, s[8:11], s54 offen sc1\n"
"s_branch MK_F1S_body\n"
"MK_F1S_np:\n"
"v_pk_mul_f32 v[104:105], v[44:45], v[44:45]\n"
"v_pk_mul_f32 v[106:107], v[46:47], v[46:47]\n"
"v_pk_fma_f32 v[104:105], v[104:105], s[58:59], v[96:97]\n"
"v_pk_fma_f32 v[106:107], v[106:107], s[58:59], v[96:97]\n"
"v_pk_mul_f32 v[104:105], v[104:105], v[44:45]\n"
"v_pk_mul_f32 v[106:107], v[106:107], v[46:47]\n"
"v_exp_f32 v104, v104\n"
"v_exp_f32 v105, v105\n"
"v_exp_f32 v106, v106\n"
"v_exp_f32 v107, v107\n"
"v_pk_add_f32 v[104:105], v[104:105], 1.0 op_sel_hi:[1,0]\n"
"v_pk_add_f32 v[106:107], v[106:107], 1.0 op_sel_hi:[1,0]\n"
"v_rcp_f32 v104, v104\n"
"v_rcp_f32 v105, v105\n"
"v_rcp_f32 v106, v106\n"
"v_rcp_f32 v107, v107\n"
"v_pk_mul_f32 v[104:105], v[104:105], v[44:45]\n"
"v_pk_mul_f32 v[106:107], v[106:107], v[46:47]\n"
"v_cvt_pk_bf16_f32 v38, v104, v105\n"
"v_cvt_pk_bf16_f32 v39, v106, v107\n"
"buffer_store_dwordx4 v[36:39], v8, s[8:11], s54 offen sc1\n"
"s_cmp_lt_u32 s28, 64\n"
"s_cbranch_scc1 MK_F1S_wait\n"
"s_waitcnt vmcnt(0)\n"
"s_barrier\n"
"s_cmp_eq_u32 s15, 0\n"
"s_cbranch_scc0 MK_pf5\n"
"v_mov_b32 v3, 63\n"
"s_mov_b64 exec, 1\n"
"buffer_store_dword v3, off, s[8:11], s31 sc1\n"
"s_mov_b64 exec, -1\n"
"MK_pf5:\n"
"s_endpgm\n"
"MK_F2:\n"
"s_memrealtime s[18:19]\n"
"s_mul_i32 s24, s12, 500\n"
"s_add_u32 s24, s24, 500\n"
"s_waitcnt lgkmcnt(0)\n"
"s_add_u32 s24, s24, s18\n"
"MK_F2_dly:\n"
"s_memrealtime s[18:19]\n"
"s_waitcnt lgkmcnt(0)\n"
"s_sub_u32 s25, s24, s18\n"
"s_cmp_gt_i32 s25, 0\n"
"s_cbranch_scc0 MK_F2_dlx\n"
"s_sleep 4\n"
"s_branch MK_F2_dly\n"
"MK_F2_dlx:\n"
"s_cmp_eq_u32 s12, 0\n"
"s_cselect_b32 s24, 0, 0\n"
"s_add_u32 s16, s16, s24\n"
"s_add_u32 s17, s17, s24\n"
"s_load_dwordx2 s[44:45], s[6:7], 64\n"
"s_mov_b32 s46, 5308416\n"
"s_mov_b32 s47, 131072\n"
"s_waitcnt lgkmcnt(0)\n"
"s_and_b32 s45, s45, 65535\n"
"v_mbcnt_lo_u32_b32 v0, -1, 0\n"
"v_mbcnt_hi_u32_b32 v0, -1, v0\n"
"v_add_u32 v17, 0, v0\n"
"v_mul_u32_u24 v18, 2731, v17\n"
"v_lshrrev_b32 v18, 16, v18\n"
"v_bfe_u32 v16, v18, 2, 1\n"
"v_mul_u32_u24 v10, 36864, v16\n"
"v_bfe_u32 v16, v18, 1, 1\n"
"v_mul_u32_u24 v16, 18432, v16\n"
"v_add_u32 v10, v10, v16\n"
"v_bfe_u32 v16, v18, 0, 1\n"
"v_mul_u32_u24 v16, 4608, v16\n"
"v_add_u32 v10, v10, v16\n"
"v_mul_u32_u24 v18, 24, v18\n"
"v_sub_u32 v17, v17, v18\n"
"v_lshl_add_u32 v10, v17, 4, v10\n"
"v_add_u32 v17, 64, v0\n"
"v_mul_u32_u24 v18, 2731, v17\n"
"v_lshrrev_b32 v18, 16, v18\n"
"v_bfe_u32 v16, v18, 2, 1\n"
"v_mul_u32_u24 v11, 36864, v16\n"
"v_bfe_u32 v16, v18, 1, 1\n"
"v_mul_u32_u24 v16, 18432, v16\n"
"v_add_u32 v11, v11, v16\n"
"v_bfe_u32 v16, v18, 0, 1\n"
"v_mul_u32_u24 v16, 4608, v16\n"
"v_add_u32 v11, v11, v16\n"
"v_mul_u32_u24 v18, 24, v18\n"
"v_sub_u32 v17, v17, v18\n"
"v_lshl_add_u32 v11, v17, 4, v11\n"
"v_add_u32 v17, 128, v0\n"
"v_mul_u32_u24 v18, 2731, v17\n"
"v_lshrrev_b32 v18, 16, v18\n"
"v_bfe_u32 v16, v18, 2, 1\n"
"v_mul_u32_u24 v12, 36864, v16\n"
"v_bfe_u32 v16, v18, 1, 1\n"
"v_mul_u32_u24 v16, 18432, v16\n"
"v_add_u32 v12, v12, v16\n"
"v_bfe_u32 v16, v18, 0, 1\n"
"v_mul_u32_u24 v16, 4608, v16\n"
"v_add_u32 v12, v12, v16\n"
"v_mul_u32_u24 v18, 24, v18\n"
"v_sub_u32 v17, v17, v18\n"
"v_lshl_add_u32 v12, v17, 4, v12\n"
"s_lshr_b32 s24, s15, 1\n"
"s_mul_i32 s24, s24, 8\n"
"s_and_b32 s25, s15, 1\n"
"s_lshl_b32 s25, s25, 1\n"
"s_add_u32 s24, s24, s25\n"
"s_mul_i32 s25, s13, 1152\n"
"s_add_u32 s24, s24, s25\n"
"s_mul_i32 s24, s24, 1152\n"
"s_mul_i32 s25, s14, 384\n"
"s_add_u32 s48, s24, s25\n"
"s_add_u32 s49, s48, 1152\n"
"s_mul_i32 s50, s15, 6208\n"
"s_mul_i32 s26, s15, 96\n"
"v_bfe_u32 v16, v0, 4, 1\n"
"v_mul_u32_u24 v13, 12416, v16\n"
"v_bfe_u32 v16, v0, 2, 2\n"
"v_mul_u32_u24 v16, 3104, v16\n"
"v_add_u32 v13, v13, v16\n"
"v_bfe_u32 v16, v0, 5, 1\n"
"v_mul_u32_u24 v16, 768, v16\n"
"v_add_u32 v13, v13, v16\n"
"v_bfe_u32 v16, v0, 0, 2\n"
"v_lshl_add_u32 v13, v16, 3, v13\n"
"v_add_u32 v13, s26, v13\n"
"v_add_u32 v14, 49152, v13\n"
"v_add_u32 v15, 49152, v14\n"
"s_add_u32 s24, s48, 0\n"
"s_add_u32 m0, s50, 0\n"
"s_nop 0\n"
"buffer_load_dwordx4 v10, s[44:47], s24 offen lds\n"
"s_add_u32 m0, s50, 1024\n"
"s_nop 0\n"
"buffer_load_dwordx4 v11, s[44:47], s24 offen lds\n"
"s_add_u32 m0, s50, 2048\n"
"s_nop 0\n"
"buffer_load_dwordx4 v12, s[44:47], s24 offen lds\n"
"s_add_u32 s24, s49, 0\n"
"s_add_u32 m0, s50, 3104\n"
"s_nop 0\n"
"buffer_load_dwordx4 v10, s[44:47], s24 offen lds\n"
"s_add_u32 m0, s50, 4128\n"
"s_nop 0\n"
"buffer_load_dwordx4 v11, s[44:47], s24 offen lds\n"
"s_add_u32 m0, s50, 5152\n"
"s_nop 0\n"
"buffer_load_dwordx4 v12, s[44:47], s24 offen lds\n"
"s_add_u32 s24, s48, 73728\n"
"s_add_u32 m0, s50, 24832\n"
"s_nop 0\n"
"buffer_load_dwordx4 v10, s[44:47], s24 offen lds\n"
"s_add_u32 m0, s50, 25856\n"
"s_nop 0\n"
"buffer_load_dwordx4 v11, s[44:47], s24 offen lds\n"
"s_add_u32 m0, s50, 26880\n"
"s_nop 0\n"
"buffer_load_dwordx4 v12, s[44:47], s24 offen lds\n"
"s_add_u32 s24, s49, 73728\n"
"s_add_u32 m0, s50, 27936\n"
"s_nop 0\n"
"buffer_load_dwordx4 v10, s[44:47], s24 offen lds\n"
"s_add_u32 m0, s50, 28960\n"
"s_nop 0\n"
"buffer_load_dwordx4 v11, s[44:47], s24 offen lds\n"
"s_add_u32 m0, s50, 29984\n"
"s_nop 0\n"
"buffer_load_dwordx4 v12, s[44:47], s24 offen lds\n"
"s_add_u32 s24, s48, 147456\n"
"s_add_u32 m0, s50, 49664\n"
"s_nop 0\n"
"buffer_load_dwordx4 v10, s[44:47], s24 offen lds\n"
"s_add_u32 m0, s50, 50688\n"
"s_nop 0\n"
"buffer_load_dwordx4 v11, s[44:47], s24 offen lds\n"
"s_add_u32 m0, s50, 51712\n"
"s_nop 0\n"
"buffer_load_dwordx4 v12, s[44:47], s24 offen lds\n"
"s_add_u32 s24, s49, 147456\n"
"s_add_u32 m0, s50, 52768\n"
"s_nop 0\n"
"buffer_load_dwordx4 v10, s[44:47], s24 offen lds\n"
"s_add_u32 m0, s50, 53792\n"
"s_nop 0\n"
"buffer_load_dwordx4 v11, s[44:47], s24 offen lds\n"
"s_add_u32 m0, s50, 54816\n"
"s_nop 0\n"
"buffer_load_dwordx4 v12, s[44:47], s24 offen lds\n"
"s_add_u32 s24, s48, 221184\n"
"s_add_u32 m0, s50, 74496\n"
"s_nop 0\n"
"buffer_load_dwordx4 v10, s[44:47], s24 offen lds\n"
"s_add_u32 m0, s50, 75520\n"
"s_nop 0\n"
"buffer_load_dwordx4 v11, s[44:47], s24 offen lds\n"
"s_add_u32 m0, s50, 76544\n"
"s_nop 0\n"
"buffer_load_dwordx4 v12, s[44:47], s24 offen lds\n"
"s_add_u32 s24, s49, 221184\n"
"s_add_u32 m0, s50, 77600\n"
"s_nop 0\n"
"buffer_load_dwordx4 v10, s[44:47], s24 offen lds\n"
"s_add_u32 m0, s50, 78624\n"
"s_nop 0\n"
"buffer_load_dwordx4 v11, s[44:47], s24 offen lds\n"
"s_add_u32 m0, s50, 79648\n"
"s_nop 0\n"
"buffer_load_dwordx4 v12, s[44:47], s24 offen lds\n"
"s_add_u32 s24, s48, 294912\n"
"s_add_u32 m0, s50, 99328\n"
"s_nop 0\n"
"buffer_load_dwordx4 v10, s[44:47], s24 offen lds\n"
"s_add_u32 m0, s50, 100352\n"
"s_nop 0\n"
"buffer_load_dwordx4 v11, s[44:47], s24 offen lds\n"
"s_add_u32 m0, s50, 101376\n"
"s_nop 0\n"
"buffer_load_dwordx4 v12, s[44:47], s24 offen lds\n"
"s_add_u32 s24, s49, 294912\n"
"s_add_u32 m0, s50, 102432\n"
"s_nop 0\n"
"buffer_load_dwordx4 v10, s[44:47], s24 offen lds\n"
"s_add_u32 m0, s50, 103456\n"
"s_nop 0\n"
"buffer_load_dwordx4 v11, s[44:47], s24 offen lds\n"
"s_add_u32 m0, s50, 104480\n"
"s_nop 0\n"
"buffer_load_dwordx4 v12, s[44:47], s24 offen lds\n"
"s_waitcnt vmcnt(24) lgkmcnt(0)\n"
"s_barrier\n"
"s_add_u32 s24, s48, 368640\n"
"s_add_u32 m0, s50, 124160\n"
"s_nop 0\n"
"buffer_load_dwordx4 v10, s[44:47], s24 offen lds\n"
"s_add_u32 m0, s50, 125184\n"
"s_nop 0\n"
"buffer_load_dwordx4 v11, s[44:47], s24 offen lds\n"
"s_add_u32 m0, s50, 126208\n"
"s_nop 0\n"
"buffer_load_dwordx4 v12, s[44:47], s24 offen lds\n"
"s_add_u32 s24, s49, 368640\n"
"s_add_u32 m0, s50, 127264\n"
"s_nop 0\n"
"buffer_load_dwordx4 v10, s[44:47], s24 offen lds\n"
"s_add_u32 m0, s50, 128288\n"
"s_nop 0\n"
"buffer_load_dwordx4 v11, s[44:47], s24 offen lds\n"
"s_add_u32 m0, s50, 129312\n"
"s_nop 0\n"
"buffer_load_dwordx4 v12, s[44:47], s24 offen lds\n"
"ds_read_b64_tr_b16 a[0:1], v13 offset:0\n"
"ds_read_b64_tr_b16 a[2:3], v13 offset:384\n"
"ds_read_b64_tr_b16 a[4:5], v13 offset:32\n"
"ds_read_b64_tr_b16 a[6:7], v13 offset:416\n"
"ds_read_b64_tr_b16 a[8:9], v13 offset:64\n"
"ds_read_b64_tr_b16 a[10:11], v13 offset:448\n"
"ds_read_b64_tr_b16 a[12:13], v13 offset:1536\n"
"ds_read_b64_tr_b16 a[14:15], v13 offset:1920\n"
"ds_read_b64_tr_b16 a[16:17], v13 offset:1568\n"
"ds_read_b64_tr_b16 a[18:19], v13 offset:1952\n"
"ds_read_b64_tr_b16 a[20:21], v13 offset:1600\n"
"ds_read_b64_tr_b16 a[22:23], v13 offset:1984\n"
"s_waitcnt vmcnt(24) lgkmcnt(0)\n"
"s_barrier\n"
"s_add_u32 s24, s48, 442368\n"
"s_add_u32 m0, s50, 0\n"
"s_nop 0\n"
"buffer_load_dwordx4 v10, s[44:47], s24 offen lds\n"
"s_add_u32 m0, s50, 1024\n"
"s_nop 0\n"
"buffer_load_dwordx4 v11, s[44:47], s24 offen lds\n"
"s_add_u32 m0, s50, 2048\n"
"s_nop 0\n"
"buffer_load_dwordx4 v12, s[44:47], s24 offen lds\n"
"s_add_u32 s24, s49, 442368\n"
"s_add_u32 m0, s50, 3104\n"
"s_nop 0\n"
"buffer_load_dwordx4 v10, s[44:47], s24 offen lds\n"
"s_add_u32 m0, s50, 4128\n"
"s_nop 0\n"
"buffer_load_dwordx4 v11, s[44:47], s24 offen lds\n"
"s_add_u32 m0, s50, 5152\n"
"s_nop 0\n"
"buffer_load_dwordx4 v12, s[44:47], s24 offen lds\n"
"ds_read_b64_tr_b16 a[24:25], v13 offset:24832\n"
"ds_read_b64_tr_b16 a[26:27], v13 offset:25216\n"
"ds_read_b64_tr_b16 a[28:29], v13 offset:24864\n"
"ds_read_b64_tr_b16 a[30:31], v13 offset:25248\n"
"ds_read_b64_tr_b16 a[32:33], v13 offset:24896\n"
"ds_read_b64_tr_b16 a[34:35], v13 offset:25280\n"
"ds_read_b64_tr_b16 a[36:37], v13 offset:26368\n"
"ds_read_b64_tr_b16 a[38:39], v13 offset:26752\n"
"ds_read_b64_tr_b16 a[40:41], v13 offset:26400\n"
"ds_read_b64_tr_b16 a[42:43], v13 offset:26784\n"
"ds_read_b64_tr_b16 a[44:45], v13 offset:26432\n"
"ds_read_b64_tr_b16 a[46:47], v13 offset:26816\n"
"s_waitcnt vmcnt(24) lgkmcnt(0)\n"
"s_barrier\n"
"s_add_u32 s24, s48, 516096\n"
"s_add_u32 m0, s50, 24832\n"
"s_nop 0\n"
"buffer_load_dwordx4 v10, s[44:47], s24 offen lds\n"
"s_add_u32 m0, s50, 25856\n"
"s_nop 0\n"
"buffer_load_dwordx4 v11, s[44:47], s24 offen lds\n"
"s_add_u32 m0, s50, 26880\n"
"s_nop 0\n"
"buffer_load_dwordx4 v12, s[44:47], s24 offen lds\n"
"s_add_u32 s24, s49, 516096\n"
"s_add_u32 m0, s50, 27936\n"
"s_nop 0\n"
"buffer_load_dwordx4 v10, s[44:47], s24 offen lds\n"
"s_add_u32 m0, s50, 28960\n"
"s_nop 0\n"
"buffer_load_dwordx4 v11, s[44:47], s24 offen lds\n"
"s_add_u32 m0, s50, 29984\n"
"s_nop 0\n"
"buffer_load_dwordx4 v12, s[44:47], s24 offen lds\n"
"ds_read_b64_tr_b16 a[48:49], v14 offset:512\n"
"ds_read_b64_tr_b16 a[50:51], v14 offset:896\n"
"ds_read_b64_tr_b16 a[52:53], v14 offset:544\n"
"ds_read_b64_tr_b16 a[54:55], v14 offset:928\n"
"ds_read_b64_tr_b16 a[56:57], v14 offset:576\n"
"ds_read_b64_tr_b16 a[58:59], v14 offset:960\n"
"ds_read_b64_tr_b16 a[60:61], v14 offset:2048\n"
"ds_read_b64_tr_b16 a[62:63], v14 offset:2432\n"
"ds_read_b64_tr_b16 a[64:65], v14 offset:2080\n"
"ds_read_b64_tr_b16 a[66:67], v14 offset:2464\n"
"ds_read_b64_tr_b16 a[68:69], v14 offset:2112\n"
"ds_read_b64_tr_b16 a[70:71], v14 offset:2496\n"
"s_waitcnt vmcnt(24) lgkmcnt(0)\n"
"s_barrier\n"
"s_add_u32 s24, s48, 589824\n"
"s_add_u32 m0, s50, 49664\n"
"s_nop 0\n"
"buffer_load_dwordx4 v10, s[44:47], s24 offen lds\n"
"s_add_u32 m0, s50, 50688\n"
"s_nop 0\n"
"buffer_load_dwordx4 v11, s[44:47], s24 offen lds\n"
"s_add_u32 m0, s50, 51712\n"
"s_nop 0\n"
"buffer_load_dwordx4 v12, s[44:47], s24 offen lds\n"
"s_add_u32 s24, s49, 589824\n"
"s_add_u32 m0, s50, 52768\n"
"s_nop 0\n"
"buffer_load_dwordx4 v10, s[44:47], s24 offen lds\n"
"s_add_u32 m0, s50, 53792\n"
"s_nop 0\n"
"buffer_load_dwordx4 v11, s[44:47], s24 offen lds\n"
"s_add_u32 m0, s50, 54816\n"
"s_nop 0\n"
"buffer_load_dwordx4 v12, s[44:47], s24 offen lds\n"
"ds_read_b64_tr_b16 a[72:73], v14 offset:25344\n"
"ds_read_b64_tr_b16 a[74:75], v14 offset:25728\n"
"ds_read_b64_tr_b16 a[76:77], v14 offset:25376\n"
"ds_read_b64_tr_b16 a[78:79], v14 offset:25760\n"
"ds_read_b64_tr_b16 a[80:81], v14 offset:25408\n"
"ds_read_b64_tr_b16 a[82:83], v14 offset:25792\n"
"ds_read_b64_tr_b16 a[84:85], v14 offset:26880\n"
"ds_read_b64_tr_b16 a[86:87], v14 offset:27264\n"
"ds_read_b64_tr_b16 a[88:89], v14 offset:26912\n"
"ds_read_b64_tr_b16 a[90:91], v14 offset:27296\n"
"ds_read_b64_tr_b16 a[92:93], v14 offset:26944\n"
"ds_read_b64_tr_b16 a[94:95], v14 offset:27328\n"
"s_waitcnt vmcnt(24) lgkmcnt(0)\n"
"s_barrier\n"
"s_add_u32 s24, s48, 663552\n"
"s_add_u32 m0, s50, 74496\n"
"s_nop 0\n"
"buffer_load_dwordx4 v10, s[44:47], s24 offen lds\n"
"s_add_u32 m0, s50, 75520\n"
"s_nop 0\n"
"buffer_load_dwordx4 v11, s[44:47], s24 offen lds\n"
"s_add_u32 m0, s50, 76544\n"
"s_nop 0\n"
"buffer_load_dwordx4 v12, s[44:47], s24 offen lds\n"
"s_add_u32 s24, s49, 663552\n"
"s_add_u32 m0, s50, 77600\n"
"s_nop 0\n"
"buffer_load_dwordx4 v10, s[44:47], s24 offen lds\n"
"s_add_u32 m0, s50, 78624\n"
"s_nop 0\n"
"buffer_load_dwordx4 v11, s[44:47], s24 offen lds\n"
"s_add_u32 m0, s50, 79648\n"
"s_nop 0\n"
"buffer_load_dwordx4 v12, s[44:47], s24 offen lds\n"
"ds_read_b64_tr_b16 a[96:97], v15 offset:1024\n"
"ds_read_b64_tr_b16 a[98:99], v15 offset:1408\n"
"ds_read_b64_tr_b16 a[100:101], v15 offset:1056\n"
"ds_read_b64_tr_b16 a[102:103], v15 offset:1440\n"
"ds_read_b64_tr_b16 a[104:105], v15 offset:1088\n"
"ds_read_b64_tr_b16 a[106:107], v15 offset:1472\n"
"ds_read_b64_tr_b16 a[108:109], v15 offset:2560\n"
"ds_read_b64_tr_b16 a[110:111], v15 offset:2944\n"
"ds_read_b64_tr_b16 a[112:113], v15 offset:2592\n"
"ds_read_b64_tr_b16 a[114:115], v15 offset:2976\n"
"ds_read_b64_tr_b16 a[116:117], v15 offset:2624\n"
"ds_read_b64_tr_b16 a[118:119], v15 offset:3008\n"
"s_waitcnt vmcnt(24) lgkmcnt(0)\n"
"s_barrier\n"
"s_add_u32 s24, s48, 737280\n"
"s_add_u32 m0, s50, 99328\n"
"s_nop 0\n"
"buffer_load_dwordx4 v10, s[44:47], s24 offen lds\n"
"s_add_u32 m0, s50, 100352\n"
"s_nop 0\n"
"buffer_load_dwordx4 v11, s[44:47], s24 offen lds\n"
"s_add_u32 m0, s50, 101376\n"
"s_nop 0\n"
"buffer_load_dwordx4 v12, s[44:47], s24 offen lds\n"
"s_add_u32 s24, s49, 737280\n"
"s_add_u32 m0, s50, 102432\n"
"s_nop 0\n"
"buffer_load_dwordx4 v10, s[44:47], s24 offen lds\n"
"s_add_u32 m0, s50, 103456\n"
"s_nop 0\n"
"buffer_load_dwordx4 v11, s[44:47], s24 offen lds\n"
"s_add_u32 m0, s50, 104480\n"
"s_nop 0\n"
"buffer_load_dwordx4 v12, s[44:47], s24 offen lds\n"
"ds_read_b64_tr_b16 a[120:121], v15 offset:25856\n"
"ds_read_b64_tr_b16 a[122:123], v15 offset:26240\n"
"ds_read_b64_tr_b16 a[124:125], v15 offset:25888\n"
"ds_read_b64_tr_b16 a[126:127], v15 offset:26272\n"
"ds_read_b64_tr_b16 a[128:129], v15 offset:25920\n"
"ds_read_b64_tr_b16 a[130:131], v15 offset:26304\n"
"ds_read_b64_tr_b16 a[132:133], v15 offset:27392\n"
"ds_read_b64_tr_b16 a[134:135], v15 offset:27776\n"
"ds_read_b64_tr_b16 a[136:137], v15 offset:27424\n"
"ds_read_b64_tr_b16 a[138:139], v15 offset:27808\n"
"ds_read_b64_tr_b16 a[140:141], v15 offset:27456\n"
"ds_read_b64_tr_b16 a[142:143], v15 offset:27840\n"
"s_waitcnt vmcnt(24) lgkmcnt(0)\n"
"s_barrier\n"
"s_add_u32 s24, s48, 811008\n"
"s_add_u32 m0, s50, 124160\n"
"s_nop 0\n"
"buffer_load_dwordx4 v10, s[44:47], s24 offen lds\n"
"s_add_u32 m0, s50, 125184\n"
"s_nop 0\n"
"buffer_load_dwordx4 v11, s[44:47], s24 offen lds\n"
"s_add_u32 m0, s50, 126208\n"
"s_nop 0\n"
"buffer_load_dwordx4 v12, s[44:47], s24 offen lds\n"
"s_add_u32 s24, s49, 811008\n"
"s_add_u32 m0, s50, 127264\n"
"s_nop 0\n"
"buffer_load_dwordx4 v10, s[44:47], s24 offen lds\n"
"s_add_u32 m0, s50, 128288\n"
"s_nop 0\n"
"buffer_load_dwordx4 v11, s[44:47], s24 offen lds\n"
"s_add_u32 m0, s50, 129312\n"
"s_nop 0\n"
"buffer_load_dwordx4 v12, s[44:47], s24 offen lds\n"
"ds_read_b64_tr_b16 a[144:145], v13 offset:0\n"
"ds_read_b64_tr_b16 a[146:147], v13 offset:384\n"
"ds_read_b64_tr_b16 a[148:149], v13 offset:32\n"
"ds_read_b64_tr_b16 a[150:151], v13 offset:416\n"
"ds_read_b64_tr_b16 a[152:153], v13 offset:64\n"
"ds_read_b64_tr_b16 a[154:155], v13 offset:448\n"
"ds_read_b64_tr_b16 a[156:157], v13 offset:1536\n"
"ds_read_b64_tr_b16 a[158:159], v13 offset:1920\n"
"ds_read_b64_tr_b16 a[160:161], v13 offset:1568\n"
"ds_read_b64_tr_b16 a[162:163], v13 offset:1952\n"
"ds_read_b64_tr_b16 a[164:165], v13 offset:1600\n"
"ds_read_b64_tr_b16 a[166:167], v13 offset:1984\n"
"s_waitcnt vmcnt(24) lgkmcnt(0)\n"
"s_barrier\n"
"s_add_u32 s24, s48, 884736\n"
"s_add_u32 m0, s50, 0\n"
"s_nop 0\n"
"buffer_load_dwordx4 v10, s[44:47], s24 offen lds\n"
"s_add_u32 m0, s50, 1024\n"
"s_nop 0\n"
"buffer_load_dwordx4 v11, s[44:47], s24 offen lds\n"
"s_add_u32 m0, s50, 2048\n"
"s_nop 0\n"
"buffer_load_dwordx4 v12, s[44:47], s24 offen lds\n"
"s_add_u32 s24, s49, 884736\n"
"s_add_u32 m0, s50, 3104\n"
"s_nop 0\n"
"buffer_load_dwordx4 v10, s[44:47], s24 offen lds\n"
"s_add_u32 m0, s50, 4128\n"
"s_nop 0\n"
"buffer_load_dwordx4 v11, s[44:47], s24 offen lds\n"
"s_add_u32 m0, s50, 5152\n"
"s_nop 0\n"
"buffer_load_dwordx4 v12, s[44:47], s24 offen lds\n"
"ds_read_b64_tr_b16 a[168:169], v13 offset:24832\n"
"ds_read_b64_tr_b16 a[170:171], v13 offset:25216\n"
"ds_read_b64_tr_b16 a[172:173], v13 offset:24864\n"
"ds_read_b64_tr_b16 a[174:175], v13 offset:25248\n"
"ds_read_b64_tr_b16 a[176:177], v13 offset:24896\n"
"ds_read_b64_tr_b16 a[178:179], v13 offset:25280\n"
"ds_read_b64_tr_b16 a[180:181], v13 offset:26368\n"
"ds_read_b64_tr_b16 a[182:183], v13 offset:26752\n"
"ds_read_b64_tr_b16 a[184:185], v13 offset:26400\n"
"ds_read_b64_tr_b16 a[186:187], v13 offset:26784\n"
"ds_read_b64_tr_b16 a[188:189], v13 offset:26432\n"
"ds_read_b64_tr_b16 a[190:191], v13 offset:26816\n"
"s_waitcnt vmcnt(24) lgkmcnt(0)\n"
"s_barrier\n"
"s_add_u32 s24, s48, 958464\n"
"s_add_u32 m0, s50, 24832\n"
"s_nop 0\n"
"buffer_load_dwordx4 v10, s[44:47], s24 offen lds\n"
"s_add_u32 m0, s50, 25856\n"
"s_nop 0\n"
"buffer_load_dwordx4 v11, s[44:47], s24 offen lds\n"
"s_add_u32 m0, s50, 26880\n"
"s_nop 0\n"
"buffer_load_dwordx4 v12, s[44:47], s24 offen lds\n"
"s_add_u32 s24, s49, 958464\n"
"s_add_u32 m0, s50, 27936\n"
"s_nop 0\n"
"buffer_load_dwordx4 v10, s[44:47], s24 offen lds\n"
"s_add_u32 m0, s50, 28960\n"
"s_nop 0\n"
"buffer_load_dwordx4 v11, s[44:47], s24 offen lds\n"
"s_add_u32 m0, s50, 29984\n"
"s_nop 0\n"
"buffer_load_dwordx4 v12, s[44:47], s24 offen lds\n"
"ds_read_b64_tr_b16 a[192:193], v14 offset:512\n"
"ds_read_b64_tr_b16 a[194:195], v14 offset:896\n"
"ds_read_b64_tr_b16 a[196:197], v14 offset:544\n"
"ds_read_b64_tr_b16 a[198:199], v14 offset:928\n"
"ds_read_b64_tr_b16 a[200:201], v14 offset:576\n"
"ds_read_b64_tr_b16 a[202:203], v14 offset:960\n"
"ds_read_b64_tr_b16 a[204:205], v14 offset:2048\n"
"ds_read_b64_tr_b16 a[206:207], v14 offset:2432\n"
"ds_read_b64_tr_b16 a[208:209], v14 offset:2080\n"
"ds_read_b64_tr_b16 a[210:211], v14 offset:2464\n"
"ds_read_b64_tr_b16 a[212:213], v14 offset:2112\n"
"ds_read_b64_tr_b16 a[214:215], v14 offset:2496\n"
"s_waitcnt vmcnt(24) lgkmcnt(0)\n"
"s_barrier\n"
"s_add_u32 s24, s48, 1032192\n"
"s_add_u32 m0, s50, 49664\n"
"s_nop 0\n"
"buffer_load_dwordx4 v10, s[44:47], s24 offen lds\n"
"s_add_u32 m0, s50, 50688\n"
"s_nop 0\n"
"buffer_load_dwordx4 v11, s[44:47], s24 offen lds\n"
"s_add_u32 m0, s50, 51712\n"
"s_nop 0\n"
"buffer_load_dwordx4 v12, s[44:47], s24 offen lds\n"
"s_add_u32 s24, s49, 1032192\n"
"s_add_u32 m0, s50, 52768\n"
"s_nop 0\n"
"buffer_load_dwordx4 v10, s[44:47], s24 offen lds\n"
"s_add_u32 m0, s50, 53792\n"
"s_nop 0\n"
"buffer_load_dwordx4 v11, s[44:47], s24 offen lds\n"
"s_add_u32 m0, s50, 54816\n"
"s_nop 0\n"
"buffer_load_dwordx4 v12, s[44:47], s24 offen lds\n"
"ds_read_b64_tr_b16 a[216:217], v14 offset:25344\n"
"ds_read_b64_tr_b16 a[218:219], v14 offset:25728\n"
"ds_read_b64_tr_b16 a[220:221], v14 offset:25376\n"
"ds_read_b64_tr_b16 a[222:223], v14 offset:25760\n"
"ds_read_b64_tr_b16 a[224:225], v14 offset:25408\n"
"ds_read_b64_tr_b16 a[226:227], v14 offset:25792\n"
"ds_read_b64_tr_b16 a[228:229], v14 offset:26880\n"
"ds_read_b64_tr_b16 a[230:231], v14 offset:27264\n"
"ds_read_b64_tr_b16 a[232:233], v14 offset:26912\n"
"ds_read_b64_tr_b16 a[234:235], v14 offset:27296\n"
"ds_read_b64_tr_b16 a[236:237], v14 offset:26944\n"
"ds_read_b64_tr_b16 a[238:239], v14 offset:27328\n"
"s_waitcnt vmcnt(24) lgkmcnt(0)\n"
"s_barrier\n"
"s_add_u32 s24, s48, 1105920\n"
"s_add_u32 m0, s50, 74496\n"
"s_nop 0\n"
"buffer_load_dwordx4 v10, s[44:47], s24 offen lds\n"
"s_add_u32 m0, s50, 75520\n"
"s_nop 0\n"
"buffer_load_dwordx4 v11, s[44:47], s24 offen lds\n"
"s_add_u32 m0, s50, 76544\n"
"s_nop 0\n"
"buffer_load_dwordx4 v12, s[44:47], s24 offen lds\n"
"s_add_u32 s24, s49, 1105920\n"
"s_add_u32 m0, s50, 77600\n"
"s_nop 0\n"
"buffer_load_dwordx4 v10, s[44:47], s24 offen lds\n"
"s_add_u32 m0, s50, 78624\n"
"s_nop 0\n"
"buffer_load_dwordx4 v11, s[44:47], s24 offen lds\n"
"s_add_u32 m0, s50, 79648\n"
"s_nop 0\n"
"buffer_load_dwordx4 v12, s[44:47], s24 offen lds\n"
"ds_read_b64_tr_b16 a[240:241], v15 offset:1024\n"
"ds_read_b64_tr_b16 a[242:243], v15 offset:1408\n"
"ds_read_b64_tr_b16 a[244:245], v15 offset:1056\n"
"ds_read_b64_tr_b16 a[246:247], v15 offset:1440\n"
"ds_read_b64_tr_b16 a[248:249], v15 offset:1088\n"
"ds_read_b64_tr_b16 a[250:251], v15 offset:1472\n"
"ds_read_b64_tr_b16 a[252:253], v15 offset:2560\n"
"ds_read_b64_tr_b16 a[254:255], v15 offset:2944\n"
"ds_read_b64_tr_b16 v[252:253], v15 offset:2592\n"
"ds_read_b64_tr_b16 v[254:255], v15 offset:2976\n"
"ds_read_b64_tr_b16 v[248:249], v15 offset:2624\n"
"ds_read_b64_tr_b16 v[250:251], v15 offset:3008\n"
"s_waitcnt vmcnt(24) lgkmcnt(0)\n"
"s_barrier\n"
"s_add_u32 s24, s48, 1179648\n"
"s_add_u32 m0, s50, 99328\n"
"s_nop 0\n"
"buffer_load_dwordx4 v10, s[44:47], s24 offen lds\n"
"s_add_u32 m0, s50, 100352\n"
"s_nop 0\n"
"buffer_load_dwordx4 v11, s[44:47], s24 offen lds\n"
"s_add_u32 m0, s50, 101376\n"
"s_nop 0\n"
"buffer_load_dwordx4 v12, s[44:47], s24 offen lds\n"
"s_add_u32 s24, s49, 1179648\n"
"s_add_u32 m0, s50, 102432\n"
"s_nop 0\n"
"buffer_load_dwordx4 v10, s[44:47], s24 offen lds\n"
"s_add_u32 m0, s50, 103456\n"
"s_nop 0\n"
"buffer_load_dwordx4 v11, s[44:47], s24 offen lds\n"
"s_add_u32 m0, s50, 104480\n"
"s_nop 0\n"
"buffer_load_dwordx4 v12, s[44:47], s24 offen lds\n"
"ds_read_b64_tr_b16 v[244:245], v15 offset:25856\n"
"ds_read_b64_tr_b16 v[246:247], v15 offset:26240\n"
"ds_read_b64_tr_b16 v[240:241], v15 offset:25888\n"
"ds_read_b64_tr_b16 v[242:243], v15 offset:26272\n"
"ds_read_b64_tr_b16 v[236:237], v15 offset:25920\n"
"ds_read_b64_tr_b16 v[238:239], v15 offset:26304\n"
"ds_read_b64_tr_b16 v[232:233], v15 offset:27392\n"
"ds_read_b64_tr_b16 v[234:235], v15 offset:27776\n"
"ds_read_b64_tr_b16 v[228:229], v15 offset:27424\n"
"ds_read_b64_tr_b16 v[230:231], v15 offset:27808\n"
"ds_read_b64_tr_b16 v[224:225], v15 offset:27456\n"
"ds_read_b64_tr_b16 v[226:227], v15 offset:27840\n"
"s_waitcnt vmcnt(24) lgkmcnt(0)\n"
"s_barrier\n"
"s_add_u32 s24, s48, 1253376\n"
"s_add_u32 m0, s50, 124160\n"
"s_nop 0\n"
"buffer_load_dwordx4 v10, s[44:47], s24 offen lds\n"
"s_add_u32 m0, s50, 125184\n"
"s_nop 0\n"
"buffer_load_dwordx4 v11, s[44:47], s24 offen lds\n"
"s_add_u32 m0, s50, 126208\n"
"s_nop 0\n"
"buffer_load_dwordx4 v12, s[44:47], s24 offen lds\n"
"s_add_u32 s24, s49, 1253376\n"
"s_add_u32 m0, s50, 127264\n"
"s_nop 0\n"
"buffer_load_dwordx4 v10, s[44:47], s24 offen lds\n"
"s_add_u32 m0, s50, 128288\n"
"s_nop 0\n"
"buffer_load_dwordx4 v11, s[44:47], s24 offen lds\n"
"s_add_u32 m0, s50, 129312\n"
"s_nop 0\n"
"buffer_load_dwordx4 v12, s[44:47], s24 offen lds\n"
"ds_read_b64_tr_b16 v[220:221], v13 offset:0\n"
"ds_read_b64_tr_b16 v[222:223], v13 offset:384\n"
"ds_read_b64_tr_b16 v[216:217], v13 offset:32\n"
"ds_read_b64_tr_b16 v[218:219], v13 offset:416\n"
"ds_read_b64_tr_b16 v[212:213], v13 offset:64\n"
"ds_read_b64_tr_b16 v[214:215], v13 offset:448\n"
"ds_read_b64_tr_b16 v[208:209], v13 offset:1536\n"
"ds_read_b64_tr_b16 v[210:211], v13 offset:1920\n"
"ds_read_b64_tr_b16 v[204:205], v13 offset:1568\n"
"ds_read_b64_tr_b16 v[206:207], v13 offset:1952\n"
"ds_read_b64_tr_b16 v[200:201], v13 offset:1600\n"
"ds_read_b64_tr_b16 v[202:203], v13 offset:1984\n"
"s_waitcnt vmcnt(24) lgkmcnt(0)\n"
"s_barrier\n"
"ds_read_b64_tr_b16 v[196:197], v13 offset:24832\n"
"ds_read_b64_tr_b16 v[198:199], v13 offset:25216\n"
"ds_read_b64_tr_b16 v[192:193], v13 offset:24864\n"
"ds_read_b64_tr_b16 v[194:195], v13 offset:25248\n"
"ds_read_b64_tr_b16 v[188:189], v13 offset:24896\n"
"ds_read_b64_tr_b16 v[190:191], v13 offset:25280\n"
"ds_read_b64_tr_b16 v[184:185], v13 offset:26368\n"
"ds_read_b64_tr_b16 v[186:187], v13 offset:26752\n"
"ds_read_b64_tr_b16 v[180:181], v13 offset:26400\n"
"ds_read_b64_tr_b16 v[182:183], v13 offset:26784\n"
"ds_read_b64_tr_b16 v[176:177], v13 offset:26432\n"
"ds_read_b64_tr_b16 v[178:179], v13 offset:26816\n"
"s_waitcnt vmcnt(18) lgkmcnt(0)\n"
"s_barrier\n"
"ds_read_b64_tr_b16 v[172:173], v14 offset:512\n"
"ds_read_b64_tr_b16 v[174:175], v14 offset:896\n"
"ds_read_b64_tr_b16 v[168:169], v14 offset:544\n"
"ds_read_b64_tr_b16 v[170:171], v14 offset:928\n"
"ds_read_b64_tr_b16 v[164:165], v14 offset:576\n"
"ds_read_b64_tr_b16 v[166:167], v14 offset:960\n"
"ds_read_b64_tr_b16 v[160:161], v14 offset:2048\n"
"ds_read_b64_tr_b16 v[162:163], v14 offset:2432\n"
"ds_read_b64_tr_b16 v[156:157], v14 offset:2080\n"
"ds_read_b64_tr_b16 v[158:159], v14 offset:2464\n"
"ds_read_b64_tr_b16 v[152:153], v14 offset:2112\n"
"ds_read_b64_tr_b16 v[154:155], v14 offset:2496\n"
"s_waitcnt vmcnt(12) lgkmcnt(0)\n"
"s_barrier\n"
"ds_read_b64_tr_b16 v[148:149], v14 offset:25344\n"
"ds_read_b64_tr_b16 v[150:151], v14 offset:25728\n"
"ds_read_b64_tr_b16 v[144:145], v14 offset:25376\n"
"ds_read_b64_tr_b16 v[146:147], v14 offset:25760\n"
"ds_read_b64_tr_b16 v[140:141], v14 offset:25408\n"
"ds_read_b64_tr_b16 v[142:143], v14 offset:25792\n"
"ds_read_b64_tr_b16 v[136:137], v14 offset:26880\n"
"ds_read_b64_tr_b16 v[138:139], v14 offset:27264\n"
"ds_read_b64_tr_b16 v[132:133], v14 offset:26912\n"
"ds_read_b64_tr_b16 v[134:135], v14 offset:27296\n"
"ds_read_b64_tr_b16 v[128:129], v14 offset:26944\n"
"ds_read_b64_tr_b16 v[130:131], v14 offset:27328\n"
"s_waitcnt vmcnt(6) lgkmcnt(0)\n"
"s_barrier\n"
"ds_read_b64_tr_b16 v[124:125], v15 offset:1024\n"
"ds_read_b64_tr_b16 v[126:127], v15 offset:1408\n"
"ds_read_b64_tr_b16 v[120:121], v15 offset:1056\n"
"ds_read_b64_tr_b16 v[122:123], v15 offset:1440\n"
"ds_read_b64_tr_b16 v[116:117], v15 offset:1088\n"
"ds_read_b64_tr_b16 v[118:119], v15 offset:1472\n"
"ds_read_b64_tr_b16 v[112:113], v15 offset:2560\n"
"ds_read_b64_tr_b16 v[114:115], v15 offset:2944\n"
"ds_read_b64_tr_b16 v[108:109], v15 offset:2592\n"
"ds_read_b64_tr_b16 v[110:111], v15 offset:2976\n"
"ds_read_b64_tr_b16 v[104:105], v15 offset:2624\n"
"ds_read_b64_tr_b16 v[106:107], v15 offset:3008\n"
"s_waitcnt vmcnt(0) lgkmcnt(0)\n"
"s_barrier\n"
"ds_read_b64_tr_b16 v[100:101], v15 offset:25856\n"
"ds_read_b64_tr_b16 v[102:103], v15 offset:26240\n"
"ds_read_b64_tr_b16 v[96:97], v15 offset:25888\n"
"ds_read_b64_tr_b16 v[98:99], v15 offset:26272\n"
"ds_read_b64_tr_b16 v[92:93], v15 offset:25920\n"
"ds_read_b64_tr_b16 v[94:95], v15 offset:26304\n"
"ds_read_b64_tr_b16 v[88:89], v15 offset:27392\n"
"ds_read_b64_tr_b16 v[90:91], v15 offset:27776\n"
"ds_read_b64_tr_b16 v[84:85], v15 offset:27424\n"
"ds_read_b64_tr_b16 v[86:87], v15 offset:27808\n"
"ds_read_b64_tr_b16 v[80:81], v15 offset:27456\n"
"ds_read_b64_tr_b16 v[82:83], v15 offset:27840\n"
"s_waitcnt lgkmcnt(0)\n"
"s_barrier\n"
"s_lshl_b32 s39, s15, 10\n"
"v_lshlrev_b32 v4, 4, v0\n"
"v_add_u32 v4, s39, v4\n"
"v_bfe_u32 v3, v0, 4, 2\n"
"v_mul_u32_u24 v5, 512, v3\n"
"v_bfe_u32 v3, v0, 0, 4\n"
"v_lshl_add_u32 v5, v3, 4, v5\n"
"v_add_u32 v6, 32768, v5\n"
"s_mov_b64 s[32:33], s[8:9]\n"
"s_mov_b32 s35, s11\n"
"s_mov_b32 s36, 0\n"
"s_mov_b32 s37, 7\n"
"s_mov_b32 s28, 0\n"
"s_mov_b32 s41, 73728\n"
"s_mov_b32 s43, -1\n"
"v_mov_b32 v64, 163776\n"
"v_mov_b32 v65, 0\n"
"v_mov_b32 v66, 0\n"
"ds_write_b32 v64, v65\n"
"s_waitcnt lgkmcnt(0)\n"
"s_barrier\n"
"s_mul_i32 s24, s13, 3\n"
"s_add_u32 s24, s24, s14\n"
"s_add_u32 s31, s24, 108\n"
"s_lshr_b32 s27, s31, 6\n"
"s_lshl_b32 s27, s27, 13\n"
"s_lshl_b32 s31, s31, 7\n"
"s_add_u32 s31, s31, s27\n"
"s_add_u32 s31, s31, s16\n"
"s_add_u32 s31, s31, 1835008\n"
"s_lshl_b32 s24, s13, 2\n"
"s_add_u32 s24, s24, 92\n"
"v_lshrrev_b32 v17, 4, v0\n"
"v_add_u32 v17, s24, v17\n"
"v_lshrrev_b32 v16, 6, v17\n"
"v_lshlrev_b32 v16, 13, v16\n"
"v_lshl_add_u32 v1, v17, 7, v16\n"
"v_add_u32 v1, 1835008, v1\n"
"v_add_u32 v1, s16, v1\n"
"s_mul_i32 s51, s13, 147456\n"
"s_add_u32 s51, s51, s17\n"
"s_add_u32 s51, s51, 2097152\n"
"s_mul_i32 s24, s14, 12\n"
"s_mul_i32 s25, s15, 3\n"
"s_add_u32 s24, s24, s25\n"
"s_mul_i32 s25, s13, 81920\n"
"s_add_u32 s25, s25, s17\n"
"s_add_u32 s25, s25, 3014656\n"
"s_add_u32 s26, s24, 0\n"
"s_lshr_b32 s27, s26, 3\n"
"s_lshl_b32 s27, s27, 13\n"
"s_lshl_b32 s52, s26, 10\n"
"s_add_u32 s52, s52, s27\n"
"s_add_u32 s52, s52, s25\n"
"s_add_u32 s26, s24, 1\n"
"s_lshr_b32 s27, s26, 3\n"
"s_lshl_b32 s27, s27, 13\n"
"s_lshl_b32 s53, s26, 10\n"
"s_add_u32 s53, s53, s27\n"
"s_add_u32 s53, s53, s25\n"
"s_add_u32 s26, s24, 2\n"
"s_lshr_b32 s27, s26, 3\n"
"s_lshl_b32 s27, s27, 13\n"
"s_lshl_b32 s54, s26, 10\n"
"s_add_u32 s54, s54, s27\n"
"s_add_u32 s54, s54, s25\n"
"v_lshlrev_b32 v7, 4, v0\n"
"s_and_b32 s38, s28, 15\n"
"s_lshl_b32 s38, s38, 22\n"
"s_add_u32 s38, s38, 33554432\n"
"s_add_u32 s38, s38, s51\n"
"MK_F2S_wait:\n"
"s_mov_b32 s29, s30\n"
"buffer_load_dword v3, v1, s[8:11], 0 offen sc1\n"
"s_waitcnt vmcnt(0)\n"
"s_mov_b64 exec, 1\n"
"ds_add_rtn_u32 v66, v64, v65\n"
"s_mov_b64 exec, -1\n"
"s_waitcnt lgkmcnt(0)\n"
"v_and_b32 v67, 3, v66\n"
"v_cmp_eq_u32 vcc, s37, v67\n"
"v_mov_b32 v68, s43\n"
"s_nop 0\n"
"v_cndmask_b32 v67, -1, 0, vcc\n"
"buffer_store_dword v68, v67, s[8:11], s31 offen sc1\n"
"s_mov_b32 s37, 7\n"
"v_mov_b32 v65, 0\n"
"s_branch MK_F2S_poll_in\n"
"MK_F2S_poll:\n"
"buffer_load_dword v3, v1, s[8:11], 0 offen sc1\n"
"s_add_u32 s29, s29, 1\n"
"s_waitcnt vmcnt(0)\n"
"MK_F2S_poll_in:\n"
"v_cmp_gt_i32 vcc, s28, v3\n"
"s_cbranch_vccz MK_F2S_poll_ok\n"
"s_cmp_lt_u32 s29, 131072\n"
"s_cbranch_scc1 MK_F2S_poll\n"
"s_mov_b32 s30, 131072\n"
"MK_F2S_poll_ok:\n"
"s_add_u32 m0, s39, 0\n"
"s_add_u32 s26, s38, 0\n"
"buffer_load_dwordx4 v4, s[8:11], s26 offen sc1 lds\n"
"s_add_u32 m0, s39, 4096\n"
"s_add_u32 s26, s38, 4096\n"
"buffer_load_dwordx4 v4, s[8:11], s26 offen sc1 lds\n"
"s_add_u32 m0, s39, 8192\n"
"s_add_u32 s26, s38, 16384\n"
"buffer_load_dwordx4 v4, s[8:11], s26 offen sc1 lds\n"
"s_add_u32 m0, s39, 12288\n"
"s_add_u32 s26, s38, 20480\n"
"buffer_load_dwordx4 v4, s[8:11], s26 offen sc1 lds\n"
"s_add_u32 m0, s39, 16384\n"
"s_add_u32 s26, s38, 32768\n"
"buffer_load_dwordx4 v4, s[8:11], s26 offen sc1 lds\n"
"s_waitcnt vmcnt(0)\n"
"s_barrier\n"
"ds_read_b128 v[40:43], v5 offset:0\n"
"ds_read_b128 v[44:47], v5 offset:256\n"
"ds_read_b128 v[48:51], v5 offset:2048\n"
"ds_read_b128 v[52:55], v5 offset:2304\n"
"MK_F2S_body:\n"
"s_waitcnt lgkmcnt(2)\n"
"v_mfma_f32_16x16x32_bf16 v[16:19], a[0:3], v[40:43], 0\n"
"ds_read_b128 v[56:59], v5 offset:4096\n"
"s_add_u32 m0, s39, 20480\n"
"s_add_u32 s26, s38, 36864\n"
"v_mfma_f32_16x16x32_bf16 v[20:23], a[4:7], v[40:43], 0\n"
"ds_read_b128 v[60:63], v5 offset:4352\n"
"s_and_b32 s40, s28, 15\n"
"s_lshl_b32 s40, s40, 22\n"
"v_mfma_f32_16x16x32_bf16 v[24:27], a[8:11], v[40:43], 0\n"
"buffer_load_dwordx4 v4, s[8:11], s26 offen sc1 lds\n"
"v_mfma_f32_16x16x32_bf16 v[28:31], a[0:3], v[44:47], 0\n"
"s_add_u32 s40, s40, 33554432\n"
"s_add_u32 s55, s40, s52\n"
"v_mfma_f32_16x16x32_bf16 v[32:35], a[4:7], v[44:47], 0\n"
"s_add_u32 s56, s40, s53\n"
"s_add_u32 s57, s40, s54\n"
"v_mfma_f32_16x16x32_bf16 v[36:39], a[8:11], v[44:47], 0\n"
"s_waitcnt lgkmcnt(2)\n"
"v_mfma_f32_16x16x32_bf16 v[16:19], a[12:15], v[48:51], v[16:19]\n"
"ds_read_b128 v[40:43], v5 offset:6144\n"
"s_add_u32 m0, s39, 24576\n"
"s_add_u32 s26, s38, 49152\n"
"v_mfma_f32_16x16x32_bf16 v[20:23], a[16:19], v[48:51], v[20:23]\n"
"ds_read_b128 v[44:47], v5 offset:6400\n"
"v_mfma_f32_16x16x32_bf16 v[24:27], a[20:23], v[48:51], v[24:27]\n"
"buffer_load_dwordx4 v4, s[8:11], s26 offen sc1 lds\n"
"v_mfma_f32_16x16x32_bf16 v[28:31], a[12:15], v[52:55], v[28:31]\n"
"v_mfma_f32_16x16x32_bf16 v[32:35], a[16:19], v[52:55], v[32:35]\n"
"v_mfma_f32_16x16x32_bf16 v[36:39], a[20:23], v[52:55], v[36:39]\n"
"s_waitcnt lgkmcnt(2)\n"
"v_mfma_f32_16x16x32_bf16 v[16:19], a[24:27], v[56:59], v[16:19]\n"
"ds_read_b128 v[48:51], v5 offset:8192\n"
"s_add_u32 m0, s39, 28672\n"
"s_add_u32 s26, s38, 53248\n"
"v_mfma_f32_16x16x32_bf16 v[20:23], a[28:31], v[56:59], v[20:23]\n"
"ds_read_b128 v[52:55], v5 offset:8448\n"
"v_mfma_f32_16x16x32_bf16 v[24:27], a[32:35], v[56:59], v[24:27]\n"
"buffer_load_dwordx4 v4, s[8:11], s26 offen sc1 lds\n"
"v_mfma_f32_16x16x32_bf16 v[28:31], a[24:27], v[60:63], v[28:31]\n"
"v_mfma_f32_16x16x32_bf16 v[32:35], a[28:31], v[60:63], v[32:35]\n"
"v_mfma_f32_16x16x32_bf16 v[36:39], a[32:35], v[60:63], v[36:39]\n"
"s_waitcnt lgkmcnt(2)\n"
"v_mfma_f32_16x16x32_bf16 v[16:19], a[36:39], v[40:43], v[16:19]\n"
"ds_read_b128 v[56:59], v5 offset:10240\n"
"s_add_u32 m0, s39, 32768\n"
"s_add_u32 s26, s38, 65536\n"
"v_mfma_f32_16x16x32_bf16 v[20:23], a[40:43], v[40:43], v[20:23]\n"
"ds_read_b128 v[60:63], v5 offset:10496\n"
"v_mfma_f32_16x16x32_bf16 v[24:27], a[44:47], v[40:43], v[24:27]\n"
"buffer_load_dwordx4 v4, s[8:11], s26 offen sc1 lds\n"
"v_mfma_f32_16x16x32_bf16 v[28:31], a[36:39], v[44:47], v[28:31]\n"
"v_mfma_f32_16x16x32_bf16 v[32:35], a[40:43], v[44:47], v[32:35]\n"
"v_mfma_f32_16x16x32_bf16 v[36:39], a[44:47], v[44:47], v[36:39]\n"
"s_waitcnt lgkmcnt(2)\n"
"v_mfma_f32_16x16x32_bf16 v[16:19], a[48:51], v[48:51], v[16:19]\n"
"ds_read_b128 v[40:43], v5 offset:12288\n"
"s_add_u32 m0, s39, 36864\n"
"s_add_u32 s26, s38, 69632\n"
"v_mfma_f32_16x16x32_bf16 v[20:23], a[52:55], v[48:51], v[20:23]\n"
"ds_read_b128 v[44:47], v5 offset:12544\n"
"v_mfma_f32_16x16x32_bf16 v[24:27], a[56:59], v[48:51], v[24:27]\n"
"buffer_load_dwordx4 v4, s[8:11], s26 offen sc1 lds\n"
"v_mfma_f32_16x16x32_bf16 v[28:31], a[48:51], v[52:55], v[28:31]\n"
"v_mfma_f32_16x16x32_bf16 v[32:35], a[52:55], v[52:55], v[32:35]\n"
"v_mfma_f32_16x16x32_bf16 v[36:39], a[56:59], v[52:55], v[36:39]\n"
"s_waitcnt lgkmcnt(2)\n"
"v_mfma_f32_16x16x32_bf16 v[16:19], a[60:63], v[56:59], v[16:19]\n"
"ds_read_b128 v[48:51], v5 offset:14336\n"
"s_add_u32 m0, s39, 40960\n"
"s_add_u32 s26, s38, 81920\n"
"v_mfma_f32_16x16x32_bf16 v[20:23], a[64:67], v[56:59], v[20:23]\n"
"ds_read_b128 v[52:55], v5 offset:14592\n"
"v_mfma_f32_16x16x32_bf16 v[24:27], a[68:71], v[56:59], v[24:27]\n"
"buffer_load_dwordx4 v4, s[8:11], s26 offen sc1 lds\n"
"v_mfma_f32_16x16x32_bf16 v[28:31], a[60:63], v[60:63], v[28:31]\n"
"v_mfma_f32_16x16x32_bf16 v[32:35], a[64:67], v[60:63], v[32:35]\n"
"v_mfma_f32_16x16x32_bf16 v[36:39], a[68:71], v[60:63], v[36:39]\n"
"s_waitcnt lgkmcnt(2)\n"
"v_mfma_f32_16x16x32_bf16 v[16:19], a[72:75], v[40:43], v[16:19]\n"
"ds_read_b128 v[56:59], v5 offset:16384\n"
"s_add_u32 m0, s39, 45056\n"
"s_add_u32 s26, s38, 86016\n"
"v_mfma_f32_16x16x32_bf16 v[20:23], a[76:79], v[40:43], v[20:23]\n"
"ds_read_b128 v[60:63], v5 offset:16640\n"
"v_mfma_f32_16x16x32_bf16 v[24:27], a[80:83], v[40:43], v[24:27]\n"
"buffer_load_dwordx4 v4, s[8:11], s26 offen sc1 lds\n"
"v_mfma_f32_16x16x32_bf16 v[28:31], a[72:75], v[44:47], v[28:31]\n"
"v_mfma_f32_16x16x32_bf16 v[32:35], a[76:79], v[44:47], v[32:35]\n"
"v_mfma_f32_16x16x32_bf16 v[36:39], a[80:83], v[44:47], v[36:39]\n"
"s_waitcnt lgkmcnt(2)\n"
"v_mfma_f32_16x16x32_bf16 v[16:19], a[84:87], v[48:51], v[16:19]\n"
"ds_read_b128 v[40:43], v5 offset:18432\n"
"s_add_u32 m0, s39, 49152\n"
"s_add_u32 s26, s38, 98304\n"
"v_mfma_f32_16x16x32_bf16 v[20:23], a[88:91], v[48:51], v[20:23]\n"
"ds_read_b128 v[44:47], v5 offset:18688\n"
"v_mfma_f32_16x16x32_bf16 v[24:27], a[92:95], v[48:51], v[24:27]\n"
"buffer_load_dwordx4 v4, s[8:11], s26 offen sc1 lds\n"
"v_mfma_f32_16x16x32_bf16 v[28:31], a[84:87], v[52:55], v[28:31]\n"
"v_mfma_f32_16x16x32_bf16 v[32:35], a[88:91], v[52:55], v[32:35]\n"
"v_mfma_f32_16x16x32_bf16 v[36:39], a[92:95], v[52:55], v[36:39]\n"
"s_waitcnt lgkmcnt(2)\n"
"v_mfma_f32_16x16x32_bf16 v[16:19], a[96:99], v[56:59], v[16:19]\n"
"s_waitcnt vmcnt(6)\n"
"s_barrier\n"
"v_mfma_f32_16x16x32_bf16 v[20:23], a[100:103], v[56:59], v[20:23]\n"
"ds_read_b128 v[48:51], v5 offset:20480\n"
"ds_read_b128 v[52:55], v5 offset:20736\n"
"v_mfma_f32_16x16x32_bf16 v[24:27], a[104:107], v[56:59], v[24:27]\n"
"s_mov_b64 exec, 1\n"
"ds_add_rtn_u32 v66, v64, v65\n"
"s_mov_b64 exec, -1\n"
"v_mfma_f32_16x16x32_bf16 v[28:31], a[96:99], v[60:63], v[28:31]\n"
"s_add_u32 m0, s39, 53248\n"
"s_add_u32 s26, s38, 102400\n"
"v_mfma_f32_16x16x32_bf16 v[32:35], a[100:103], v[60:63], v[32:35]\n"
"buffer_load_dwordx4 v4, s[8:11], s26 offen sc1 lds\n"
"v_mfma_f32_16x16x32_bf16 v[36:39], a[104:107], v[60:63], v[36:39]\n"
"s_waitcnt lgkmcnt(3)\n"
"v_mfma_f32_16x16x32_bf16 v[16:19], a[108:111], v[40:43], v[16:19]\n"
"ds_read_b128 v[56:59], v5 offset:22528\n"
"v_mfma_f32_16x16x32_bf16 v[20:23], a[112:115], v[40:43], v[20:23]\n"
"ds_read_b128 v[60:63], v5 offset:22784\n"
"v_mfma_f32_16x16x32_bf16 v[24:27], a[116:119], v[40:43], v[24:27]\n"
"s_add_u32 m0, s39, 57344\n"
"s_add_u32 s26, s38, 114688\n"
"s_waitcnt lgkmcnt(2)\n"
"v_and_b32 v67, 3, v66\n"
"v_mfma_f32_16x16x32_bf16 v[28:31], a[108:111], v[44:47], v[28:31]\n"
"buffer_load_dwordx4 v4, s[8:11], s26 offen sc1 lds\n"
"v_mfma_f32_16x16x32_bf16 v[32:35], a[112:115], v[44:47], v[32:35]\n"
"v_cmp_eq_u32 vcc, s37, v67\n"
"v_mov_b32 v68, s43\n"
"v_mfma_f32_16x16x32_bf16 v[36:39], a[116:119], v[44:47], v[36:39]\n"
"s_nop 0\n"
"v_cndmask_b32 v67, -1, 0, vcc\n"
"v_mfma_f32_16x16x32_bf16 v[16:19], a[120:123], v[48:51], v[16:19]\n"
"ds_read_b128 v[40:43], v5 offset:24576\n"
"ds_read_b128 v[44:47], v5 offset:24832\n"
"v_mfma_f32_16x16x32_bf16 v[20:23], a[124:127], v[48:51], v[20:23]\n"
"buffer_store_dword v68, v67, s[8:11], s31 offen sc1\n"
"v_mfma_f32_16x16x32_bf16 v[24:27], a[128:131], v[48:51], v[24:27]\n"
"s_add_u32 m0, s39, 61440\n"
"s_add_u32 s26, s38, 118784\n"
"v_mfma_f32_16x16x32_bf16 v[28:31], a[120:123], v[52:55], v[28:31]\n"
"buffer_load_dwordx4 v4, s[8:11], s26 offen sc1 lds\n"
"v_mfma_f32_16x16x32_bf16 v[32:35], a[124:127], v[52:55], v[32:35]\n"
"v_mfma_f32_16x16x32_bf16 v[36:39], a[128:131], v[52:55], v[36:39]\n"
"s_waitcnt lgkmcnt(2)\n"
"v_mfma_f32_16x16x32_bf16 v[16:19], a[132:135], v[56:59], v[16:19]\n"
"ds_read_b128 v[48:51], v5 offset:26624\n"
"v_mfma_f32_16x16x32_bf16 v[20:23], a[136:139], v[56:59], v[20:23]\n"
"ds_read_b128 v[52:55], v5 offset:26880\n"
"s_add_u32 m0, s39, 65536\n"
"s_add_u32 s26, s38, 131072\n"
"v_mfma_f32_16x16x32_bf16 v[24:27], a[140:143], v[56:59], v[24:27]\n"
"buffer_load_dwordx4 v4, s[8:11], s26 offen sc1 lds\n"
"v_mfma_f32_16x16x32_bf16 v[28:31], a[132:135], v[60:63], v[28:31]\n"
"v_mfma_f32_16x16x32_bf16 v[32:35], a[136:139], v[60:63], v[32:35]\n"
"v_mfma_f32_16x16x32_bf16 v[36:39], a[140:143], v[60:63], v[36:39]\n"
"s_waitcnt lgkmcnt(2)\n"
"v_mfma_f32_16x16x32_bf16 v[16:19], a[144:147], v[40:43], v[16:19]\n"
"s_waitcnt vmcnt(8)\n"
"s_barrier\n"
"v_mfma_f32_16x16x32_bf16 v[20:23], a[148:151], v[40:43], v[20:23]\n"
"ds_read_b128 v[56:59], v5 offset:28672\n"
"s_add_u32 m0, s39, 69632\n"
"s_add_u32 s26, s38, 135168\n"
"v_mfma_f32_16x16x32_bf16 v[24:27], a[152:155], v[40:43], v[24:27]\n"
"ds_read_b128 v[60:63], v5 offset:28928\n"
"v_mfma_f32_16x16x32_bf16 v[28:31], a[144:147], v[44:47], v[28:31]\n"
"buffer_load_dwordx4 v4, s[8:11], s26 offen sc1 lds\n"
"v_mfma_f32_16x16x32_bf16 v[32:35], a[148:151], v[44:47], v[32:35]\n"
"v_mfma_f32_16x16x32_bf16 v[36:39], a[152:155], v[44:47], v[36:39]\n"
"s_waitcnt lgkmcnt(2)\n"
"v_mfma_f32_16x16x32_bf16 v[16:19], a[156:159], v[48:51], v[16:19]\n"
"ds_read_b128 v[40:43], v5 offset:30720\n"
"v_mfma_f32_16x16x32_bf16 v[20:23], a[160:163], v[48:51], v[20:23]\n"
"ds_read_b128 v[44:47], v5 offset:30976\n"
"v_mfma_f32_16x16x32_bf16 v[24:27], a[164:167], v[48:51], v[24:27]\n"
"v_mfma_f32_16x16x32_bf16 v[28:31], a[156:159], v[52:55], v[28:31]\n"
"v_mfma_f32_16x16x32_bf16 v[32:35], a[160:163], v[52:55], v[32:35]\n"
"v_mfma_f32_16x16x32_bf16 v[36:39], a[164:167], v[52:55], v[36:39]\n"
"s_waitcnt lgkmcnt(2)\n"
"v_mfma_f32_16x16x32_bf16 v[16:19], a[168:171], v[56:59], v[16:19]\n"
"ds_read_b128 v[48:51], v6 offset:0\n"
"v_mfma_f32_16x16x32_bf16 v[20:23], a[172:175], v[56:59], v[20:23]\n"
"ds_read_b128 v[52:55], v6 offset:256\n"
"v_mfma_f32_16x16x32_bf16 v[24:27], a[176:179], v[56:59], v[24:27]\n"
"v_mfma_f32_16x16x32_bf16 v[28:31], a[168:171], v[60:63], v[28:31]\n"
"v_mfma_f32_16x16x32_bf16 v[32:35], a[172:175], v[60:63], v[32:35]\n"
"v_mfma_f32_16x16x32_bf16 v[36:39], a[176:179], v[60:63], v[36:39]\n"
"s_waitcnt lgkmcnt(2)\n"
"v_mfma_f32_16x16x32_bf16 v[16:19], a[180:183], v[40:43], v[16:19]\n"
"ds_read_b128 v[56:59], v6 offset:2048\n"
"v_mfma_f32_16x16x32_bf16 v[20:23], a[184:187], v[40:43], v[20:23]\n"
"ds_read_b128 v[60:63], v6 offset:2304\n"
"v_mfma_f32_16x16x32_bf16 v[24:27], a[188:191], v[40:43], v[24:27]\n"
"v_mfma_f32_16x16x32_bf16 v[28:31], a[180:183], v[44:47], v[28:31]\n"
"v_mfma_f32_16x16x32_bf16 v[32:35], a[184:187], v[44:47], v[32:35]\n"
"v_mfma_f32_16x16x32_bf16 v[36:39], a[188:191], v[44:47], v[36:39]\n"
"s_waitcnt lgkmcnt(2)\n"
"v_mfma_f32_16x16x32_bf16 v[16:19], a[192:195], v[48:51], v[16:19]\n"
"ds_read_b128 v[40:43], v6 offset:4096\n"
"v_mfma_f32_16x16x32_bf16 v[20:23], a[196:199], v[48:51], v[20:23]\n"
"ds_read_b128 v[44:47], v6 offset:4352\n"
"v_mfma_f32_16x16x32_bf16 v[24:27], a[200:203], v[48:51], v[24:27]\n"
"v_mfma_f32_16x16x32_bf16 v[28:31], a[192:195], v[52:55], v[28:31]\n"
"v_mfma_f32_16x16x32_bf16 v[32:35], a[196:199], v[52:55], v[32:35]\n"
"v_mfma_f32_16x16x32_bf16 v[36:39], a[200:203], v[52:55], v[36:39]\n"
"s_waitcnt lgkmcnt(2)\n"
"v_mfma_f32_16x16x32_bf16 v[16:19], a[204:207], v[56:59], v[16:19]\n"
"ds_read_b128 v[48:51], v6 offset:6144\n"
"v_mfma_f32_16x16x32_bf16 v[20:23], a[208:211], v[56:59], v[20:23]\n"
"ds_read_b128 v[52:55], v6 offset:6400\n"
"v_mfma_f32_16x16x32_bf16 v[24:27], a[212:215], v[56:59], v[24:27]\n"
"v_mfma_f32_16x16x32_bf16 v[28:31], a[204:207], v[60:63], v[28:31]\n"
"v_mfma_f32_16x16x32_bf16 v[32:35], a[208:211], v[60:63], v[32:35]\n"
"v_mfma_f32_16x16x32_bf16 v[36:39], a[212:215], v[60:63], v[36:39]\n"
"s_waitcnt lgkmcnt(2)\n"
"v_mfma_f32_16x16x32_bf16 v[16:19], a[216:219], v[40:43], v[16:19]\n"
"s_waitcnt vmcnt(0)\n"
"s_barrier\n"
"v_mfma_f32_16x16x32_bf16 v[20:23], a[220:223], v[40:43], v[20:23]\n"
"ds_read_b128 v[56:59], v6 offset:8192\n"
"v_mfma_f32_16x16x32_bf16 v[24:27], a[224:227], v[40:43], v[24:27]\n"
"ds_read_b128 v[60:63], v6 offset:8448\n"
"v_mfma_f32_16x16x32_bf16 v[28:31], a[216:219], v[44:47], v[28:31]\n"
"v_mfma_f32_16x16x32_bf16 v[32:35], a[220:223], v[44:47], v[32:35]\n"
"v_mfma_f32_16x16x32_bf16 v[36:39], a[224:227], v[44:47], v[36:39]\n"
"s_waitcnt lgkmcnt(2)\n"
"v_mfma_f32_16x16x32_bf16 v[16:19], a[228:231], v[48:51], v[16:19]\n"
"ds_read_b128 v[40:43], v6 offset:10240\n"
"ds_read_b128 v[44:47], v6 offset:10496\n"
"v_mfma_f32_16x16x32_bf16 v[20:23], a[232:235], v[48:51], v[20:23]\n"
"buffer_load_dword v3, v1, s[8:11], 0 offen sc1\n"
"v_mfma_f32_16x16x32_bf16 v[24:27], a[236:239], v[48:51], v[24:27]\n"
"s_add_u32 s42, s28, 1\n"
"s_add_u32 s39, s39, s41\n"
"s_and_b32 s38, s42, 15\n"
"v_mfma_f32_16x16x32_bf16 v[28:31], a[228:231], v[52:55], v[28:31]\n"
"s_lshl_b32 s38, s38, 22\n"
"s_add_u32 s38, s38, 33554432\n"
"s_add_u32 s38, s38, s51\n"
"v_mfma_f32_16x16x32_bf16 v[32:35], a[232:235], v[52:55], v[32:35]\n"
"v_mfma_f32_16x16x32_bf16 v[36:39], a[236:239], v[52:55], v[36:39]\n"
"s_waitcnt lgkmcnt(2)\n"
"v_mfma_f32_16x16x32_bf16 v[16:19], a[240:243], v[56:59], v[16:19]\n"
"ds_read_b128 v[48:51], v6 offset:12288\n"
"v_mfma_f32_16x16x32_bf16 v[20:23], a[244:247], v[56:59], v[20:23]\n"
"ds_read_b128 v[52:55], v6 offset:12544\n"
"v_mfma_f32_16x16x32_bf16 v[24:27], a[248:251], v[56:59], v[24:27]\n"
"v_mfma_f32_16x16x32_bf16 v[28:31], a[240:243], v[60:63], v[28:31]\n"
"v_mfma_f32_16x16x32_bf16 v[32:35], a[244:247], v[60:63], v[32:35]\n"
"v_mfma_f32_16x16x32_bf16 v[36:39], a[248:251], v[60:63], v[36:39]\n"
"s_waitcnt lgkmcnt(2)\n"
"v_mfma_f32_16x16x32_bf16 v[16:19], a[252:255], v[40:43], v[16:19]\n"
"ds_read_b128 v[56:59], v6 offset:14336\n"
"v_mfma_f32_16x16x32_bf16 v[20:23], v[252:255], v[40:43], v[20:23]\n"
"ds_read_b128 v[60:63], v6 offset:14592\n"
"v_mfma_f32_16x16x32_bf16 v[24:27], v[248:251], v[40:43], v[24:27]\n"
"v_mfma_f32_16x16x32_bf16 v[28:31], a[252:255], v[44:47], v[28:31]\n"
"v_mfma_f32_16x16x32_bf16 v[32:35], v[252:255], v[44:47], v[32:35]\n"
"v_mfma_f32_16x16x32_bf16 v[36:39], v[248:251], v[44:47], v[36:39]\n"
"s_waitcnt lgkmcnt(2)\n"
"v_mfma_f32_16x16x32_bf16 v[16:19], v[244:247], v[48:51], v[16:19]\n"
"ds_read_b128 v[40:43], v6 offset:16384\n"
"v_mfma_f32_16x16x32_bf16 v[20:23], v[240:243], v[48:51], v[20:23]\n"
"ds_read_b128 v[44:47], v6 offset:16640\n"
"v_mfma_f32_16x16x32_bf16 v[24:27], v[236:239], v[48:51], v[24:27]\n"
"v_mfma_f32_16x16x32_bf16 v[28:31], v[244:247], v[52:55], v[28:31]\n"
"v_mfma_f32_16x16x32_bf16 v[32:35], v[240:243], v[52:55], v[32:35]\n"
"v_mfma_f32_16x16x32_bf16 v[36:39], v[236:239], v[52:55], v[36:39]\n"
"s_waitcnt lgkmcnt(2)\n"
"v_mfma_f32_16x16x32_bf16 v[16:19], v[232:235], v[56:59], v[16:19]\n"
"ds_read_b128 v[48:51], v6 offset:18432\n"
"v_mfma_f32_16x16x32_bf16 v[20:23], v[228:231], v[56:59], v[20:23]\n"
"ds_read_b128 v[52:55], v6 offset:18688\n"
"v_mfma_f32_16x16x32_bf16 v[24:27], v[224:227], v[56:59], v[24:27]\n"
"v_mfma_f32_16x16x32_bf16 v[28:31], v[232:235], v[60:63], v[28:31]\n"
"v_mfma_f32_16x16x32_bf16 v[32:35], v[228:231], v[60:63], v[32:35]\n"
"v_mfma_f32_16x16x32_bf16 v[36:39], v[224:227], v[60:63], v[36:39]\n"
"s_waitcnt lgkmcnt(2)\n"
"v_mfma_f32_16x16x32_bf16 v[16:19], v[220:223], v[40:43], v[16:19]\n"
"ds_read_b128 v[56:59], v6 offset:20480\n"
"ds_read_b128 v[60:63], v6 offset:20736\n"
"v_mfma_f32_16x16x32_bf16 v[20:23], v[216:219], v[40:43], v[20:23]\n"
"s_waitcnt vmcnt(0)\n"
"v_cmp_gt_i32 s[76:77], s42, v3\n"
"v_mfma_f32_16x16x32_bf16 v[24:27], v[212:215], v[40:43], v[24:27]\n"
"v_mfma_f32_16x16x32_bf16 v[28:31], v[220:223], v[44:47], v[28:31]\n"
"s_lshr_b32 s27, s42, 6\n"
"s_or_b32 s76, s76, s27\n"
"v_mfma_f32_16x16x32_bf16 v[32:35], v[216:219], v[44:47], v[32:35]\n"
"s_cmp_eq_u64 s[76:77], 0\n"
"s_cselect_b32 s36, 1, 0\n"
"s_cselect_b32 s34, s10, 0\n"
"v_mfma_f32_16x16x32_bf16 v[36:39], v[212:215], v[44:47], v[36:39]\n"
"s_waitcnt lgkmcnt(2)\n"
"s_add_u32 m0, s39, 0\n"
"s_add_u32 s26, s38, 0\n"
"v_mfma_f32_16x16x32_bf16 v[16:19], v[208:211], v[48:51], v[16:19]\n"
"ds_read_b128 v[40:43], v6 offset:22528\n"
"ds_read_b128 v[44:47], v6 offset:22784\n"
"v_mfma_f32_16x16x32_bf16 v[20:23], v[204:207], v[48:51], v[20:23]\n"
"buffer_load_dwordx4 v4, s[32:35], s26 offen sc1 lds\n"
"v_mfma_f32_16x16x32_bf16 v[24:27], v[200:203], v[48:51], v[24:27]\n"
"v_mfma_f32_16x16x32_bf16 v[28:31], v[208:211], v[52:55], v[28:31]\n"
"v_mfma_f32_16x16x32_bf16 v[32:35], v[204:207], v[52:55], v[32:35]\n"
"v_mfma_f32_16x16x32_bf16 v[36:39], v[200:203], v[52:55], v[36:39]\n"
"s_waitcnt lgkmcnt(2)\n"
"s_add_u32 m0, s39, 4096\n"
"s_add_u32 s26, s38, 4096\n"
"v_mfma_f32_16x16x32_bf16 v[16:19], v[196:199], v[56:59], v[16:19]\n"
"ds_read_b128 v[48:51], v6 offset:24576\n"
"ds_read_b128 v[52:55], v6 offset:24832\n"
"v_mfma_f32_16x16x32_bf16 v[20:23], v[192:195], v[56:59], v[20:23]\n"
"buffer_load_dwordx4 v4, s[32:35], s26 offen sc1 lds\n"
"v_mfma_f32_16x16x32_bf16 v[24:27], v[188:191], v[56:59], v[24:27]\n"
"v_mfma_f32_16x16x32_bf16 v[28:31], v[196:199], v[60:63], v[28:31]\n"
"v_mfma_f32_16x16x32_bf16 v[32:35], v[192:195], v[60:63], v[32:35]\n"
"v_mfma_f32_16x16x32_bf16 v[36:39], v[188:191], v[60:63], v[36:39]\n"
"s_waitcnt lgkmcnt(2)\n"
"s_add_u32 m0, s39, 8192\n"
"s_add_u32 s26, s38, 16384\n"
"v_mfma_f32_16x16x32_bf16 v[16:19], v[184:187], v[40:43], v[16:19]\n"
"ds_read_b128 v[56:59], v6 offset:26624\n"
"ds_read_b128 v[60:63], v6 offset:26880\n"
"v_mfma_f32_16x16x32_bf16 v[20:23], v[180:183], v[40:43], v[20:23]\n"
"buffer_load_dwordx4 v4, s[32:35], s26 offen sc1 lds\n"
"v_mfma_f32_16x16x32_bf16 v[24:27], v[176:179], v[40:43], v[24:27]\n"
"v_mfma_f32_16x16x32_bf16 v[28:31], v[184:187], v[44:47], v[28:31]\n"
"v_mfma_f32_16x16x32_bf16 v[32:35], v[180:183], v[44:47], v[32:35]\n"
"v_mfma_f32_16x16x32_bf16 v[36:39], v[176:179], v[44:47], v[36:39]\n"
"s_waitcnt lgkmcnt(2)\n"
"s_add_u32 m0, s39, 12288\n"
"s_add_u32 s26, s38, 20480\n"
"v_mfma_f32_16x16x32_bf16 v[16:19], v[172:175], v[48:51], v[16:19]\n"
"ds_read_b128 v[40:43], v6 offset:28672\n"
"ds_read_b128 v[44:47], v6 offset:28928\n"
"v_mfma_f32_16x16x32_bf16 v[20:23], v[168:171], v[48:51], v[20:23]\n"
"buffer_load_dwordx4 v4, s[32:35], s26 offen sc1 lds\n"
"v_mfma_f32_16x16x32_bf16 v[24:27], v[164:167], v[48:51], v[24:27]\n"
"v_mfma_f32_16x16x32_bf16 v[28:31], v[172:175], v[52:55], v[28:31]\n"
"v_mfma_f32_16x16x32_bf16 v[32:35], v[168:171], v[52:55], v[32:35]\n"
"v_mfma_f32_16x16x32_bf16 v[36:39], v[164:167], v[52:55], v[36:39]\n"
"s_waitcnt lgkmcnt(2)\n"
"s_add_u32 m0, s39, 16384\n"
"s_add_u32 s26, s38, 32768\n"
"v_mfma_f32_16x16x32_bf16 v[16:19], v[160:163], v[56:59], v[16:19]\n"
"ds_read_b128 v[48:51], v6 offset:30720\n"
"ds_read_b128 v[52:55], v6 offset:30976\n"
"v_mfma_f32_16x16x32_bf16 v[20:23], v[156:159], v[56:59], v[20:23]\n"
"buffer_load_dwordx4 v4, s[32:35], s26 offen sc1 lds\n"
"v_mfma_f32_16x16x32_bf16 v[24:27], v[152:155], v[56:59], v[24:27]\n"
"v_mfma_f32_16x16x32_bf16 v[28:31], v[160:163], v[60:63], v[28:31]\n"
"v_mfma_f32_16x16x32_bf16 v[32:35], v[156:159], v[60:63], v[32:35]\n"
"v_mfma_f32_16x16x32_bf16 v[36:39], v[152:155], v[60:63], v[36:39]\n"
"s_waitcnt lgkmcnt(2)\n"
"v_mfma_f32_16x16x32_bf16 v[16:19], v[148:151], v[40:43], v[16:19]\n"
"ds_read_b128 v[56:59], v6 offset:32768\n"
"v_mfma_f32_16x16x32_bf16 v[20:23], v[144:147], v[40:43], v[20:23]\n"
"ds_read_b128 v[60:63], v6 offset:33024\n"
"v_mfma_f32_16x16x32_bf16 v[24:27], v[140:143], v[40:43], v[24:27]\n"
"v_mfma_f32_16x16x32_bf16 v[28:31], v[148:151], v[44:47], v[28:31]\n"
"v_mfma_f32_16x16x32_bf16 v[32:35], v[144:147], v[44:47], v[32:35]\n"
"v_mfma_f32_16x16x32_bf16 v[36:39], v[140:143], v[44:47], v[36:39]\n"
"s_waitcnt lgkmcnt(2)\n"
"v_mfma_f32_16x16x32_bf16 v[16:19], v[136:139], v[48:51], v[16:19]\n"
"ds_read_b128 v[40:43], v6 offset:34816\n"
"v_mfma_f32_16x16x32_bf16 v[20:23], v[132:135], v[48:51], v[20:23]\n"
"ds_read_b128 v[44:47], v6 offset:35072\n"
"v_mfma_f32_16x16x32_bf16 v[24:27], v[128:131], v[48:51], v[24:27]\n"
"v_mfma_f32_16x16x32_bf16 v[28:31], v[136:139], v[52:55], v[28:31]\n"
"v_mfma_f32_16x16x32_bf16 v[32:35], v[132:135], v[52:55], v[32:35]\n"
"v_mfma_f32_16x16x32_bf16 v[36:39], v[128:131], v[52:55], v[36:39]\n"
"s_waitcnt lgkmcnt(2)\n"
"v_mfma_f32_16x16x32_bf16 v[16:19], v[124:127], v[56:59], v[16:19]\n"
"ds_read_b128 v[48:51], v6 offset:36864\n"
"v_mfma_f32_16x16x32_bf16 v[20:23], v[120:123], v[56:59], v[20:23]\n"
"ds_read_b128 v[52:55], v6 offset:37120\n"
"v_mfma_f32_16x16x32_bf16 v[24:27], v[116:119], v[56:59], v[24:27]\n"
"v_mfma_f32_16x16x32_bf16 v[28:31], v[124:127], v[60:63], v[28:31]\n"
"v_mfma_f32_16x16x32_bf16 v[32:35], v[120:123], v[60:63], v[32:35]\n"
"v_mfma_f32_16x16x32_bf16 v[36:39], v[116:119], v[60:63], v[36:39]\n"
"s_waitcnt lgkmcnt(2)\n"
"v_mfma_f32_16x16x32_bf16 v[16:19], v[112:115], v[40:43], v[16:19]\n"
"ds_read_b128 v[56:59], v6 offset:38912\n"
"v_mfma_f32_16x16x32_bf16 v[28:31], v[112:115], v[44:47], v[28:31]\n"
"ds_read_b128 v[60:63], v6 offset:39168\n"
"s_waitcnt lgkmcnt(3)\n"
"v_mfma_f32_16x16x32_bf16 v[16:19], v[100:103], v[48:51], v[16:19]\n"
"s_waitcnt lgkmcnt(2)\n"
"v_mfma_f32_16x16x32_bf16 v[28:31], v[100:103], v[52:55], v[28:31]\n"
"s_waitcnt lgkmcnt(1)\n"
"v_mfma_f32_16x16x32_bf16 v[16:19], v[88:91], v[56:59], v[16:19]\n"
"s_waitcnt lgkmcnt(0)\n"
"v_mfma_f32_16x16x32_bf16 v[28:31], v[88:91], v[60:63], v[28:31]\n"
"v_mfma_f32_16x16x32_bf16 v[20:23], v[108:111], v[40:43], v[20:23]\n"
"v_add_u32 v5, s41, v5\n"
"v_add_u32 v6, s41, v6\n"
"s_sub_u32 s41, 0, s41\n"
"v_mfma_f32_16x16x32_bf16 v[32:35], v[108:111], v[44:47], v[32:35]\n"
"s_mov_b32 s37, 3\n"
"v_mov_b32 v65, 1\n"
"s_mov_b32 s43, s28\n"
"v_mfma_f32_16x16x32_bf16 v[20:23], v[96:99], v[48:51], v[20:23]\n"
"s_mov_b32 s28, s42\n"
"v_cvt_pk_bf16_f32 v16, v16, v17\n"
"v_cvt_pk_bf16_f32 v17, v18, v19\n"
"v_mfma_f32_16x16x32_bf16 v[32:35], v[96:99], v[52:55], v[32:35]\n"
"v_cvt_pk_bf16_f32 v18, v28, v29\n"
"v_cvt_pk_bf16_f32 v19, v30, v31\n"
"v_mfma_f32_16x16x32_bf16 v[20:23], v[84:87], v[56:59], v[20:23]\n"
"v_permlane16_swap_b32 v16, v18\n"
"v_mfma_f32_16x16x32_bf16 v[32:35], v[84:87], v[60:63], v[32:35]\n"
"v_permlane16_swap_b32 v17, v19\n"
"v_mfma_f32_16x16x32_bf16 v[24:27], v[104:107], v[40:43], v[24:27]\n"
"buffer_store_dwordx4 v[16:19], v7, s[8:11], s55 offen sc1\n"
"v_mfma_f32_16x16x32_bf16 v[36:39], v[104:107], v[44:47], v[36:39]\n"
"s_nop 1\n"
"v_mfma_f32_16x16x32_bf16 v[24:27], v[92:95], v[48:51], v[24:27]\n"
"v_cvt_pk_bf16_f32 v20, v20, v21\n"
"v_cvt_pk_bf16_f32 v21, v22, v23\n"
"v_mfma_f32_16x16x32_bf16 v[36:39], v[92:95], v[52:55], v[36:39]\n"
"v_cvt_pk_bf16_f32 v22, v32, v33\n"
"v_cvt_pk_bf16_f32 v23, v34, v35\n"
"v_mfma_f32_16x16x32_bf16 v[24:27], v[80:83], v[56:59], v[24:27]\n"
"v_permlane16_swap_b32 v20, v22\n"
"v_mfma_f32_16x16x32_bf16 v[36:39], v[80:83], v[60:63], v[36:39]\n"
"v_permlane16_swap_b32 v21, v23\n"
"buffer_store_dwordx4 v[20:23], v7, s[8:11], s56 offen sc1\n"
"s_cmp_eq_u32 s36, 1\n"
"s_cbranch_scc0 MK_F2S_np\n"
"s_waitcnt vmcnt(2)\n"
"s_barrier\n"
"ds_read_b128 v[40:43], v5 offset:0\n"
"ds_read_b128 v[44:47], v5 offset:256\n"
"ds_read_b128 v[48:51], v5 offset:2048\n"
"ds_read_b128 v[52:55], v5 offset:2304\n"
"v_cvt_pk_bf16_f32 v24, v24, v25\n"
"v_cvt_pk_bf16_f32 v25, v26, v27\n"
"v_cvt_pk_bf16_f32 v26, v36, v37\n"
"v_cvt_pk_bf16_f32 v27, v38, v39\n"
"s_nop 0\n"
"v_permlane16_swap_b32 v24, v26\n"
"v_permlane16_swap_b32 v25, v27\n"
"buffer_store_dwordx4 v[24:27], v7, s[8:11], s57 offen sc1\n"
"s_branch MK_F2S_body\n"
"MK_F2S_np:\n"
"s_nop 1\n"
"v_cvt_pk_bf16_f32 v24, v24, v25\n"
"v_cvt_pk_bf16_f32 v25, v26, v27\n"
"v_cvt_pk_bf16_f32 v26, v36, v37\n"
"v_cvt_pk_bf16_f32 v27, v38, v39\n"
"s_nop 0\n"
"v_permlane16_swap_b32 v24, v26\n"
"v_permlane16_swap_b32 v25, v27\n"
"buffer_store_dwordx4 v[24:27], v7, s[8:11], s57 offen sc1\n"
"s_cmp_lt_u32 s28, 64\n"
"s_cbranch_scc1 MK_F2S_wait\n"
"s_waitcnt vmcnt(0)\n"
"s_barrier\n"
"s_cmp_eq_u32 s15, 0\n"
"s_cbranch_scc0 MK_pf6\n"
"v_mov_b32 v3, 63\n"
"s_mov_b64 exec, 1\n"
"buffer_store_dword v3, off, s[8:11], s31 sc1\n"
"s_mov_b64 exec, -1\n"
"MK_pf6:\n"
"s_endpgm\n"
"MK_R1:\n"
"s_mov_b32 s49, 0\n"
"s_branch MK_R_go\n"
"MK_R2:\n"
"s_mov_b32 s49, 1\n"
"MK_R_go:\n"
"s_load_dwordx2 s[32:33], s[6:7], 0\n"
"s_mov_b32 s34, 2359296\n"
"s_mov_b32 s35, 131072\n"
"s_waitcnt lgkmcnt(0)\n"
"s_and_b32 s33, s33, 65535\n"
"s_load_dwordx2 s[36:37], s[6:7], 72\n"
"s_mov_b32 s38, 1152\n"
"s_mov_b32 s39, 131072\n"
"s_waitcnt lgkmcnt(0)\n"
"s_and_b32 s37, s37, 65535\n"
"v_mbcnt_lo_u32_b32 v0, -1, 0\n"
"v_mbcnt_hi_u32_b32 v0, -1, v0\n"
"s_mul_i32 s45, s15, 5\n"
"s_cmp_eq_u32 s15, 3\n"
"s_cselect_b32 s45, 14, s45\n"
"s_add_u32 s24, s45, 0\n"
"s_lshr_b32 s25, s24, 2\n"
"s_lshl_b32 s25, s25, 13\n"
"s_lshl_b32 s24, s24, 11\n"
"s_add_u32 s40, s24, s25\n"
"s_add_u32 s24, s45, 1\n"
"s_lshr_b32 s25, s24, 2\n"
"s_lshl_b32 s25, s25, 13\n"
"s_lshl_b32 s24, s24, 11\n"
"s_add_u32 s41, s24, s25\n"
"s_add_u32 s24, s45, 2\n"
"s_lshr_b32 s25, s24, 2\n"
"s_lshl_b32 s25, s25, 13\n"
"s_lshl_b32 s24, s24, 11\n"
"s_add_u32 s42, s24, s25\n"
"s_add_u32 s24, s45, 3\n"
"s_lshr_b32 s25, s24, 2\n"
"s_lshl_b32 s25, s25, 13\n"
"s_lshl_b32 s24, s24, 11\n"
"s_add_u32 s43, s24, s25\n"
"s_add_u32 s24, s45, 4\n"
"s_lshr_b32 s25, s24, 2\n"
"s_lshl_b32 s25, s25, 13\n"
"s_lshl_b32 s24, s24, 11\n"
"s_add_u32 s44, s24, s25\n"
"s_cmp_ge_u32 s15, 2\n"
"s_cselect_b32 s46, 2147483648, 0\n"
"s_lshl_b32 s47, s13, 7\n"
"v_bfe_u32 v1, v0, 4, 2\n"
"v_mul_u32_u24 v4, 81920, v1\n"
"v_bfe_u32 v1, v0, 3, 1\n"
"v_lshl_add_u32 v4, v1, 9, v4\n"
"v_bfe_u32 v1, v0, 0, 3\n"
"v_lshl_add_u32 v4, v1, 4, v4\n"
"v_add_u32 v4, s47, v4\n"
"v_or_b32 v5, s46, v4\n"
"v_bfe_u32 v1, v0, 5, 1\n"
"v_mul_u32_u24 v12, 1024, v1\n"
"v_bfe_u32 v1, v0, 3, 1\n"
"v_lshl_add_u32 v12, v1, 9, v12\n"
"v_bfe_u32 v1, v0, 0, 3\n"
"v_lshl_add_u32 v12, v1, 4, v12\n"
"v_bfe_u32 v1, v0, 4, 1\n"
"v_lshl_add_u32 v12, v1, 3, v12\n"
"v_add_u32 v12, s47, v12\n"
"v_or_b32 v13, s46, v12\n"
"s_mul_i32 s48, s13, 9216\n"
"s_lshl_b32 s24, s45, 6\n"
"s_add_u32 s48, s48, s24\n"
"v_bfe_u32 v1, v0, 0, 3\n"
"v_mul_u32_u24 v14, 1152, v1\n"
"v_bfe_u32 v1, v0, 5, 1\n"
"v_lshl_add_u32 v14, v1, 5, v14\n"
"v_bfe_u32 v1, v0, 3, 1\n"
"v_lshl_add_u32 v14, v1, 4, v14\n"
"v_bfe_u32 v1, v0, 4, 1\n"
"v_lshl_add_u32 v14, v1, 3, v14\n"
"v_add_u32 v14, s48, v14\n"
"v_or_b32 v15, s46, v14\n"
"v_bfe_u32 v1, v0, 4, 1\n"
"v_mul_u32_u24 v2, 1024, v1\n"
"v_bfe_u32 v1, v0, 3, 1\n"
"v_lshl_add_u32 v2, v1, 9, v2\n"
"v_bfe_u32 v1, v0, 0, 3\n"
"v_lshl_add_u32 v2, v1, 4, v2\n"
"v_add_u32 v2, s47, v2\n"
"v_bfe_u32 v1, v0, 0, 3\n"
"v_mul_u32_u24 v3, 1152, v1\n"
"v_bfe_u32 v1, v0, 4, 2\n"
"v_lshl_add_u32 v3, v1, 5, v3\n"
"v_bfe_u32 v1, v0, 3, 1\n"
"v_lshl_add_u32 v3, v1, 4, v3\n"
"v_add_u32 v3, s48, v3\n"
"s_mov_b32 s54, 0\n"
"s_mov_b32 s55, -1\n"
"v_mov_b32 v1, s40\n"
"v_mov_b32 v6, s41\n"
"v_cndmask_b32 v6, v1, v6, s[54:55]\n"
"v_add_u32 v6, v6, v2\n"
"v_add_u32 v9, 0, v3\n"
"v_mov_b32 v1, s42\n"
"v_mov_b32 v7, s43\n"
"v_cndmask_b32 v7, v1, v7, s[54:55]\n"
"v_add_u32 v7, v7, v2\n"
"v_add_u32 v10, 128, v3\n"
"v_mov_b32 v1, s44\n"
"v_mov_b32 v8, 2147483648\n"
"v_cndmask_b32 v8, v1, v8, s[54:55]\n"
"v_add_u32 v8, v8, v2\n"
"v_add_u32 v11, 256, v3\n"
"v_or_b32 v8, s46, v8\n"
"v_mov_b32 v1, 2147483648\n"
"v_cndmask_b32 v1, 0, v1, s[54:55]\n"
"v_or3_b32 v11, v11, v1, s46\n"
"v_lshlrev_b32 v1, 4, v0\n"
"v_and_b32 v1, 16, v1\n"
"v_mov_b32 v2, 16256\n"
"v_lshlrev_b32 v1, v1, v2\n"
"s_mov_b32 s54, 0x3030303\n"
"s_mov_b32 s55, 0x3030303\n"
"v_cndmask_b32 v56, 0, v1, s[54:55]\n"
"s_mov_b32 s54, 0xc0c0c0c\n"
"s_mov_b32 s55, 0xc0c0c0c\n"
"v_cndmask_b32 v57, 0, v1, s[54:55]\n"
"s_mov_b32 s54, 0x30303030\n"
"s_mov_b32 s55, 0x30303030\n"
"v_cndmask_b32 v58, 0, v1, s[54:55]\n"
"s_mov_b32 s54, 0xc0c0c0c0\n"
"s_mov_b32 s55, 0xc0c0c0c0\n"
"v_cndmask_b32 v59, 0, v1, s[54:55]\n"
"s_mov_b32 s54, 0xff00ff\n"
"s_mov_b32 s55, 0xff00ff\n"
"v_cndmask_b32 v20, 0, v56, s[54:55]\n"
"v_cndmask_b32 v21, 0, v57, s[54:55]\n"
"v_cndmask_b32 v22, 0, v58, s[54:55]\n"
"v_cndmask_b32 v23, 0, v59, s[54:55]\n"
"s_mov_b32 s54, 0xff00ff00\n"
"s_mov_b32 s55, 0xff00ff00\n"
"v_cndmask_b32 v24, 0, v56, s[54:55]\n"
"v_cndmask_b32 v25, 0, v57, s[54:55]\n"
"v_cndmask_b32 v26, 0, v58, s[54:55]\n"
"v_cndmask_b32 v27, 0, v59, s[54:55]\n"
"s_mov_b32 s54, 0xff0000ff\n"
"s_mov_b32 s55, 0x0\n"
"v_cndmask_b32 v28, 0, v56, s[54:55]\n"
"v_cndmask_b32 v29, 0, v57, s[54:55]\n"
"v_cndmask_b32 v30, 0, v58, s[54:55]\n"
"v_cndmask_b32 v31, 0, v59, s[54:55]\n"
"s_mov_b32 s54, 0x0\n"
"s_mov_b32 s55, 0xff0000ff\n"
"v_cndmask_b32 v32, 0, v56, s[54:55]\n"
"v_cndmask_b32 v33, 0, v57, s[54:55]\n"
"v_cndmask_b32 v34, 0, v58, s[54:55]\n"
"v_cndmask_b32 v35, 0, v59, s[54:55]\n"
"s_lshl_b32 s24, s45, 6\n"
"v_bfe_u32 v1, v0, 5, 1\n"
"v_mul_u32_u24 v2, 32, v1\n"
"v_bfe_u32 v1, v0, 3, 1\n"
"v_lshl_add_u32 v2, v1, 4, v2\n"
"v_bfe_u32 v1, v0, 4, 1\n"
"v_lshl_add_u32 v2, v1, 3, v2\n"
"v_add_u32 v2, s24, v2\n"
"v_or_b32 v3, s46, v2\n"
"s_mul_i32 s38, s38, s49\n"
"buffer_load_dwordx2 v[76:77], v2, s[36:39], 0 offen offset:0\n"
"buffer_load_dwordx2 v[84:85], v2, s[36:39], 0 offen offset:64\n"
"buffer_load_dwordx2 v[92:93], v2, s[36:39], 0 offen offset:128\n"
"buffer_load_dwordx2 v[100:101], v2, s[36:39], 0 offen offset:192\n"
"buffer_load_dwordx2 v[108:109], v3, s[36:39], 0 offen offset:256\n"
"s_waitcnt vmcnt(4)\n"
"v_lshlrev_b32 v36, 16, v76\n"
"v_and_b32 v37, 4294901760, v76\n"
"v_lshlrev_b32 v38, 16, v77\n"
"v_and_b32 v39, 4294901760, v77\n"
"s_waitcnt vmcnt(3)\n"
"v_lshlrev_b32 v40, 16, v84\n"
"v_and_b32 v41, 4294901760, v84\n"
"v_lshlrev_b32 v42, 16, v85\n"
"v_and_b32 v43, 4294901760, v85\n"
"s_waitcnt vmcnt(2)\n"
"v_lshlrev_b32 v44, 16, v92\n"
"v_and_b32 v45, 4294901760, v92\n"
"v_lshlrev_b32 v46, 16, v93\n"
"v_and_b32 v47, 4294901760, v93\n"
"s_waitcnt vmcnt(1)\n"
"v_lshlrev_b32 v48, 16, v100\n"
"v_and_b32 v49, 4294901760, v100\n"
"v_lshlrev_b32 v50, 16, v101\n"
"v_and_b32 v51, 4294901760, v101\n"
"s_waitcnt vmcnt(0)\n"
"v_lshlrev_b32 v52, 16, v108\n"
"v_and_b32 v53, 4294901760, v108\n"
"v_lshlrev_b32 v54, 16, v109\n"
"v_and_b32 v55, 4294901760, v109\n"
"s_load_dwordx2 s[36:37], s[6:7], 80\n"
"s_mov_b32 s38, 2359296\n"
"s_mov_b32 s39, 131072\n"
"s_waitcnt lgkmcnt(0)\n"
"s_and_b32 s37, s37, 65535\n"
"s_lshl_b32 s24, s15, 2\n"
"v_bfe_u32 v1, v0, 0, 3\n"
"v_mul_u32_u24 v17, 32, v1\n"
"v_bfe_u32 v1, v0, 4, 1\n"
"v_lshl_add_u32 v17, v1, 4, v17\n"
"v_add_u32 v17, s24, v17\n"
"v_bfe_u32 v1, v0, 0, 3\n"
"v_mul_u32_u24 v18, 32, v1\n"
"s_mov_b32 s56, 0x3ae38e39\n"
"s_mov_b32 s57, 0x3727c5ac\n"
"s_lshl_b32 s24, s49, 2\n"
"s_add_u32 s24, s24, s13\n"
"s_add_u32 s31, s24, 120\n"
"s_lshr_b32 s27, s31, 6\n"
"s_lshl_b32 s27, s27, 13\n"
"s_lshl_b32 s31, s31, 7\n"
"s_add_u32 s31, s31, s27\n"
"s_add_u32 s31, s31, s16\n"
"s_add_u32 s31, s31, 1835008\n"
"s_cmp_eq_u32 s12, 0\n"
"s_cselect_b32 s68, 0, 0\n"
"s_mul_i32 s69, s68, s49\n"
"s_add_u32 s31, s31, s68\n"
"s_sub_u32 s31, s31, s69\n"
"s_mul_i32 s24, s49, s12\n"
"s_lshl_b32 s24, s24, 2\n"
"s_add_u32 s24, s24, s13\n"
"s_add_u32 s50, s24, 128\n"
"s_lshr_b32 s27, s50, 6\n"
"s_lshl_b32 s27, s27, 13\n"
"s_lshl_b32 s50, s50, 7\n"
"s_add_u32 s50, s50, s27\n"
"s_add_u32 s50, s50, s17\n"
"s_add_u32 s50, s50, 1835008\n"
"s_mul_i32 s24, s49, 28\n"
"s_add_u32 s24, s24, 80\n"
"v_lshrrev_b32 v2, 2, v0\n"
"v_add_u32 v2, s24, v2\n"
"s_cmp_eq_u32 s49, 0\n"
"s_cselect_b32 s24, 8, 0\n"
"s_add_u32 s24, s24, 120\n"
"s_add_u32 s24, s24, s13\n"
"s_mov_b32 s60, 0\n"
"s_mov_b32 s61, 0xffff0000\n"
"s_not_b64 s[58:59], s[60:61]\n"
"v_mov_b32 v3, s24\n"
"v_cndmask_b32 v2, v2, v3, s[60:61]\n"
"v_lshrrev_b32 v1, 6, v2\n"
"v_lshlrev_b32 v1, 13, v1\n"
"v_lshl_add_u32 v16, v2, 7, v1\n"
"v_add_u32 v16, 1835008, v16\n"
"v_add_u32 v16, s16, v16\n"
"v_add_u32 v16, s69, v16\n"
"s_cmp_eq_u32 s49, 0\n"
"s_cselect_b32 s24, 81920, 0\n"
"s_cselect_b32 s25, 0, 131072\n"
"s_add_u32 s24, s24, s25\n"
"s_lshl_b32 s25, s13, 7\n"
"v_bfe_u32 v1, v0, 5, 1\n"
"v_mul_u32_u24 v19, 1024, v1\n"
"v_bfe_u32 v1, v0, 3, 1\n"
"v_lshl_add_u32 v19, v1, 9, v19\n"
"v_bfe_u32 v1, v0, 0, 3\n"
"v_lshl_add_u32 v19, v1, 4, v19\n"
"v_add_u32 v19, s25, v19\n"
"v_bfe_u32 v1, v0, 4, 1\n"
"v_mul_lo_u32 v1, v1, s24\n"
"v_add_u32 v19, v19, v1\n"
"s_cmp_eq_u32 s12, 0\n"
"s_cbranch_scc1 MK_R_main\n"
"s_mov_b32 s28, s49\n"
"s_sub_u32 s24, s28, 1\n"
"s_cmp_eq_u32 s15, 0\n"
"s_cbranch_scc0 MK_pf7\n"
"v_mov_b32 v1, s24\n"
"s_mov_b64 exec, 1\n"
"buffer_store_dword v1, off, s[8:11], s50 sc1\n"
"s_mov_b64 exec, -1\n"
"MK_pf7:\n"
"s_mul_i32 s46, s28, 36864\n"
"buffer_load_dwordx4 v[116:119], v9, s[32:35], s46 offen\n"
"buffer_load_dwordx4 v[120:123], v10, s[32:35], s46 offen\n"
"buffer_load_dwordx4 v[124:127], v11, s[32:35], s46 offen\n"
"s_waitcnt vmcnt(0)\n"
"MK_LN0:\n"
"v_mfma_f32_16x16x32_bf16 v[56:59], v[28:31], v[116:119], 0\n"
"v_mfma_f32_16x16x32_bf16 v[60:63], v[32:35], v[116:119], 0\n"
"v_mfma_f32_16x16x32_bf16 v[64:67], v[28:31], v[120:123], 0\n"
"v_mfma_f32_16x16x32_bf16 v[68:71], v[32:35], v[120:123], 0\n"
"v_mfma_f32_16x16x32_bf16 v[72:75], v[28:31], v[124:127], 0\n"
"s_cmp_ge_u32 s28, 47\n"
"s_cselect_b32 s24, 1, 2\n"
"s_add_u32 s52, s28, s24\n"
"s_cmp_eq_u32 s52, 48\n"
"s_cselect_b32 s24, 1, 0\n"
"s_andn2_b32 s24, s24, s49\n"
"s_cmp_eq_u32 s24, 1\n"
"s_cselect_b32 s52, 64, s52\n"
"s_min_u32 s25, s52, 63\n"
"s_mul_i32 s46, s25, 36864\n"
"buffer_load_dwordx4 v[116:119], v9, s[32:35], s46 offen\n"
"buffer_load_dwordx4 v[120:123], v10, s[32:35], s46 offen\n"
"buffer_load_dwordx4 v[124:127], v11, s[32:35], s46 offen\n"
"s_lshl_b32 s24, s28, 10\n"
"s_and_b32 s24, s24, 8192\n"
"s_xor_b32 s24, s24, s17\n"
"s_lshl_b32 s48, s28, 18\n"
"s_add_u32 s48, s48, s24\n"
"s_add_u32 s48, s48, 8388608\n"
"v_add_f32 v128, v56, v57\n"
"v_mul_f32 v130, v56, v56\n"
"v_fmac_f32 v130, v57, v57\n"
"v_add_f32 v128, v128, v58\n"
"v_fmac_f32 v130, v58, v58\n"
"v_add_f32 v128, v128, v59\n"
"v_fmac_f32 v130, v59, v59\n"
"v_add_f32 v128, v128, v60\n"
"v_fmac_f32 v130, v60, v60\n"
"v_add_f32 v128, v128, v61\n"
"v_fmac_f32 v130, v61, v61\n"
"v_add_f32 v128, v128, v62\n"
"v_fmac_f32 v130, v62, v62\n"
"v_add_f32 v128, v128, v63\n"
"v_fmac_f32 v130, v63, v63\n"
"v_add_f32 v128, v128, v64\n"
"v_fmac_f32 v130, v64, v64\n"
"v_add_f32 v128, v128, v65\n"
"v_fmac_f32 v130, v65, v65\n"
"v_add_f32 v128, v128, v66\n"
"v_fmac_f32 v130, v66, v66\n"
"v_add_f32 v128, v128, v67\n"
"v_fmac_f32 v130, v67, v67\n"
"v_add_f32 v128, v128, v68\n"
"v_fmac_f32 v130, v68, v68\n"
"v_add_f32 v128, v128, v69\n"
"v_fmac_f32 v130, v69, v69\n"
"v_add_f32 v128, v128, v70\n"
"v_fmac_f32 v130, v70, v70\n"
"v_add_f32 v128, v128, v71\n"
"v_fmac_f32 v130, v71, v71\n"
"v_add_f32 v128, v128, v72\n"
"v_fmac_f32 v130, v72, v72\n"
"v_add_f32 v128, v128, v73\n"
"v_fmac_f32 v130, v73, v73\n"
"v_add_f32 v128, v128, v74\n"
"v_fmac_f32 v130, v74, v74\n"
"v_add_f32 v128, v128, v75\n"
"v_fmac_f32 v130, v75, v75\n"
"s_nop 1\n"
"v_permlane16_swap_b32 v128, v130\n"
"v_add_f32 v128, v128, v130\n"
"s_nop 1\n"
"v_add_f32_dpp v128, v128, v128 row_ror:8 row_mask:0xf bank_mask:0xf\n"
"v_mov_b32 v129, v128\n"
"s_nop 1\n"
"v_permlane32_swap_b32 v128, v129\n"
"v_add_f32 v128, v128, v129\n"
"ds_write_b32 v17, v128\n"
"s_waitcnt lgkmcnt(0)\n"
"s_barrier\n"
"ds_read_b128 v[128:131], v18\n"
"ds_read_b128 v[132:135], v18 offset:16\n"
"v_xor_b32 v17, 256, v17\n"
"v_xor_b32 v18, 256, v18\n"
"s_waitcnt lgkmcnt(1)\n"
"v_pk_add_f32 v[128:129], v[128:129], v[130:131]\n"
"s_waitcnt lgkmcnt(0)\n"
"v_pk_add_f32 v[132:133], v[132:133], v[134:135]\n"
"v_add_f32 v128, v128, v129\n"
"v_add_f32 v132, v132, v133\n"
"v_mul_f32 v128, s56, v128\n"
"v_mul_f32 v132, s56, v132\n"
"v_fma_f32 v132, -v128, v128, v132\n"
"v_max_f32 v132, 0, v132\n"
"v_add_f32 v132, s57, v132\n"
"v_rsq_f32 v132, v132\n"
"s_nop 0\n"
"v_mul_f32 v128, v128, v132\n"
"v_pk_fma_f32 v[56:57], v[56:57], v[132:133], v[128:129] op_sel_hi:[1,0,0] neg_lo:[0,0,1] neg_hi:[0,0,1]\n"
"v_pk_fma_f32 v[58:59], v[58:59], v[132:133], v[128:129] op_sel_hi:[1,0,0] neg_lo:[0,0,1] neg_hi:[0,0,1]\n"
"v_cvt_pk_bf16_f32 v136, v56, v57\n"
"v_cvt_pk_bf16_f32 v137, v58, v59\n"
"s_add_u32 s24, s48, s40\n"
"buffer_store_dwordx2 v[136:137], v12, s[8:11], s24 offen sc1\n"
"v_pk_fma_f32 v[60:61], v[60:61], v[132:133], v[128:129] op_sel_hi:[1,0,0] neg_lo:[0,0,1] neg_hi:[0,0,1]\n"
"v_pk_fma_f32 v[62:63], v[62:63], v[132:133], v[128:129] op_sel_hi:[1,0,0] neg_lo:[0,0,1] neg_hi:[0,0,1]\n"
"v_cvt_pk_bf16_f32 v140, v60, v61\n"
"v_cvt_pk_bf16_f32 v141, v62, v63\n"
"s_add_u32 s24, s48, s41\n"
"buffer_store_dwordx2 v[140:141], v12, s[8:11], s24 offen sc1\n"
"v_pk_fma_f32 v[64:65], v[64:65], v[132:133], v[128:129] op_sel_hi:[1,0,0] neg_lo:[0,0,1] neg_hi:[0,0,1]\n"
"v_pk_fma_f32 v[66:67], v[66:67], v[132:133], v[128:129] op_sel_hi:[1,0,0] neg_lo:[0,0,1] neg_hi:[0,0,1]\n"
"v_cvt_pk_bf16_f32 v144, v64, v65\n"
"v_cvt_pk_bf16_f32 v145, v66, v67\n"
"s_add_u32 s24, s48, s42\n"
"buffer_store_dwordx2 v[144:145], v12, s[8:11], s24 offen sc1\n"
"v_pk_fma_f32 v[68:69], v[68:69], v[132:133], v[128:129] op_sel_hi:[1,0,0] neg_lo:[0,0,1] neg_hi:[0,0,1]\n"
"v_pk_fma_f32 v[70:71], v[70:71], v[132:133], v[128:129] op_sel_hi:[1,0,0] neg_lo:[0,0,1] neg_hi:[0,0,1]\n"
"v_cvt_pk_bf16_f32 v148, v68, v69\n"
"v_cvt_pk_bf16_f32 v149, v70, v71\n"
"s_add_u32 s24, s48, s43\n"
"buffer_store_dwordx2 v[148:149], v12, s[8:11], s24 offen sc1\n"
"v_pk_fma_f32 v[72:73], v[72:73], v[132:133], v[128:129] op_sel_hi:[1,0,0] neg_lo:[0,0,1] neg_hi:[0,0,1]\n"
"v_pk_fma_f32 v[74:75], v[74:75], v[132:133], v[128:129] op_sel_hi:[1,0,0] neg_lo:[0,0,1] neg_hi:[0,0,1]\n"
"v_cvt_pk_bf16_f32 v152, v72, v73\n"
"v_cvt_pk_bf16_f32 v153, v74, v75\n"
"s_add_u32 s24, s48, s44\n"
"buffer_store_dwordx2 v[152:153], v13, s[8:11], s24 offen sc1\n"
"s_waitcnt vmcnt(0)\n"
"s_barrier\n"
"s_sub_u32 s24, s52, 1\n"
"s_cmp_eq_u32 s52, 64\n"
"s_cselect_b32 s24, 1000, s24\n"
"s_cmp_eq_u32 s15, 0\n"
"s_cbranch_scc0 MK_pf8\n"
"v_mov_b32 v1, s24\n"
"s_mov_b64 exec, 1\n"
"buffer_store_dword v1, off, s[8:11], s50 sc1\n"
"s_mov_b64 exec, -1\n"
"MK_pf8:\n"
"s_mov_b32 s28, s52\n"
"s_cmp_lt_u32 s28, 64\n"
"s_cbranch_scc1 MK_LN0\n"
"s_waitcnt vmcnt(0)\n"
"MK_R_main:\n"
"s_mov_b32 s28, 0\n"
"s_add_u32 s24, s49, s12\n"
"s_cmp_eq_u32 s24, 0\n"
"s_cbranch_scc1 MK_R_x0\n"
"s_mov_b64 s[32:33], s[8:9]\n"
"s_mov_b64 s[34:35], s[10:11]\n"
"v_mov_b32 v9, v6\n"
"v_mov_b32 v10, v7\n"
"v_mov_b32 v11, v8\n"
"s_branch MK_R_sel\n"
"MK_R_x0:\n"
"s_mov_b64 s[60:61], 0\n"
"MK_R_sel:\n"
"s_lshl_b32 s24, s49, s12\n"
"s_lshl_b32 s24, s24, 1\n"
"s_lshr_b32 s25, s15, 1\n"
"s_add_u32 s24, s24, s25\n"
"s_cmp_eq_u32 s24, 1\n"
"s_cbranch_scc1 MK_Rv1\n"
"s_cmp_eq_u32 s24, 2\n"
"s_cbranch_scc1 MK_Rv2\n"
"s_cmp_eq_u32 s24, 3\n"
"s_cbranch_scc1 MK_Rv3\n"
"s_cmp_eq_u32 s24, 4\n"
"s_cbranch_scc1 MK_Rv4\n"
"s_cmp_eq_u32 s24, 5\n"
"s_cbranch_scc1 MK_Rv5\n"
"MK_Rv0:\n"
"s_and_b32 s45, s28, 15\n"
"s_lshl_b32 s45, s45, 22\n"
"s_add_u32 s45, s45, 33554432\n"
"s_add_u32 s45, s45, s17\n"
"s_lshl_b32 s24, s28, 10\n"
"s_and_b32 s24, s24, 8192\n"
"s_xor_b32 s24, s24, s16\n"
"s_lshl_b32 s46, s28, 18\n"
"s_add_u32 s46, s46, s24\n"
"s_add_u32 s46, s46, 8388608\n"
"s_add_u32 s46, s46, 131072\n"
"s_mul_i32 s24, s28, 36864\n"
"s_cmp_eq_u32 s12, 0\n"
"s_cselect_b32 s46, s24, s46\n"
"s_add_u32 s48, s45, 3342336\n"
"s_add_u32 s48, s48, s68\n"
"s_add_u32 s25, s45, 2686976\n"
"s_add_u32 s62, s25, s40\n"
"s_add_u32 s63, s25, s41\n"
"s_add_u32 s64, s25, s42\n"
"s_add_u32 s65, s25, s43\n"
"s_add_u32 s66, s25, s44\n"
"s_mov_b32 s29, s30\n"
"MK_Rv0q:\n"
"buffer_load_dword v1, v16, s[8:11], 0 offen sc1\n"
"s_add_u32 s29, s29, 1\n"
"s_waitcnt vmcnt(0)\n"
"v_cmp_gt_i32 vcc, s28, v1\n"
"s_and_b64 vcc, vcc, s[60:61]\n"
"s_cbranch_vccz MK_Rv0q_ok\n"
"s_cmp_lt_u32 s29, 131072\n"
"s_cbranch_scc1 MK_Rv0q\n"
"s_mov_b32 s30, 131072\n"
"MK_Rv0q_ok:\n"
"buffer_load_dwordx4 v[116:119], v9, s[32:35], s46 offen sc1\n"
"buffer_load_dwordx4 v[120:123], v10, s[32:35], s46 offen sc1\n"
"buffer_load_dwordx4 v[124:127], v11, s[32:35], s46 offen sc1\n"
"v_cmp_gt_i32 vcc, s28, v1\n"
"s_and_b64 vcc, vcc, s[58:59]\n"
"s_cbranch_vccz MK_Rv0p_ok\n"
"s_mov_b32 s29, s30\n"
"MK_Rv0p:\n"
"buffer_load_dword v1, v16, s[8:11], 0 offen sc1\n"
"s_add_u32 s29, s29, 1\n"
"s_waitcnt vmcnt(0)\n"
"v_cmp_gt_i32 vcc, s28, v1\n"
"s_and_b64 vcc, vcc, s[58:59]\n"
"s_cbranch_vccz MK_Rv0p_ok\n"
"s_cmp_lt_u32 s29, 131072\n"
"s_cbranch_scc1 MK_Rv0p\n"
"s_mov_b32 s30, 131072\n"
"MK_Rv0p_ok:\n"
"buffer_load_dwordx4 v[76:79], v4, s[8:11], s62 offen offset:0 sc1\n"
"buffer_load_dwordx4 v[80:83], v4, s[8:11], s62 offen offset:1024 sc1\n"
"buffer_load_dwordx4 v[84:87], v4, s[8:11], s63 offen offset:0 sc1\n"
"buffer_load_dwordx4 v[88:91], v4, s[8:11], s63 offen offset:1024 sc1\n"
"buffer_load_dwordx4 v[92:95], v4, s[8:11], s64 offen offset:0 sc1\n"
"buffer_load_dwordx4 v[96:99], v4, s[8:11], s64 offen offset:1024 sc1\n"
"buffer_load_dwordx4 v[100:103], v4, s[8:11], s65 offen offset:0 sc1\n"
"buffer_load_dwordx4 v[104:107], v4, s[8:11], s65 offen offset:1024 sc1\n"
"buffer_load_dwordx4 v[108:111], v4, s[8:11], s66 offen offset:0 sc1\n"
"buffer_load_dwordx4 v[112:115], v4, s[8:11], s66 offen offset:1024 sc1\n"
"s_waitcnt vmcnt(12)\n"
"v_mfma_f32_16x16x32_bf16 v[56:59], v[28:31], v[116:119], v[36:39]\n"
"v_mfma_f32_16x16x32_bf16 v[60:63], v[32:35], v[116:119], v[40:43]\n"
"s_waitcnt vmcnt(11)\n"
"v_mfma_f32_16x16x32_bf16 v[64:67], v[28:31], v[120:123], v[44:47]\n"
"v_mfma_f32_16x16x32_bf16 v[68:71], v[32:35], v[120:123], v[48:51]\n"
"s_waitcnt vmcnt(10)\n"
"v_mfma_f32_16x16x32_bf16 v[72:75], v[28:31], v[124:127], v[52:55]\n"
"s_waitcnt vmcnt(9)\n"
"v_mfma_f32_16x16x32_bf16 v[56:59], v[20:23], v[76:79], v[56:59]\n"
"s_waitcnt vmcnt(8)\n"
"v_mfma_f32_16x16x32_bf16 v[56:59], v[24:27], v[80:83], v[56:59]\n"
"s_waitcnt vmcnt(7)\n"
"v_mfma_f32_16x16x32_bf16 v[60:63], v[20:23], v[84:87], v[60:63]\n"
"s_waitcnt vmcnt(6)\n"
"v_mfma_f32_16x16x32_bf16 v[60:63], v[24:27], v[88:91], v[60:63]\n"
"s_nop 3\n"
"v_add_f32 v128, v56, v57\n"
"v_mul_f32 v130, v56, v56\n"
"v_fmac_f32 v130, v57, v57\n"
"v_add_f32 v128, v128, v58\n"
"v_fmac_f32 v130, v58, v58\n"
"v_add_f32 v128, v128, v59\n"
"v_fmac_f32 v130, v59, v59\n"
"s_waitcnt vmcnt(5)\n"
"v_mfma_f32_16x16x32_bf16 v[64:67], v[20:23], v[92:95], v[64:67]\n"
"s_waitcnt vmcnt(4)\n"
"v_mfma_f32_16x16x32_bf16 v[64:67], v[24:27], v[96:99], v[64:67]\n"
"v_add_f32 v128, v128, v60\n"
"v_fmac_f32 v130, v60, v60\n"
"v_add_f32 v128, v128, v61\n"
"v_fmac_f32 v130, v61, v61\n"
"v_add_f32 v128, v128, v62\n"
"v_fmac_f32 v130, v62, v62\n"
"v_add_f32 v128, v128, v63\n"
"v_fmac_f32 v130, v63, v63\n"
"s_waitcnt vmcnt(3)\n"
"v_mfma_f32_16x16x32_bf16 v[68:71], v[20:23], v[100:103], v[68:71]\n"
"s_waitcnt vmcnt(2)\n"
"v_mfma_f32_16x16x32_bf16 v[68:71], v[24:27], v[104:107], v[68:71]\n"
"v_add_f32 v128, v128, v64\n"
"v_fmac_f32 v130, v64, v64\n"
"v_add_f32 v128, v128, v65\n"
"v_fmac_f32 v130, v65, v65\n"
"v_add_f32 v128, v128, v66\n"
"v_fmac_f32 v130, v66, v66\n"
"v_add_f32 v128, v128, v67\n"
"v_fmac_f32 v130, v67, v67\n"
"s_waitcnt vmcnt(1)\n"
"v_mfma_f32_16x16x32_bf16 v[72:75], v[20:23], v[108:111], v[72:75]\n"
"s_waitcnt vmcnt(0)\n"
"v_mfma_f32_16x16x32_bf16 v[72:75], v[24:27], v[112:115], v[72:75]\n"
"v_add_f32 v128, v128, v68\n"
"v_fmac_f32 v130, v68, v68\n"
"v_add_f32 v128, v128, v69\n"
"v_fmac_f32 v130, v69, v69\n"
"v_add_f32 v128, v128, v70\n"
"v_fmac_f32 v130, v70, v70\n"
"v_add_f32 v128, v128, v71\n"
"v_fmac_f32 v130, v71, v71\n"
"v_add_f32 v128, v128, v72\n"
"v_fmac_f32 v130, v72, v72\n"
"v_add_f32 v128, v128, v73\n"
"v_fmac_f32 v130, v73, v73\n"
"v_add_f32 v128, v128, v74\n"
"v_fmac_f32 v130, v74, v74\n"
"v_add_f32 v128, v128, v75\n"
"v_fmac_f32 v130, v75, v75\n"
"s_nop 1\n"
"v_permlane16_swap_b32 v128, v130\n"
"v_add_f32 v128, v128, v130\n"
"s_nop 1\n"
"v_add_f32_dpp v128, v128, v128 row_ror:8 row_mask:0xf bank_mask:0xf\n"
"v_mov_b32 v129, v128\n"
"s_nop 1\n"
"v_permlane32_swap_b32 v128, v129\n"
"v_add_f32 v128, v128, v129\n"
"ds_write_b32 v17, v128\n"
"v_cvt_pk_bf16_f32 v138, v56, v57\n"
"v_cvt_pk_bf16_f32 v139, v58, v59\n"
"v_cvt_pk_bf16_f32 v142, v60, v61\n"
"v_cvt_pk_bf16_f32 v143, v62, v63\n"
"v_cvt_pk_bf16_f32 v146, v64, v65\n"
"v_cvt_pk_bf16_f32 v147, v66, v67\n"
"v_cvt_pk_bf16_f32 v150, v68, v69\n"
"v_cvt_pk_bf16_f32 v151, v70, v71\n"
"v_cvt_pk_bf16_f32 v154, v72, v73\n"
"v_cvt_pk_bf16_f32 v155, v74, v75\n"
"s_waitcnt lgkmcnt(0)\n"
"s_barrier\n"
"ds_read_b128 v[128:131], v18\n"
"ds_read_b128 v[132:135], v18 offset:16\n"
"v_xor_b32 v17, 256, v17\n"
"v_xor_b32 v18, 256, v18\n"
"s_waitcnt lgkmcnt(1)\n"
"v_pk_add_f32 v[128:129], v[128:129], v[130:131]\n"
"s_waitcnt lgkmcnt(0)\n"
"v_pk_add_f32 v[132:133], v[132:133], v[134:135]\n"
"v_add_f32 v128, v128, v129\n"
"v_add_f32 v132, v132, v133\n"
"v_mul_f32 v128, s56, v128\n"
"v_mul_f32 v132, s56, v132\n"
"v_fma_f32 v132, -v128, v128, v132\n"
"v_max_f32 v132, 0, v132\n"
"v_add_f32 v132, s57, v132\n"
"v_rsq_f32 v132, v132\n"
"s_nop 0\n"
"v_mul_f32 v128, v128, v132\n"
"v_pk_fma_f32 v[56:57], v[56:57], v[132:133], v[128:129] op_sel_hi:[1,0,0] neg_lo:[0,0,1] neg_hi:[0,0,1]\n"
"v_pk_fma_f32 v[58:59], v[58:59], v[132:133], v[128:129] op_sel_hi:[1,0,0] neg_lo:[0,0,1] neg_hi:[0,0,1]\n"
"v_cvt_pk_bf16_f32 v136, v56, v57\n"
"v_cvt_pk_bf16_f32 v137, v58, v59\n"
"s_add_u32 s24, s48, s40\n"
"v_permlane16_swap_b32 v136, v138\n"
"v_permlane16_swap_b32 v137, v139\n"
"buffer_store_dwordx4 v[136:139], v19, s[8:11], s24 offen sc1\n"
"v_pk_fma_f32 v[60:61], v[60:61], v[132:133], v[128:129] op_sel_hi:[1,0,0] neg_lo:[0,0,1] neg_hi:[0,0,1]\n"
"v_pk_fma_f32 v[62:63], v[62:63], v[132:133], v[128:129] op_sel_hi:[1,0,0] neg_lo:[0,0,1] neg_hi:[0,0,1]\n"
"v_cvt_pk_bf16_f32 v140, v60, v61\n"
"v_cvt_pk_bf16_f32 v141, v62, v63\n"
"s_add_u32 s24, s48, s41\n"
"v_permlane16_swap_b32 v140, v142\n"
"v_permlane16_swap_b32 v141, v143\n"
"buffer_store_dwordx4 v[140:143], v19, s[8:11], s24 offen sc1\n"
"v_pk_fma_f32 v[64:65], v[64:65], v[132:133], v[128:129] op_sel_hi:[1,0,0] neg_lo:[0,0,1] neg_hi:[0,0,1]\n"
"v_pk_fma_f32 v[66:67], v[66:67], v[132:133], v[128:129] op_sel_hi:[1,0,0] neg_lo:[0,0,1] neg_hi:[0,0,1]\n"
"v_cvt_pk_bf16_f32 v144, v64, v65\n"
"v_cvt_pk_bf16_f32 v145, v66, v67\n"
"s_add_u32 s24, s48, s42\n"
"v_permlane16_swap_b32 v144, v146\n"
"v_permlane16_swap_b32 v145, v147\n"
"buffer_store_dwordx4 v[144:147], v19, s[8:11], s24 offen sc1\n"
"v_pk_fma_f32 v[68:69], v[68:69], v[132:133], v[128:129] op_sel_hi:[1,0,0] neg_lo:[0,0,1] neg_hi:[0,0,1]\n"
"v_pk_fma_f32 v[70:71], v[70:71], v[132:133], v[128:129] op_sel_hi:[1,0,0] neg_lo:[0,0,1] neg_hi:[0,0,1]\n"
"v_cvt_pk_bf16_f32 v148, v68, v69\n"
"v_cvt_pk_bf16_f32 v149, v70, v71\n"
"s_add_u32 s24, s48, s43\n"
"v_permlane16_swap_b32 v148, v150\n"
"v_permlane16_swap_b32 v149, v151\n"
"buffer_store_dwordx4 v[148:151], v19, s[8:11], s24 offen sc1\n"
"v_pk_fma_f32 v[72:73], v[72:73], v[132:133], v[128:129] op_sel_hi:[1,0,0] neg_lo:[0,0,1] neg_hi:[0,0,1]\n"
"v_pk_fma_f32 v[74:75], v[74:75], v[132:133], v[128:129] op_sel_hi:[1,0,0] neg_lo:[0,0,1] neg_hi:[0,0,1]\n"
"v_cvt_pk_bf16_f32 v152, v72, v73\n"
"v_cvt_pk_bf16_f32 v153, v74, v75\n"
"s_add_u32 s24, s48, s44\n"
"v_permlane16_swap_b32 v152, v154\n"
"v_permlane16_swap_b32 v153, v155\n"
"buffer_store_dwordx4 v[152:155], v19, s[8:11], s24 offen sc1\n"
"s_waitcnt vmcnt(0)\n"
"s_barrier\n"
"s_cmp_eq_u32 s15, 0\n"
"s_cbranch_scc0 MK_pf9\n"
"v_mov_b32 v2, s28\n"
"s_mov_b64 exec, 1\n"
"buffer_store_dword v2, off, s[8:11], s31 sc1\n"
"s_mov_b64 exec, -1\n"
"MK_pf9:\n"
"s_add_u32 s28, s28, 1\n"
"s_cmp_lt_u32 s28, 64\n"
"s_cbranch_scc1 MK_Rv0\n"
"s_endpgm\n"
"MK_Rv1:\n"
"s_and_b32 s45, s28, 15\n"
"s_lshl_b32 s45, s45, 22\n"
"s_add_u32 s45, s45, 33554432\n"
"s_add_u32 s45, s45, s17\n"
"s_lshl_b32 s24, s28, 10\n"
"s_and_b32 s24, s24, 8192\n"
"s_xor_b32 s24, s24, s16\n"
"s_lshl_b32 s46, s28, 18\n"
"s_add_u32 s46, s46, s24\n"
"s_add_u32 s46, s46, 8388608\n"
"s_add_u32 s46, s46, 131072\n"
"s_mul_i32 s24, s28, 36864\n"
"s_cmp_eq_u32 s12, 0\n"
"s_cselect_b32 s46, s24, s46\n"
"s_add_u32 s48, s45, 3342336\n"
"s_add_u32 s48, s48, s68\n"
"s_add_u32 s25, s45, 2686976\n"
"s_add_u32 s62, s25, s40\n"
"s_add_u32 s63, s25, s41\n"
"s_add_u32 s64, s25, s42\n"
"s_add_u32 s65, s25, s43\n"
"s_mov_b32 s29, s30\n"
"MK_Rv1q:\n"
"buffer_load_dword v1, v16, s[8:11], 0 offen sc1\n"
"s_add_u32 s29, s29, 1\n"
"s_waitcnt vmcnt(0)\n"
"v_cmp_gt_i32 vcc, s28, v1\n"
"s_and_b64 vcc, vcc, s[60:61]\n"
"s_cbranch_vccz MK_Rv1q_ok\n"
"s_cmp_lt_u32 s29, 131072\n"
"s_cbranch_scc1 MK_Rv1q\n"
"s_mov_b32 s30, 131072\n"
"MK_Rv1q_ok:\n"
"buffer_load_dwordx4 v[116:119], v9, s[32:35], s46 offen sc1\n"
"buffer_load_dwordx4 v[120:123], v10, s[32:35], s46 offen sc1\n"
"v_cmp_gt_i32 vcc, s28, v1\n"
"s_and_b64 vcc, vcc, s[58:59]\n"
"s_cbranch_vccz MK_Rv1p_ok\n"
"s_mov_b32 s29, s30\n"
"MK_Rv1p:\n"
"buffer_load_dword v1, v16, s[8:11], 0 offen sc1\n"
"s_add_u32 s29, s29, 1\n"
"s_waitcnt vmcnt(0)\n"
"v_cmp_gt_i32 vcc, s28, v1\n"
"s_and_b64 vcc, vcc, s[58:59]\n"
"s_cbranch_vccz MK_Rv1p_ok\n"
"s_cmp_lt_u32 s29, 131072\n"
"s_cbranch_scc1 MK_Rv1p\n"
"s_mov_b32 s30, 131072\n"
"MK_Rv1p_ok:\n"
"buffer_load_dwordx4 v[76:79], v4, s[8:11], s62 offen offset:0 sc1\n"
"buffer_load_dwordx4 v[80:83], v4, s[8:11], s62 offen offset:1024 sc1\n"
"buffer_load_dwordx4 v[84:87], v4, s[8:11], s63 offen offset:0 sc1\n"
"buffer_load_dwordx4 v[88:91], v4, s[8:11], s63 offen offset:1024 sc1\n"
"buffer_load_dwordx4 v[92:95], v4, s[8:11], s64 offen offset:0 sc1\n"
"buffer_load_dwordx4 v[96:99], v4, s[8:11], s64 offen offset:1024 sc1\n"
"buffer_load_dwordx4 v[100:103], v4, s[8:11], s65 offen offset:0 sc1\n"
"buffer_load_dwordx4 v[104:107], v4, s[8:11], s65 offen offset:1024 sc1\n"
"s_waitcnt vmcnt(9)\n"
"v_mfma_f32_16x16x32_bf16 v[56:59], v[28:31], v[116:119], v[36:39]\n"
"v_mfma_f32_16x16x32_bf16 v[60:63], v[32:35], v[116:119], v[40:43]\n"
"s_waitcnt vmcnt(8)\n"
"v_mfma_f32_16x16x32_bf16 v[64:67], v[28:31], v[120:123], v[44:47]\n"
"v_mfma_f32_16x16x32_bf16 v[68:71], v[32:35], v[120:123], v[48:51]\n"
"s_waitcnt vmcnt(7)\n"
"v_mfma_f32_16x16x32_bf16 v[56:59], v[20:23], v[76:79], v[56:59]\n"
"s_waitcnt vmcnt(6)\n"
"v_mfma_f32_16x16x32_bf16 v[56:59], v[24:27], v[80:83], v[56:59]\n"
"s_waitcnt vmcnt(5)\n"
"v_mfma_f32_16x16x32_bf16 v[60:63], v[20:23], v[84:87], v[60:63]\n"
"s_waitcnt vmcnt(4)\n"
"v_mfma_f32_16x16x32_bf16 v[60:63], v[24:27], v[88:91], v[60:63]\n"
"s_nop 3\n"
"v_add_f32 v128, v56, v57\n"
"v_mul_f32 v130, v56, v56\n"
"v_fmac_f32 v130, v57, v57\n"
"v_add_f32 v128, v128, v58\n"
"v_fmac_f32 v130, v58, v58\n"
"v_add_f32 v128, v128, v59\n"
"v_fmac_f32 v130, v59, v59\n"
"s_waitcnt vmcnt(3)\n"
"v_mfma_f32_16x16x32_bf16 v[64:67], v[20:23], v[92:95], v[64:67]\n"
"s_waitcnt vmcnt(2)\n"
"v_mfma_f32_16x16x32_bf16 v[64:67], v[24:27], v[96:99], v[64:67]\n"
"v_add_f32 v128, v128, v60\n"
"v_fmac_f32 v130, v60, v60\n"
"v_add_f32 v128, v128, v61\n"
"v_fmac_f32 v130, v61, v61\n"
"v_add_f32 v128, v128, v62\n"
"v_fmac_f32 v130, v62, v62\n"
"v_add_f32 v128, v128, v63\n"
"v_fmac_f32 v130, v63, v63\n"
"s_waitcnt vmcnt(1)\n"
"v_mfma_f32_16x16x32_bf16 v[68:71], v[20:23], v[100:103], v[68:71]\n"
"s_waitcnt vmcnt(0)\n"
"v_mfma_f32_16x16x32_bf16 v[68:71], v[24:27], v[104:107], v[68:71]\n"
"v_add_f32 v128, v128, v64\n"
"v_fmac_f32 v130, v64, v64\n"
"v_add_f32 v128, v128, v65\n"
"v_fmac_f32 v130, v65, v65\n"
"v_add_f32 v128, v128, v66\n"
"v_fmac_f32 v130, v66, v66\n"
"v_add_f32 v128, v128, v67\n"
"v_fmac_f32 v130, v67, v67\n"
"v_add_f32 v128, v128, v68\n"
"v_fmac_f32 v130, v68, v68\n"
"v_add_f32 v128, v128, v69\n"
"v_fmac_f32 v130, v69, v69\n"
"v_add_f32 v128, v128, v70\n"
"v_fmac_f32 v130, v70, v70\n"
"v_add_f32 v128, v128, v71\n"
"v_fmac_f32 v130, v71, v71\n"
"s_nop 1\n"
"v_permlane16_swap_b32 v128, v130\n"
"v_add_f32 v128, v128, v130\n"
"s_nop 1\n"
"v_add_f32_dpp v128, v128, v128 row_ror:8 row_mask:0xf bank_mask:0xf\n"
"v_mov_b32 v129, v128\n"
"s_nop 1\n"
"v_permlane32_swap_b32 v128, v129\n"
"v_add_f32 v128, v128, v129\n"
"ds_write_b32 v17, v128\n"
"v_cvt_pk_bf16_f32 v138, v56, v57\n"
"v_cvt_pk_bf16_f32 v139, v58, v59\n"
"v_cvt_pk_bf16_f32 v142, v60, v61\n"
"v_cvt_pk_bf16_f32 v143, v62, v63\n"
"v_cvt_pk_bf16_f32 v146, v64, v65\n"
"v_cvt_pk_bf16_f32 v147, v66, v67\n"
"v_cvt_pk_bf16_f32 v150, v68, v69\n"
"v_cvt_pk_bf16_f32 v151, v70, v71\n"
"s_waitcnt lgkmcnt(0)\n"
"s_barrier\n"
"ds_read_b128 v[128:131], v18\n"
"ds_read_b128 v[132:135], v18 offset:16\n"
"v_xor_b32 v17, 256, v17\n"
"v_xor_b32 v18, 256, v18\n"
"s_waitcnt lgkmcnt(1)\n"
"v_pk_add_f32 v[128:129], v[128:129], v[130:131]\n"
"s_waitcnt lgkmcnt(0)\n"
"v_pk_add_f32 v[132:133], v[132:133], v[134:135]\n"
"v_add_f32 v128, v128, v129\n"
"v_add_f32 v132, v132, v133\n"
"v_mul_f32 v128, s56, v128\n"
"v_mul_f32 v132, s56, v132\n"
"v_fma_f32 v132, -v128, v128, v132\n"
"v_max_f32 v132, 0, v132\n"
"v_add_f32 v132, s57, v132\n"
"v_rsq_f32 v132, v132\n"
"s_nop 0\n"
"v_mul_f32 v128, v128, v132\n"
"v_pk_fma_f32 v[56:57], v[56:57], v[132:133], v[128:129] op_sel_hi:[1,0,0] neg_lo:[0,0,1] neg_hi:[0,0,1]\n"
"v_pk_fma_f32 v[58:59], v[58:59], v[132:133], v[128:129] op_sel_hi:[1,0,0] neg_lo:[0,0,1] neg_hi:[0,0,1]\n"
"v_cvt_pk_bf16_f32 v136, v56, v57\n"
"v_cvt_pk_bf16_f32 v137, v58, v59\n"
"s_add_u32 s24, s48, s40\n"
"v_permlane16_swap_b32 v136, v138\n"
"v_permlane16_swap_b32 v137, v139\n"
"buffer_store_dwordx4 v[136:139], v19, s[8:11], s24 offen sc1\n"
"v_pk_fma_f32 v[60:61], v[60:61], v[132:133], v[128:129] op_sel_hi:[1,0,0] neg_lo:[0,0,1] neg_hi:[0,0,1]\n"
"v_pk_fma_f32 v[62:63], v[62:63], v[132:133], v[128:129] op_sel_hi:[1,0,0] neg_lo:[0,0,1] neg_hi:[0,0,1]\n"
"v_cvt_pk_bf16_f32 v140, v60, v61\n"
"v_cvt_pk_bf16_f32 v141, v62, v63\n"
"s_add_u32 s24, s48, s41\n"
"v_permlane16_swap_b32 v140, v142\n"
"v_permlane16_swap_b32 v141, v143\n"
"buffer_store_dwordx4 v[140:143], v19, s[8:11], s24 offen sc1\n"
"v_pk_fma_f32 v[64:65], v[64:65], v[132:133], v[128:129] op_sel_hi:[1,0,0] neg_lo:[0,0,1] neg_hi:[0,0,1]\n"
"v_pk_fma_f32 v[66:67], v[66:67], v[132:133], v[128:129] op_sel_hi:[1,0,0] neg_lo:[0,0,1] neg_hi:[0,0,1]\n"
"v_cvt_pk_bf16_f32 v144, v64, v65\n"
"v_cvt_pk_bf16_f32 v145, v66, v67\n"
"s_add_u32 s24, s48, s42\n"
"v_permlane16_swap_b32 v144, v146\n"
"v_permlane16_swap_b32 v145, v147\n"
"buffer_store_dwordx4 v[144:147], v19, s[8:11], s24 offen sc1\n"
"v_pk_fma_f32 v[68:69], v[68:69], v[132:133], v[128:129] op_sel_hi:[1,0,0] neg_lo:[0,0,1] neg_hi:[0,0,1]\n"
"v_pk_fma_f32 v[70:71], v[70:71], v[132:133], v[128:129] op_sel_hi:[1,0,0] neg_lo:[0,0,1] neg_hi:[0,0,1]\n"
"v_cvt_pk_bf16_f32 v148, v68, v69\n"
"v_cvt_pk_bf16_f32 v149, v70, v71\n"
"s_add_u32 s24, s48, s43\n"
"v_permlane16_swap_b32 v148, v150\n"
"v_permlane16_swap_b32 v149, v151\n"
"buffer_store_dwordx4 v[148:151], v19, s[8:11], s24 offen sc1\n"
"s_waitcnt vmcnt(0)\n"
"s_barrier\n"
"s_cmp_eq_u32 s15, 0\n"
"s_cbranch_scc0 MK_pf10\n"
"v_mov_b32 v2, s28\n"
"s_mov_b64 exec, 1\n"
"buffer_store_dword v2, off, s[8:11], s31 sc1\n"
"s_mov_b64 exec, -1\n"
"MK_pf10:\n"
"s_add_u32 s28, s28, 1\n"
"s_cmp_lt_u32 s28, 64\n"
"s_cbranch_scc1 MK_Rv1\n"
"s_endpgm\n"
"MK_Rv2:\n"
"s_and_b32 s45, s28, 15\n"
"s_lshl_b32 s45, s45, 22\n"
"s_add_u32 s45, s45, 33554432\n"
"s_add_u32 s45, s45, s17\n"
"s_add_u32 s45, s45, s68\n"
"s_add_u32 s46, s45, 3424256\n"
"s_lshl_b32 s24, s28, 10\n"
"s_and_b32 s24, s24, 8192\n"
"s_xor_b32 s24, s24, s17\n"
"s_lshl_b32 s48, s28, 18\n"
"s_add_u32 s48, s48, s24\n"
"s_add_u32 s48, s48, 8388608\n"
"s_add_u32 s25, s45, 3014656\n"
"s_add_u32 s62, s25, s40\n"
"s_add_u32 s63, s25, s41\n"
"s_add_u32 s64, s25, s42\n"
"s_add_u32 s65, s25, s43\n"
"s_add_u32 s66, s25, s44\n"
"s_mov_b32 s29, s30\n"
"MK_Rv2q:\n"
"buffer_load_dword v1, v16, s[8:11], 0 offen sc1\n"
"s_add_u32 s29, s29, 1\n"
"s_waitcnt vmcnt(0)\n"
"v_cmp_gt_i32 vcc, s28, v1\n"
"s_and_b64 vcc, vcc, s[60:61]\n"
"s_cbranch_vccz MK_Rv2q_ok\n"
"s_cmp_lt_u32 s29, 131072\n"
"s_cbranch_scc1 MK_Rv2q\n"
"s_mov_b32 s30, 131072\n"
"MK_Rv2q_ok:\n"
"buffer_load_dwordx4 v[116:119], v9, s[32:35], s46 offen sc1\n"
"buffer_load_dwordx4 v[120:123], v10, s[32:35], s46 offen sc1\n"
"buffer_load_dwordx4 v[124:127], v11, s[32:35], s46 offen sc1\n"
"v_cmp_gt_i32 vcc, s28, v1\n"
"s_and_b64 vcc, vcc, s[58:59]\n"
"s_cbranch_vccz MK_Rv2p_ok\n"
"s_mov_b32 s29, s30\n"
"MK_Rv2p:\n"
"buffer_load_dword v1, v16, s[8:11], 0 offen sc1\n"
"s_add_u32 s29, s29, 1\n"
"s_waitcnt vmcnt(0)\n"
"v_cmp_gt_i32 vcc, s28, v1\n"
"s_and_b64 vcc, vcc, s[58:59]\n"
"s_cbranch_vccz MK_Rv2p_ok\n"
"s_cmp_lt_u32 s29, 131072\n"
"s_cbranch_scc1 MK_Rv2p\n"
"s_mov_b32 s30, 131072\n"
"MK_Rv2p_ok:\n"
"buffer_load_dwordx4 v[76:79], v4, s[8:11], s62 offen offset:0 sc1\n"
"buffer_load_dwordx4 v[80:83], v4, s[8:11], s62 offen offset:1024 sc1\n"
"buffer_load_dwordx4 v[84:87], v4, s[8:11], s63 offen offset:0 sc1\n"
"buffer_load_dwordx4 v[88:91], v4, s[8:11], s63 offen offset:1024 sc1\n"
"buffer_load_dwordx4 v[92:95], v4, s[8:11], s64 offen offset:0 sc1\n"
"buffer_load_dwordx4 v[96:99], v4, s[8:11], s64 offen offset:1024 sc1\n"
"buffer_load_dwordx4 v[100:103], v4, s[8:11], s65 offen offset:0 sc1\n"
"buffer_load_dwordx4 v[104:107], v4, s[8:11], s65 offen offset:1024 sc1\n"
"buffer_load_dwordx4 v[108:111], v4, s[8:11], s66 offen offset:0 sc1\n"
"buffer_load_dwordx4 v[112:115], v4, s[8:11], s66 offen offset:1024 sc1\n"
"s_waitcnt vmcnt(12)\n"
"v_mfma_f32_16x16x32_bf16 v[56:59], v[28:31], v[116:119], v[36:39]\n"
"v_mfma_f32_16x16x32_bf16 v[60:63], v[32:35], v[116:119], v[40:43]\n"
"s_waitcnt vmcnt(11)\n"
"v_mfma_f32_16x16x32_bf16 v[64:67], v[28:31], v[120:123], v[44:47]\n"
"v_mfma_f32_16x16x32_bf16 v[68:71], v[32:35], v[120:123], v[48:51]\n"
"s_waitcnt vmcnt(10)\n"
"v_mfma_f32_16x16x32_bf16 v[72:75], v[28:31], v[124:127], v[52:55]\n"
"s_waitcnt vmcnt(9)\n"
"v_mfma_f32_16x16x32_bf16 v[56:59], v[20:23], v[76:79], v[56:59]\n"
"s_waitcnt vmcnt(8)\n"
"v_mfma_f32_16x16x32_bf16 v[56:59], v[24:27], v[80:83], v[56:59]\n"
"s_waitcnt vmcnt(7)\n"
"v_mfma_f32_16x16x32_bf16 v[60:63], v[20:23], v[84:87], v[60:63]\n"
"s_waitcnt vmcnt(6)\n"
"v_mfma_f32_16x16x32_bf16 v[60:63], v[24:27], v[88:91], v[60:63]\n"
"s_nop 3\n"
"v_add_f32 v128, v56, v57\n"
"v_mul_f32 v130, v56, v56\n"
"v_fmac_f32 v130, v57, v57\n"
"v_add_f32 v128, v128, v58\n"
"v_fmac_f32 v130, v58, v58\n"
"v_add_f32 v128, v128, v59\n"
"v_fmac_f32 v130, v59, v59\n"
"s_waitcnt vmcnt(5)\n"
"v_mfma_f32_16x16x32_bf16 v[64:67], v[20:23], v[92:95], v[64:67]\n"
"s_waitcnt vmcnt(4)\n"
"v_mfma_f32_16x16x32_bf16 v[64:67], v[24:27], v[96:99], v[64:67]\n"
"v_add_f32 v128, v128, v60\n"
"v_fmac_f32 v130, v60, v60\n"
"v_add_f32 v128, v128, v61\n"
"v_fmac_f32 v130, v61, v61\n"
"v_add_f32 v128, v128, v62\n"
"v_fmac_f32 v130, v62, v62\n"
"v_add_f32 v128, v128, v63\n"
"v_fmac_f32 v130, v63, v63\n"
"s_waitcnt vmcnt(3)\n"
"v_mfma_f32_16x16x32_bf16 v[68:71], v[20:23], v[100:103], v[68:71]\n"
"s_waitcnt vmcnt(2)\n"
"v_mfma_f32_16x16x32_bf16 v[68:71], v[24:27], v[104:107], v[68:71]\n"
"v_add_f32 v128, v128, v64\n"
"v_fmac_f32 v130, v64, v64\n"
"v_add_f32 v128, v128, v65\n"
"v_fmac_f32 v130, v65, v65\n"
"v_add_f32 v128, v128, v66\n"
"v_fmac_f32 v130, v66, v66\n"
"v_add_f32 v128, v128, v67\n"
"v_fmac_f32 v130, v67, v67\n"
"s_waitcnt vmcnt(1)\n"
"v_mfma_f32_16x16x32_bf16 v[72:75], v[20:23], v[108:111], v[72:75]\n"
"s_waitcnt vmcnt(0)\n"
"v_mfma_f32_16x16x32_bf16 v[72:75], v[24:27], v[112:115], v[72:75]\n"
"v_add_f32 v128, v128, v68\n"
"v_fmac_f32 v130, v68, v68\n"
"v_add_f32 v128, v128, v69\n"
"v_fmac_f32 v130, v69, v69\n"
"v_add_f32 v128, v128, v70\n"
"v_fmac_f32 v130, v70, v70\n"
"v_add_f32 v128, v128, v71\n"
"v_fmac_f32 v130, v71, v71\n"
"v_add_f32 v128, v128, v72\n"
"v_fmac_f32 v130, v72, v72\n"
"v_add_f32 v128, v128, v73\n"
"v_fmac_f32 v130, v73, v73\n"
"v_add_f32 v128, v128, v74\n"
"v_fmac_f32 v130, v74, v74\n"
"v_add_f32 v128, v128, v75\n"
"v_fmac_f32 v130, v75, v75\n"
"s_nop 1\n"
"v_permlane16_swap_b32 v128, v130\n"
"v_add_f32 v128, v128, v130\n"
"s_nop 1\n"
"v_add_f32_dpp v128, v128, v128 row_ror:8 row_mask:0xf bank_mask:0xf\n"
"v_mov_b32 v129, v128\n"
"s_nop 1\n"
"v_permlane32_swap_b32 v128, v129\n"
"v_add_f32 v128, v128, v129\n"
"ds_write_b32 v17, v128\n"
"v_cvt_pk_bf16_f32 v138, v56, v57\n"
"v_cvt_pk_bf16_f32 v139, v58, v59\n"
"v_cvt_pk_bf16_f32 v142, v60, v61\n"
"v_cvt_pk_bf16_f32 v143, v62, v63\n"
"v_cvt_pk_bf16_f32 v146, v64, v65\n"
"v_cvt_pk_bf16_f32 v147, v66, v67\n"
"v_cvt_pk_bf16_f32 v150, v68, v69\n"
"v_cvt_pk_bf16_f32 v151, v70, v71\n"
"v_cvt_pk_bf16_f32 v154, v72, v73\n"
"v_cvt_pk_bf16_f32 v155, v74, v75\n"
"s_waitcnt lgkmcnt(0)\n"
"s_barrier\n"
"ds_read_b128 v[128:131], v18\n"
"ds_read_b128 v[132:135], v18 offset:16\n"
"v_xor_b32 v17, 256, v17\n"
"v_xor_b32 v18, 256, v18\n"
"s_waitcnt lgkmcnt(1)\n"
"v_pk_add_f32 v[128:129], v[128:129], v[130:131]\n"
"s_waitcnt lgkmcnt(0)\n"
"v_pk_add_f32 v[132:133], v[132:133], v[134:135]\n"
"v_add_f32 v128, v128, v129\n"
"v_add_f32 v132, v132, v133\n"
"v_mul_f32 v128, s56, v128\n"
"v_mul_f32 v132, s56, v132\n"
"v_fma_f32 v132, -v128, v128, v132\n"
"v_max_f32 v132, 0, v132\n"
"v_add_f32 v132, s57, v132\n"
"v_rsq_f32 v132, v132\n"
"s_nop 0\n"
"v_mul_f32 v128, v128, v132\n"
"v_pk_fma_f32 v[56:57], v[56:57], v[132:133], v[128:129] op_sel_hi:[1,0,0] neg_lo:[0,0,1] neg_hi:[0,0,1]\n"
"v_pk_fma_f32 v[58:59], v[58:59], v[132:133], v[128:129] op_sel_hi:[1,0,0] neg_lo:[0,0,1] neg_hi:[0,0,1]\n"
"v_cvt_pk_bf16_f32 v136, v56, v57\n"
"v_cvt_pk_bf16_f32 v137, v58, v59\n"
"s_add_u32 s24, s48, s40\n"
"v_permlane16_swap_b32 v136, v138\n"
"v_permlane16_swap_b32 v137, v139\n"
"buffer_store_dwordx4 v[136:139], v19, s[8:11], s24 offen sc1\n"
"v_pk_fma_f32 v[60:61], v[60:61], v[132:133], v[128:129] op_sel_hi:[1,0,0] neg_lo:[0,0,1] neg_hi:[0,0,1]\n"
"v_pk_fma_f32 v[62:63], v[62:63], v[132:133], v[128:129] op_sel_hi:[1,0,0] neg_lo:[0,0,1] neg_hi:[0,0,1]\n"
"v_cvt_pk_bf16_f32 v140, v60, v61\n"
"v_cvt_pk_bf16_f32 v141, v62, v63\n"
"s_add_u32 s24, s48, s41\n"
"v_permlane16_swap_b32 v140, v142\n"
"v_permlane16_swap_b32 v141, v143\n"
"buffer_store_dwordx4 v[140:143], v19, s[8:11], s24 offen sc1\n"
"v_pk_fma_f32 v[64:65], v[64:65], v[132:133], v[128:129] op_sel_hi:[1,0,0] neg_lo:[0,0,1] neg_hi:[0,0,1]\n"
"v_pk_fma_f32 v[66:67], v[66:67], v[132:133], v[128:129] op_sel_hi:[1,0,0] neg_lo:[0,0,1] neg_hi:[0,0,1]\n"
"v_cvt_pk_bf16_f32 v144, v64, v65\n"
"v_cvt_pk_bf16_f32 v145, v66, v67\n"
"s_add_u32 s24, s48, s42\n"
"v_permlane16_swap_b32 v144, v146\n"
"v_permlane16_swap_b32 v145, v147\n"
"buffer_store_dwordx4 v[144:147], v19, s[8:11], s24 offen sc1\n"
"v_pk_fma_f32 v[68:69], v[68:69], v[132:133], v[128:129] op_sel_hi:[1,0,0] neg_lo:[0,0,1] neg_hi:[0,0,1]\n"
"v_pk_fma_f32 v[70:71], v[70:71], v[132:133], v[128:129] op_sel_hi:[1,0,0] neg_lo:[0,0,1] neg_hi:[0,0,1]\n"
"v_cvt_pk_bf16_f32 v148, v68, v69\n"
"v_cvt_pk_bf16_f32 v149, v70, v71\n"
"s_add_u32 s24, s48, s43\n"
"v_permlane16_swap_b32 v148, v150\n"
"v_permlane16_swap_b32 v149, v151\n"
"buffer_store_dwordx4 v[148:151], v19, s[8:11], s24 offen sc1\n"
"v_pk_fma_f32 v[72:73], v[72:73], v[132:133], v[128:129] op_sel_hi:[1,0,0] neg_lo:[0,0,1] neg_hi:[0,0,1]\n"
"v_pk_fma_f32 v[74:75], v[74:75], v[132:133], v[128:129] op_sel_hi:[1,0,0] neg_lo:[0,0,1] neg_hi:[0,0,1]\n"
"v_cvt_pk_bf16_f32 v152, v72, v73\n"
"v_cvt_pk_bf16_f32 v153, v74, v75\n"
"s_add_u32 s24, s48, s44\n"
"v_permlane16_swap_b32 v152, v154\n"
"v_permlane16_swap_b32 v153, v155\n"
"buffer_store_dwordx4 v[152:155], v19, s[8:11], s24 offen sc1\n"
"s_waitcnt vmcnt(0)\n"
"s_barrier\n"
"s_cmp_eq_u32 s28, 63\n"
"s_cselect_b32 s24, 1000, s28\n"
"s_cmp_eq_u32 s15, 0\n"
"s_cbranch_scc0 MK_pf11\n"
"v_mov_b32 v1, s24\n"
"s_mov_b64 exec, 1\n"
"buffer_store_dword v1, off, s[8:11], s50 sc1\n"
"s_mov_b64 exec, -1\n"
"MK_pf11:\n"
"s_cmp_eq_u32 s15, 1\n"
"s_cbranch_scc0 MK_pf12\n"
"v_mov_b32 v2, s28\n"
"s_mov_b64 exec, 1\n"
"buffer_store_dword v2, off, s[8:11], s31 sc1\n"
"s_mov_b64 exec, -1\n"
"MK_pf12:\n"
"s_add_u32 s28, s28, 1\n"
"s_cmp_lt_u32 s28, 64\n"
"s_cbranch_scc1 MK_Rv2\n"
"s_endpgm\n"
"MK_Rv3:\n"
"s_and_b32 s45, s28, 15\n"
"s_lshl_b32 s45, s45, 22\n"
"s_add_u32 s45, s45, 33554432\n"
"s_add_u32 s45, s45, s17\n"
"s_add_u32 s45, s45, s68\n"
"s_add_u32 s46, s45, 3424256\n"
"s_lshl_b32 s24, s28, 10\n"
"s_and_b32 s24, s24, 8192\n"
"s_xor_b32 s24, s24, s17\n"
"s_lshl_b32 s48, s28, 18\n"
"s_add_u32 s48, s48, s24\n"
"s_add_u32 s48, s48, 8388608\n"
"s_add_u32 s25, s45, 3014656\n"
"s_add_u32 s62, s25, s40\n"
"s_add_u32 s63, s25, s41\n"
"s_add_u32 s64, s25, s42\n"
"s_add_u32 s65, s25, s43\n"
"s_mov_b32 s29, s30\n"
"MK_Rv3q:\n"
"buffer_load_dword v1, v16, s[8:11], 0 offen sc1\n"
"s_add_u32 s29, s29, 1\n"
"s_waitcnt vmcnt(0)\n"
"v_cmp_gt_i32 vcc, s28, v1\n"
"s_and_b64 vcc, vcc, s[60:61]\n"
"s_cbranch_vccz MK_Rv3q_ok\n"
"s_cmp_lt_u32 s29, 131072\n"
"s_cbranch_scc1 MK_Rv3q\n"
"s_mov_b32 s30, 131072\n"
"MK_Rv3q_ok:\n"
"buffer_load_dwordx4 v[116:119], v9, s[32:35], s46 offen sc1\n"
"buffer_load_dwordx4 v[120:123], v10, s[32:35], s46 offen sc1\n"
"v_cmp_gt_i32 vcc, s28, v1\n"
"s_and_b64 vcc, vcc, s[58:59]\n"
"s_cbranch_vccz MK_Rv3p_ok\n"
"s_mov_b32 s29, s30\n"
"MK_Rv3p:\n"
"buffer_load_dword v1, v16, s[8:11], 0 offen sc1\n"
"s_add_u32 s29, s29, 1\n"
"s_waitcnt vmcnt(0)\n"
"v_cmp_gt_i32 vcc, s28, v1\n"
"s_and_b64 vcc, vcc, s[58:59]\n"
"s_cbranch_vccz MK_Rv3p_ok\n"
"s_cmp_lt_u32 s29, 131072\n"
"s_cbranch_scc1 MK_Rv3p\n"
"s_mov_b32 s30, 131072\n"
"MK_Rv3p_ok:\n"
"buffer_load_dwordx4 v[76:79], v4, s[8:11], s62 offen offset:0 sc1\n"
"buffer_load_dwordx4 v[80:83], v4, s[8:11], s62 offen offset:1024 sc1\n"
"buffer_load_dwordx4 v[84:87], v4, s[8:11], s63 offen offset:0 sc1\n"
"buffer_load_dwordx4 v[88:91], v4, s[8:11], s63 offen offset:1024 sc1\n"
"buffer_load_dwordx4 v[92:95], v4, s[8:11], s64 offen offset:0 sc1\n"
"buffer_load_dwordx4 v[96:99], v4, s[8:11], s64 offen offset:1024 sc1\n"
"buffer_load_dwordx4 v[100:103], v4, s[8:11], s65 offen offset:0 sc1\n"
"buffer_load_dwordx4 v[104:107], v4, s[8:11], s65 offen offset:1024 sc1\n"
"s_waitcnt vmcnt(9)\n"
"v_mfma_f32_16x16x32_bf16 v[56:59], v[28:31], v[116:119], v[36:39]\n"
"v_mfma_f32_16x16x32_bf16 v[60:63], v[32:35], v[116:119], v[40:43]\n"
"s_waitcnt vmcnt(8)\n"
"v_mfma_f32_16x16x32_bf16 v[64:67], v[28:31], v[120:123], v[44:47]\n"
"v_mfma_f32_16x16x32_bf16 v[68:71], v[32:35], v[120:123], v[48:51]\n"
"s_waitcnt vmcnt(7)\n"
"v_mfma_f32_16x16x32_bf16 v[56:59], v[20:23], v[76:79], v[56:59]\n"
"s_waitcnt vmcnt(6)\n"
"v_mfma_f32_16x16x32_bf16 v[56:59], v[24:27], v[80:83], v[56:59]\n"
"s_waitcnt vmcnt(5)\n"
"v_mfma_f32_16x16x32_bf16 v[60:63], v[20:23], v[84:87], v[60:63]\n"
"s_waitcnt vmcnt(4)\n"
"v_mfma_f32_16x16x32_bf16 v[60:63], v[24:27], v[88:91], v[60:63]\n"
"s_nop 3\n"
"v_add_f32 v128, v56, v57\n"
"v_mul_f32 v130, v56, v56\n"
"v_fmac_f32 v130, v57, v57\n"
"v_add_f32 v128, v128, v58\n"
"v_fmac_f32 v130, v58, v58\n"
"v_add_f32 v128, v128, v59\n"
"v_fmac_f32 v130, v59, v59\n"
"s_waitcnt vmcnt(3)\n"
"v_mfma_f32_16x16x32_bf16 v[64:67], v[20:23], v[92:95], v[64:67]\n"
"s_waitcnt vmcnt(2)\n"
"v_mfma_f32_16x16x32_bf16 v[64:67], v[24:27], v[96:99], v[64:67]\n"
"v_add_f32 v128, v128, v60\n"
"v_fmac_f32 v130, v60, v60\n"
"v_add_f32 v128, v128, v61\n"
"v_fmac_f32 v130, v61, v61\n"
"v_add_f32 v128, v128, v62\n"
"v_fmac_f32 v130, v62, v62\n"
"v_add_f32 v128, v128, v63\n"
"v_fmac_f32 v130, v63, v63\n"
"s_waitcnt vmcnt(1)\n"
"v_mfma_f32_16x16x32_bf16 v[68:71], v[20:23], v[100:103], v[68:71]\n"
"s_waitcnt vmcnt(0)\n"
"v_mfma_f32_16x16x32_bf16 v[68:71], v[24:27], v[104:107], v[68:71]\n"
"v_add_f32 v128, v128, v64\n"
"v_fmac_f32 v130, v64, v64\n"
"v_add_f32 v128, v128, v65\n"
"v_fmac_f32 v130, v65, v65\n"
"v_add_f32 v128, v128, v66\n"
"v_fmac_f32 v130, v66, v66\n"
"v_add_f32 v128, v128, v67\n"
"v_fmac_f32 v130, v67, v67\n"
"v_add_f32 v128, v128, v68\n"
"v_fmac_f32 v130, v68, v68\n"
"v_add_f32 v128, v128, v69\n"
"v_fmac_f32 v130, v69, v69\n"
"v_add_f32 v128, v128, v70\n"
"v_fmac_f32 v130, v70, v70\n"
"v_add_f32 v128, v128, v71\n"
"v_fmac_f32 v130, v71, v71\n"
"s_nop 1\n"
"v_permlane16_swap_b32 v128, v130\n"
"v_add_f32 v128, v128, v130\n"
"s_nop 1\n"
"v_add_f32_dpp v128, v128, v128 row_ror:8 row_mask:0xf bank_mask:0xf\n"
"v_mov_b32 v129, v128\n"
"s_nop 1\n"
"v_permlane32_swap_b32 v128, v129\n"
"v_add_f32 v128, v128, v129\n"
"ds_write_b32 v17, v128\n"
"v_cvt_pk_bf16_f32 v138, v56, v57\n"
"v_cvt_pk_bf16_f32 v139, v58, v59\n"
"v_cvt_pk_bf16_f32 v142, v60, v61\n"
"v_cvt_pk_bf16_f32 v143, v62, v63\n"
"v_cvt_pk_bf16_f32 v146, v64, v65\n"
"v_cvt_pk_bf16_f32 v147, v66, v67\n"
"v_cvt_pk_bf16_f32 v150, v68, v69\n"
"v_cvt_pk_bf16_f32 v151, v70, v71\n"
"s_waitcnt lgkmcnt(0)\n"
"s_barrier\n"
"ds_read_b128 v[128:131], v18\n"
"ds_read_b128 v[132:135], v18 offset:16\n"
"v_xor_b32 v17, 256, v17\n"
"v_xor_b32 v18, 256, v18\n"
"s_waitcnt lgkmcnt(1)\n"
"v_pk_add_f32 v[128:129], v[128:129], v[130:131]\n"
"s_waitcnt lgkmcnt(0)\n"
"v_pk_add_f32 v[132:133], v[132:133], v[134:135]\n"
"v_add_f32 v128, v128, v129\n"
"v_add_f32 v132, v132, v133\n"
"v_mul_f32 v128, s56, v128\n"
"v_mul_f32 v132, s56, v132\n"
"v_fma_f32 v132, -v128, v128, v132\n"
"v_max_f32 v132, 0, v132\n"
"v_add_f32 v132, s57, v132\n"
"v_rsq_f32 v132, v132\n"
"s_nop 0\n"
"v_mul_f32 v128, v128, v132\n"
"v_pk_fma_f32 v[56:57], v[56:57], v[132:133], v[128:129] op_sel_hi:[1,0,0] neg_lo:[0,0,1] neg_hi:[0,0,1]\n"
"v_pk_fma_f32 v[58:59], v[58:59], v[132:133], v[128:129] op_sel_hi:[1,0,0] neg_lo:[0,0,1] neg_hi:[0,0,1]\n"
"v_cvt_pk_bf16_f32 v136, v56, v57\n"
"v_cvt_pk_bf16_f32 v137, v58, v59\n"
"s_add_u32 s24, s48, s40\n"
"v_permlane16_swap_b32 v136, v138\n"
"v_permlane16_swap_b32 v137, v139\n"
"buffer_store_dwordx4 v[136:139], v19, s[8:11], s24 offen sc1\n"
"v_pk_fma_f32 v[60:61], v[60:61], v[132:133], v[128:129] op_sel_hi:[1,0,0] neg_lo:[0,0,1] neg_hi:[0,0,1]\n"
"v_pk_fma_f32 v[62:63], v[62:63], v[132:133], v[128:129] op_sel_hi:[1,0,0] neg_lo:[0,0,1] neg_hi:[0,0,1]\n"
"v_cvt_pk_bf16_f32 v140, v60, v61\n"
"v_cvt_pk_bf16_f32 v141, v62, v63\n"
"s_add_u32 s24, s48, s41\n"
"v_permlane16_swap_b32 v140, v142\n"
"v_permlane16_swap_b32 v141, v143\n"
"buffer_store_dwordx4 v[140:143], v19, s[8:11], s24 offen sc1\n"
"v_pk_fma_f32 v[64:65], v[64:65], v[132:133], v[128:129] op_sel_hi:[1,0,0] neg_lo:[0,0,1] neg_hi:[0,0,1]\n"
"v_pk_fma_f32 v[66:67], v[66:67], v[132:133], v[128:129] op_sel_hi:[1,0,0] neg_lo:[0,0,1] neg_hi:[0,0,1]\n"
"v_cvt_pk_bf16_f32 v144, v64, v65\n"
"v_cvt_pk_bf16_f32 v145, v66, v67\n"
"s_add_u32 s24, s48, s42\n"
"v_permlane16_swap_b32 v144, v146\n"
"v_permlane16_swap_b32 v145, v147\n"
"buffer_store_dwordx4 v[144:147], v19, s[8:11], s24 offen sc1\n"
"v_pk_fma_f32 v[68:69], v[68:69], v[132:133], v[128:129] op_sel_hi:[1,0,0] neg_lo:[0,0,1] neg_hi:[0,0,1]\n"
"v_pk_fma_f32 v[70:71], v[70:71], v[132:133], v[128:129] op_sel_hi:[1,0,0] neg_lo:[0,0,1] neg_hi:[0,0,1]\n"
"v_cvt_pk_bf16_f32 v148, v68, v69\n"
"v_cvt_pk_bf16_f32 v149, v70, v71\n"
"s_add_u32 s24, s48, s43\n"
"v_permlane16_swap_b32 v148, v150\n"
"v_permlane16_swap_b32 v149, v151\n"
"buffer_store_dwordx4 v[148:151], v19, s[8:11], s24 offen sc1\n"
"s_waitcnt vmcnt(0)\n"
"s_barrier\n"
"s_cmp_eq_u32 s28, 63\n"
"s_cselect_b32 s24, 1000, s28\n"
"s_cmp_eq_u32 s15, 0\n"
"s_cbranch_scc0 MK_pf13\n"
"v_mov_b32 v1, s24\n"
"s_mov_b64 exec, 1\n"
"buffer_store_dword v1, off, s[8:11], s50 sc1\n"
"s_mov_b64 exec, -1\n"
"MK_pf13:\n"
"s_cmp_eq_u32 s15, 1\n"
"s_cbranch_scc0 MK_pf14\n"
"v_mov_b32 v2, s28\n"
"s_mov_b64 exec, 1\n"
"buffer_store_dword v2, off, s[8:11], s31 sc1\n"
"s_mov_b64 exec, -1\n"
"MK_pf14:\n"
"s_add_u32 s28, s28, 1\n"
"s_cmp_lt_u32 s28, 64\n"
"s_cbranch_scc1 MK_Rv3\n"
"s_endpgm\n"
"MK_Rv4:\n"
"s_and_b32 s45, s28, 15\n"
"s_lshl_b32 s45, s45, 22\n"
"s_add_u32 s45, s45, 33554432\n"
"s_add_u32 s45, s45, s17\n"
"s_add_u32 s45, s45, s68\n"
"s_add_u32 s46, s45, 3424256\n"
"s_mul_i32 s48, s28, 36864\n"
"s_add_u32 s25, s45, 3014656\n"
"s_add_u32 s62, s25, s40\n"
"s_add_u32 s63, s25, s41\n"
"s_add_u32 s64, s25, s42\n"
"s_add_u32 s65, s25, s43\n"
"s_add_u32 s66, s25, s44\n"
"s_mov_b32 s29, s30\n"
"MK_Rv4q:\n"
"buffer_load_dword v1, v16, s[8:11], 0 offen sc1\n"
"s_add_u32 s29, s29, 1\n"
"s_waitcnt vmcnt(0)\n"
"v_cmp_gt_i32 vcc, s28, v1\n"
"s_and_b64 vcc, vcc, s[60:61]\n"
"s_cbranch_vccz MK_Rv4q_ok\n"
"s_cmp_lt_u32 s29, 131072\n"
"s_cbranch_scc1 MK_Rv4q\n"
"s_mov_b32 s30, 131072\n"
"MK_Rv4q_ok:\n"
"buffer_load_dwordx4 v[116:119], v9, s[32:35], s46 offen sc1\n"
"buffer_load_dwordx4 v[120:123], v10, s[32:35], s46 offen sc1\n"
"buffer_load_dwordx4 v[124:127], v11, s[32:35], s46 offen sc1\n"
"v_cmp_gt_i32 vcc, s28, v1\n"
"s_and_b64 vcc, vcc, s[58:59]\n"
"s_cbranch_vccz MK_Rv4p_ok\n"
"s_mov_b32 s29, s30\n"
"MK_Rv4p:\n"
"buffer_load_dword v1, v16, s[8:11], 0 offen sc1\n"
"s_add_u32 s29, s29, 1\n"
"s_waitcnt vmcnt(0)\n"
"v_cmp_gt_i32 vcc, s28, v1\n"
"s_and_b64 vcc, vcc, s[58:59]\n"
"s_cbranch_vccz MK_Rv4p_ok\n"
"s_cmp_lt_u32 s29, 131072\n"
"s_cbranch_scc1 MK_Rv4p\n"
"s_mov_b32 s30, 131072\n"
"MK_Rv4p_ok:\n"
"buffer_load_dwordx4 v[76:79], v4, s[8:11], s62 offen offset:0 sc1\n"
"buffer_load_dwordx4 v[80:83], v4, s[8:11], s62 offen offset:1024 sc1\n"
"buffer_load_dwordx4 v[84:87], v4, s[8:11], s63 offen offset:0 sc1\n"
"buffer_load_dwordx4 v[88:91], v4, s[8:11], s63 offen offset:1024 sc1\n"
"buffer_load_dwordx4 v[92:95], v4, s[8:11], s64 offen offset:0 sc1\n"
"buffer_load_dwordx4 v[96:99], v4, s[8:11], s64 offen offset:1024 sc1\n"
"buffer_load_dwordx4 v[100:103], v4, s[8:11], s65 offen offset:0 sc1\n"
"buffer_load_dwordx4 v[104:107], v4, s[8:11], s65 offen offset:1024 sc1\n"
"buffer_load_dwordx4 v[108:111], v4, s[8:11], s66 offen offset:0 sc1\n"
"buffer_load_dwordx4 v[112:115], v4, s[8:11], s66 offen offset:1024 sc1\n"
"s_waitcnt vmcnt(12)\n"
"v_mfma_f32_16x16x32_bf16 v[56:59], v[28:31], v[116:119], v[36:39]\n"
"v_mfma_f32_16x16x32_bf16 v[60:63], v[32:35], v[116:119], v[40:43]\n"
"s_waitcnt vmcnt(11)\n"
"v_mfma_f32_16x16x32_bf16 v[64:67], v[28:31], v[120:123], v[44:47]\n"
"v_mfma_f32_16x16x32_bf16 v[68:71], v[32:35], v[120:123], v[48:51]\n"
"s_waitcnt vmcnt(10)\n"
"v_mfma_f32_16x16x32_bf16 v[72:75], v[28:31], v[124:127], v[52:55]\n"
"s_waitcnt vmcnt(9)\n"
"v_mfma_f32_16x16x32_bf16 v[56:59], v[20:23], v[76:79], v[56:59]\n"
"s_waitcnt vmcnt(8)\n"
"v_mfma_f32_16x16x32_bf16 v[56:59], v[24:27], v[80:83], v[56:59]\n"
"s_waitcnt vmcnt(7)\n"
"v_mfma_f32_16x16x32_bf16 v[60:63], v[20:23], v[84:87], v[60:63]\n"
"s_waitcnt vmcnt(6)\n"
"v_mfma_f32_16x16x32_bf16 v[60:63], v[24:27], v[88:91], v[60:63]\n"
"s_waitcnt vmcnt(5)\n"
"v_mfma_f32_16x16x32_bf16 v[64:67], v[20:23], v[92:95], v[64:67]\n"
"s_waitcnt vmcnt(4)\n"
"v_mfma_f32_16x16x32_bf16 v[64:67], v[24:27], v[96:99], v[64:67]\n"
"s_waitcnt vmcnt(3)\n"
"v_mfma_f32_16x16x32_bf16 v[68:71], v[20:23], v[100:103], v[68:71]\n"
"s_waitcnt vmcnt(2)\n"
"v_mfma_f32_16x16x32_bf16 v[68:71], v[24:27], v[104:107], v[68:71]\n"
"s_waitcnt vmcnt(1)\n"
"v_mfma_f32_16x16x32_bf16 v[72:75], v[20:23], v[108:111], v[72:75]\n"
"s_waitcnt vmcnt(0)\n"
"v_mfma_f32_16x16x32_bf16 v[72:75], v[24:27], v[112:115], v[72:75]\n"
"v_cvt_pk_bf16_f32 v136, v56, v57\n"
"v_cvt_pk_bf16_f32 v137, v58, v59\n"
"buffer_store_dwordx2 v[136:137], v14, s[36:39], s48 offen offset:0\n"
"v_cvt_pk_bf16_f32 v140, v60, v61\n"
"v_cvt_pk_bf16_f32 v141, v62, v63\n"
"buffer_store_dwordx2 v[140:141], v14, s[36:39], s48 offen offset:64\n"
"v_cvt_pk_bf16_f32 v144, v64, v65\n"
"v_cvt_pk_bf16_f32 v145, v66, v67\n"
"buffer_store_dwordx2 v[144:145], v14, s[36:39], s48 offen offset:128\n"
"v_cvt_pk_bf16_f32 v148, v68, v69\n"
"v_cvt_pk_bf16_f32 v149, v70, v71\n"
"buffer_store_dwordx2 v[148:149], v14, s[36:39], s48 offen offset:192\n"
"v_cvt_pk_bf16_f32 v152, v72, v73\n"
"v_cvt_pk_bf16_f32 v153, v74, v75\n"
"buffer_store_dwordx2 v[152:153], v14, s[36:39], s48 offen offset:256\n"
"s_barrier\n"
"s_cmp_eq_u32 s15, 0\n"
"s_cbranch_scc0 MK_pf15\n"
"v_mov_b32 v2, s28\n"
"s_mov_b64 exec, 1\n"
"buffer_store_dword v2, off, s[8:11], s31 sc1\n"
"s_mov_b64 exec, -1\n"
"MK_pf15:\n"
"s_add_u32 s28, s28, 1\n"
"s_cmp_lt_u32 s28, 64\n"
"s_cbranch_scc1 MK_Rv4\n"
"s_endpgm\n"
"MK_Rv5:\n"
"s_and_b32 s45, s28, 15\n"
"s_lshl_b32 s45, s45, 22\n"
"s_add_u32 s45, s45, 33554432\n"
"s_add_u32 s45, s45, s17\n"
"s_add_u32 s45, s45, s68\n"
"s_add_u32 s46, s45, 3424256\n"
"s_mul_i32 s48, s28, 36864\n"
"s_add_u32 s25, s45, 3014656\n"
"s_add_u32 s62, s25, s40\n"
"s_add_u32 s63, s25, s41\n"
"s_add_u32 s64, s25, s42\n"
"s_add_u32 s65, s25, s43\n"
"s_mov_b32 s29, s30\n"
"MK_Rv5q:\n"
"buffer_load_dword v1, v16, s[8:11], 0 offen sc1\n"
"s_add_u32 s29, s29, 1\n"
"s_waitcnt vmcnt(0)\n"
"v_cmp_gt_i32 vcc, s28, v1\n"
"s_and_b64 vcc, vcc, s[60:61]\n"
"s_cbranch_vccz MK_Rv5q_ok\n"
"s_cmp_lt_u32 s29, 131072\n"
"s_cbranch_scc1 MK_Rv5q\n"
"s_mov_b32 s30, 131072\n"
"MK_Rv5q_ok:\n"
"buffer_load_dwordx4 v[116:119], v9, s[32:35], s46 offen sc1\n"
"buffer_load_dwordx4 v[120:123], v10, s[32:35], s46 offen sc1\n"
"v_cmp_gt_i32 vcc, s28, v1\n"
"s_and_b64 vcc, vcc, s[58:59]\n"
"s_cbranch_vccz MK_Rv5p_ok\n"
"s_mov_b32 s29, s30\n"
"MK_Rv5p:\n"
"buffer_load_dword v1, v16, s[8:11], 0 offen sc1\n"
"s_add_u32 s29, s29, 1\n"
"s_waitcnt vmcnt(0)\n"
"v_cmp_gt_i32 vcc, s28, v1\n"
"s_and_b64 vcc, vcc, s[58:59]\n"
"s_cbranch_vccz MK_Rv5p_ok\n"
"s_cmp_lt_u32 s29, 131072\n"
"s_cbranch_scc1 MK_Rv5p\n"
"s_mov_b32 s30, 131072\n"
"MK_Rv5p_ok:\n"
"buffer_load_dwordx4 v[76:79], v4, s[8:11], s62 offen offset:0 sc1\n"
"buffer_load_dwordx4 v[80:83], v4, s[8:11], s62 offen offset:1024 sc1\n"
"buffer_load_dwordx4 v[84:87], v4, s[8:11], s63 offen offset:0 sc1\n"
"buffer_load_dwordx4 v[88:91], v4, s[8:11], s63 offen offset:1024 sc1\n"
"buffer_load_dwordx4 v[92:95], v4, s[8:11], s64 offen offset:0 sc1\n"
"buffer_load_dwordx4 v[96:99], v4, s[8:11], s64 offen offset:1024 sc1\n"
"buffer_load_dwordx4 v[100:103], v4, s[8:11], s65 offen offset:0 sc1\n"
"buffer_load_dwordx4 v[104:107], v4, s[8:11], s65 offen offset:1024 sc1\n"
"s_waitcnt vmcnt(9)\n"
"v_mfma_f32_16x16x32_bf16 v[56:59], v[28:31], v[116:119], v[36:39]\n"
"v_mfma_f32_16x16x32_bf16 v[60:63], v[32:35], v[116:119], v[40:43]\n"
"s_waitcnt vmcnt(8)\n"
"v_mfma_f32_16x16x32_bf16 v[64:67], v[28:31], v[120:123], v[44:47]\n"
"v_mfma_f32_16x16x32_bf16 v[68:71], v[32:35], v[120:123], v[48:51]\n"
"s_waitcnt vmcnt(7)\n"
"v_mfma_f32_16x16x32_bf16 v[56:59], v[20:23], v[76:79], v[56:59]\n"
"s_waitcnt vmcnt(6)\n"
"v_mfma_f32_16x16x32_bf16 v[56:59], v[24:27], v[80:83], v[56:59]\n"
"s_waitcnt vmcnt(5)\n"
"v_mfma_f32_16x16x32_bf16 v[60:63], v[20:23], v[84:87], v[60:63]\n"
"s_waitcnt vmcnt(4)\n"
"v_mfma_f32_16x16x32_bf16 v[60:63], v[24:27], v[88:91], v[60:63]\n"
"s_waitcnt vmcnt(3)\n"
"v_mfma_f32_16x16x32_bf16 v[64:67], v[20:23], v[92:95], v[64:67]\n"
"s_waitcnt vmcnt(2)\n"
"v_mfma_f32_16x16x32_bf16 v[64:67], v[24:27], v[96:99], v[64:67]\n"
"s_waitcnt vmcnt(1)\n"
"v_mfma_f32_16x16x32_bf16 v[68:71], v[20:23], v[100:103], v[68:71]\n"
"s_waitcnt vmcnt(0)\n"
"v_mfma_f32_16x16x32_bf16 v[68:71], v[24:27], v[104:107], v[68:71]\n"
"v_cvt_pk_bf16_f32 v136, v56, v57\n"
"v_cvt_pk_bf16_f32 v137, v58, v59\n"
"buffer_store_dwordx2 v[136:137], v14, s[36:39], s48 offen offset:0\n"
"v_cvt_pk_bf16_f32 v140, v60, v61\n"
"v_cvt_pk_bf16_f32 v141, v62, v63\n"
"buffer_store_dwordx2 v[140:141], v14, s[36:39], s48 offen offset:64\n"
"v_cvt_pk_bf16_f32 v144, v64, v65\n"
"v_cvt_pk_bf16_f32 v145, v66, v67\n"
"buffer_store_dwordx2 v[144:145], v14, s[36:39], s48 offen offset:128\n"
"v_cvt_pk_bf16_f32 v148, v68, v69\n"
"v_cvt_pk_bf16_f32 v149, v70, v71\n"
"buffer_store_dwordx2 v[148:149], v14, s[36:39], s48 offen offset:192\n"
"s_barrier\n"
"s_cmp_eq_u32 s15, 0\n"
"s_cbranch_scc0 MK_pf16\n"
"v_mov_b32 v2, s28\n"
"s_mov_b64 exec, 1\n"
"buffer_store_dword v2, off, s[8:11], s31 sc1\n"
"s_mov_b64 exec, -1\n"
"MK_pf16:\n"
"s_add_u32 s28, s28, 1\n"
"s_cmp_lt_u32 s28, 64\n"
"s_cbranch_scc1 MK_Rv5\n"
"s_endpgm\n"
  :: "s"(__builtin_amdgcn_kernarg_segment_ptr()), "s"(blockIdx.x), "s"(wave)
  : "v0", "v1", "v2", "v3", "v4", "v5", "v6", "v7", "v8", "v9", "v10", "v11", "v12", "v13", "v14", "v15", "v16", "v17", "v18", "v19", "v20", "v21", "v22", "v23", "v24", "v25", "v26", "v27", "v28", "v29", "v30", "v31", "v32", "v33", "v34", "v35", "v36", "v37", "v38", "v39", "v40", "v41", "v42", "v43", "v44", "v45", "v46", "v47", "v48", "v49", "v50", "v51", "v52", "v53", "v54", "v55", "v56", "v57", "v58", "v59", "v60", "v61", "v62", "v63", "v64", "v65", "v66", "v67", "v68", "v69", "v70", "v71", "v72", "v73", "v74", "v75", "v76", "v77", "v78", "v79", "v80", "v81", "v82", "v83", "v84", "v85", "v86", "v87", "v88", "v89", "v90", "v91", "v92", "v93", "v94", "v95", "v96", "v97", "v98", "v99", "v100", "v101", "v102", "v103", "v104", "v105", "v106", "v107", "v108", "v109", "v110", "v111", "v112", "v113", "v114", "v115", "v116", "v117", "v118", "v119", "v120", "v121", "v122", "v123", "v124", "v125", "v126", "v127", "v128", "v129", "v130", "v131", "v132", "v133", "v134", "v135", "v136", "v137", "v138", "v139", "v140", "v141", "v142", "v143", "v144", "v145", "v146", "v147", "v148", "v149", "v150", "v151", "v152", "v153", "v154", "v155", "v156", "v157", "v158", "v159", "v160", "v161", "v162", "v163", "v164", "v165", "v166", "v167", "v168", "v169", "v170", "v171", "v172", "v173", "v174", "v175", "v176", "v177", "v178", "v179", "v180", "v181", "v182", "v183", "v184", "v185", "v186", "v187", "v188", "v189", "v190", "v191", "v192", "v193", "v194", "v195", "v196", "v197", "v198", "v199", "v200", "v201", "v202", "v203", "v204", "v205", "v206", "v207", "v208", "v209", "v210", "v211", "v212", "v213", "v214", "v215", "v216", "v217", "v218", "v219", "v220", "v221", "v222", "v223", "v224", "v225", "v226", "v227", "v228", "v229", "v230", "v231", "v232", "v233", "v234", "v235", "v236", "v237", "v238", "v239", "v240", "v241", "v242", "v243", "v244", "v245", "v246", "v247", "v248", "v249", "v250", "v251", "v252", "v253", "v254", "v255", "a0", "a1", "a2", "a3", "a4", "a5", "a6", "a7", "a8", "a9", "a10", "a11", "a12", "a13", "a14", "a15", "a16", "a17", "a18", "a19", "a20", "a21", "a22", "a23", "a24", "a25", "a26", "a27", "a28", "a29", "a30", "a31", "a32", "a33", "a34", "a35", "a36", "a37", "a38", "a39", "a40", "a41", "a42", "a43", "a44", "a45", "a46", "a47", "a48", "a49", "a50", "a51", "a52", "a53", "a54", "a55", "a56", "a57", "a58", "a59", "a60", "a61", "a62", "a63", "a64", "a65", "a66", "a67", "a68", "a69", "a70", "a71", "a72", "a73", "a74", "a75", "a76", "a77", "a78", "a79", "a80", "a81", "a82", "a83", "a84", "a85", "a86", "a87", "a88", "a89", "a90", "a91", "a92", "a93", "a94", "a95", "a96", "a97", "a98", "a99", "a100", "a101", "a102", "a103", "a104", "a105", "a106", "a107", "a108", "a109", "a110", "a111", "a112", "a113", "a114", "a115", "a116", "a117", "a118", "a119", "a120", "a121", "a122", "a123", "a124", "a125", "a126", "a127", "a128", "a129", "a130", "a131", "a132", "a133", "a134", "a135", "a136", "a137", "a138", "a139", "a140", "a141", "a142", "a143", "a144", "a145", "a146", "a147", "a148", "a149", "a150", "a151", "a152", "a153", "a154", "a155", "a156", "a157", "a158", "a159", "a160", "a161", "a162", "a163", "a164", "a165", "a166", "a167", "a168", "a169", "a170", "a171", "a172", "a173", "a174", "a175", "a176", "a177", "a178", "a179", "a180", "a181", "a182", "a183", "a184", "a185", "a186", "a187", "a188", "a189", "a190", "a191", "a192", "a193", "a194", "a195", "a196", "a197", "a198", "a199", "a200", "a201", "a202", "a203", "a204", "a205", "a206", "a207", "a208", "a209", "a210", "a211", "a212", "a213", "a214", "a215", "a216", "a217", "a218", "a219", "a220", "a221", "a222", "a223", "a224", "a225", "a226", "a227", "a228", "a229", "a230", "a231", "a232", "a233", "a234", "a235", "a236", "a237", "a238", "a239", "a240", "a241", "a242", "a243", "a244", "a245", "a246", "a247", "a248", "a249", "a250", "a251", "a252", "a253", "a254", "a255", "s4", "s5", "s6", "s7", "s8", "s9", "s10", "s11", "s12", "s13", "s14", "s15", "s16", "s17", "s18", "s19", "s20", "s21", "s22", "s23", "s24", "s25", "s26", "s27", "s28", "s29", "s30", "s31", "s32", "s33", "s34", "s35", "s36", "s37", "s38", "s39", "s40", "s41", "s42", "s43", "s44", "s45", "s46", "s47", "s48", "s49", "s50", "s51", "s52", "s53", "s54", "s55", "s56", "s57", "s58", "s59", "s60", "s61", "s62", "s63", "s64", "s65", "s66", "s67", "s68", "s69", "s70", "s71", "s72", "s73", "s74", "s75", "s76", "s77", "s78", "s79", "s80", "s81", "s82", "s83", "s84", "s85", "s86", "s87", "s88", "s89", "s90", "s91", "s92", "s93", "s94", "s95", "s96", "s97", "s98", "s99", "s100", "s101", "vcc", "scc", "memory");
}

static const uint32_t g_tune[4] = {0,9622,508893722,151388928};
extern "C" void kernel_launch(void* const* d_in, const int* in_sizes, int n_in, void* d_out, int out_size, void* d_ws, size_t ws_size,
                              hipStream_t stream) {
  Params p;
  p.x0 = d_in[0]; p.mem0 = d_in[1]; p.wq = d_in[2]; p.wk = d_in[3]; p.wv = d_in[4]; p.wo = d_in[5];
  p.w1 = d_in[8]; p.b1 = d_in[9]; p.w2 = d_in[10]; p.b2 = d_in[11];
  p.out = d_out; p.ws = d_ws;
  for (int i = 0; i < 4; ++i) p.tune[i] = g_tune[i];
  (void)hipFuncSetAttribute((const void*)rmt_pipeline, hipFuncAttributeMaxDynamicSharedMemorySize, 160 * 1024);
  rmt_setup<<<253u + 1, 256, 0, stream>>>((char*)d_ws, (const uint16_t*)d_in[1]);
  rmt_pipeline<<<256, 256, 160 * 1024, stream>>>(p);
}
```
